```python
import torch

def _softmax(x, axis=-1):
    return torch.softmax(x, dim=axis)

def _gelu(x):
    return torch.nn.functional.gelu(x, approximate='tanh')

def _silu(x):
    return torch.nn.functional.silu(x)

def _sigmoid(x):
    return torch.sigmoid(x)

def _layernorm(x, axis=-1, eps=1e-05):
    m = x.mean(dim=axis, keepdim=True)
    v = x.var(dim=axis, keepdim=True, unbiased=False)
    return (x - m) / torch.sqrt(v + eps)

def _scatter_add(tgt, idx, src, axis=0):
    return tgt.clone().index_add_(axis, idx, src)

def _segment_sum(src, seg, n):
    out = torch.zeros((n,) + src.shape[1:], dtype=src.dtype)
    return out.index_add_(0, seg, src)

def _pad(x, pw):
    flat = tuple((v for pair in reversed(pw) for v in pair))
    return torch.nn.functional.pad(x, flat)
kernel_name = 'adaLN_cond__sliding_block__dense_activation_squared_relu__prompt'

def setup_inputs():

    _w = lambda a: (lambda t: t.to(torch.bfloat16) if t.is_floating_point() else t)(torch.as_tensor(a))

    def _A_setup():
        B, S, D = (1, 2048, 512)
        return {'x': _w((lambda *s: torch.randn(*s, dtype=torch.bfloat16) * 0.05)(B, S, D)), 'wqkv': _w((lambda *s: torch.randn(*s, dtype=torch.bfloat16) * 0.05)(D, 3 * D)), 'wo': _w((lambda *s: torch.randn(*s, dtype=torch.bfloat16) * 0.05)(D, D))}
    _A_in = _A_setup()

    def _B_setup():
        B, S, D, F = (1, 2048, 512, 1792)
        return {'x': _w((lambda *s: torch.randn(*s, dtype=torch.bfloat16) * 0.05)(B, S, D)), 'w1': _w((lambda *s: torch.randn(*s, dtype=torch.bfloat16) * 0.05)(D, F)), 'b1': _w((lambda *s: torch.randn(*s, dtype=torch.bfloat16) * 0.4)(F)), 'w2': _w((lambda *s: torch.randn(*s, dtype=torch.bfloat16) * 0.05)(F, D)), 'b2': _w((lambda *s: torch.randn(*s, dtype=torch.bfloat16) * 3.2)(D))}
    _B_in = _B_setup()
    return {'A_x': _A_in['x'], 'A_wqkv': _A_in['wqkv'], 'A_wo': _A_in['wo'], 'B_x': _B_in['x'], 'B_w1': _B_in['w1'], 'B_b1': _B_in['b1'], 'B_w2': _B_in['w2'], 'B_b2': _B_in['b2'], 'c': _w((lambda *s: torch.randn(*s, dtype=torch.bfloat16) * 0.1)(1, 512) * 8.0), 'w_mod': _w((lambda *s: torch.randn(*s, dtype=torch.bfloat16) * 0.1)(512, 6 * 512) * 512 ** (-0.5) * 8.0)}

def reference(A_x, A_wqkv, A_wo, B_x, B_w1, B_b1, B_w2, B_b2, c, w_mod):

    def _A_ref(x, wqkv, wo):
        B, S, D = x.shape
        H = 32
        Dh = D // H
        W = 256
        qkv = (lambda x, *d: x.permute(*d))((x @ wqkv).reshape(B, S, 3, H, Dh), 2, 0, 3, 1, 4)
        q, k, v = (qkv[0], qkv[1], qkv[2])
        scores = torch.einsum('bhqd,bhkd->bhqk', q, k) * Dh ** (-0.5)
        idx = torch.arange(S)
        mask = (idx[:, None] - idx[None, :] >= 0) & (idx[:, None] - idx[None, :] < W)
        scores = torch.where(mask[None, None], scores, -1000000000.0)
        p = _softmax(scores, axis=-1)
        o = torch.einsum('bhqk,bhkd->bhqd', p, v)
        return (lambda x, *d: x.permute(*d))(o, 0, 2, 1, 3).reshape(B, S, D) @ wo

    def _B_ref(x, w1, b1, w2, b2):
        h = torch.einsum('bsd,df->bsf', x, w1) + b1
        zero = torch.zeros_like(h)
        r = torch.where(h > zero, h, zero)
        h = r * r
        out = torch.einsum('bsf,fd->bsd', h, w2) + b2
        return out
    x = A_x
    mod = (_silu(c) @ w_mod).reshape(c.shape[0], 6, 1, -1)
    sa, ca, ga, sb, cb, gb = (mod[:, 0].clone(), mod[:, 1].clone(), mod[:, 2].clone(), mod[:, 3].clone(), mod[:, 4].clone(), mod[:, 5].clone())
    h = _layernorm(x, axis=-1) * (1 + ca) + sa
    x = x + ga * _A_ref(h, A_wqkv, A_wo)
    h = _layernorm(x, axis=-1) * (1 + cb) + sb
    x = x + gb * _B_ref(h, B_w1, B_b1, B_w2, B_b2)
    return x
```

```cpp
#include <hip/hip_runtime.h>
#include <hip/hip_bf16.h>
#include <stdint.h>
namespace nref {
typedef unsigned short u16;
__device__ __forceinline__ float b2f(u16 b){ return __uint_as_float(((unsigned)b)<<16); }
__device__ __forceinline__ u16 f2b(float f){ unsigned u=__float_as_uint(f); u += 0x7FFFu + ((u>>16)&1u); return (u16)(u>>16); }
__device__ __forceinline__ float rb(float f){ return b2f(f2b(f)); }
constexpr int S=2048, D=512, F=1792, H=32, DH=16, W=256;

__global__ void k_mod(const u16* c, const u16* wm, float* mod){
  int n = blockIdx.x*blockDim.x+threadIdx.x; if(n>=6*D) return;
  float acc=0;
  for(int k=0;k<D;k++){ float cv=b2f(c[k]); float s=rb(cv/(1.f+__expf(-cv))); acc += s*b2f(wm[(size_t)k*6*D+n]); }
  mod[n]=rb(acc);
}
__global__ void k_ln(const u16* x, const float* shift, const float* scale, u16* h){
  int r=blockIdx.x; int t=threadIdx.x;
  __shared__ float red[512];
  float v=b2f(x[r*D+t]);
  red[t]=v; __syncthreads();
  for(int s=256;s>0;s>>=1){ if(t<s) red[t]+=red[t+s]; __syncthreads(); }
  float mean=red[0]/D; __syncthreads();
  float d=v-mean; red[t]=d*d; __syncthreads();
  for(int s=256;s>0;s>>=1){ if(t<s) red[t]+=red[t+s]; __syncthreads(); }
  float var=rb(red[0]/D); float m=rb(mean);
  float xn = rb(rb(v-m)/rb(sqrtf(rb(var+1e-5f))));
  float o = rb(rb(xn*rb(1.f+scale[t]))+shift[t]);
  h[r*D+t]=f2b(o);
}
__global__ void k_gemm(const u16* A, const u16* B, u16* C, int N, int K, int mode, const u16* res, const float* g, const u16* bias){
  int n = blockIdx.x*blockDim.x+threadIdx.x; int m=blockIdx.y;
  if(n>=N) return;
  float acc=0;
  for(int k=0;k<K;k++) acc += b2f(A[(size_t)m*K+k])*b2f(B[(size_t)k*N+n]);
  acc=rb(acc);
  float o;
  if(mode==0) o=acc;
  else if(mode==1) o = b2f(res[(size_t)m*N+n]) + rb(g[n]*acc);
  else if(mode==2){ float h=rb(acc+b2f(bias[n])); h = h>0?h:0; o=h*h; }
  else { float h=rb(acc+b2f(bias[n])); o = b2f(res[(size_t)m*N+n]) + rb(g[n]*h); }
  C[(size_t)m*N+n]=f2b(o);
}
__global__ void k_attn(const u16* qkv, u16* o){
  int q=blockIdx.x; int h=blockIdx.y; int t=threadIdx.x;
  __shared__ float p[256]; __shared__ float red[256];
  int key=q-t; float s=-1e9f;
  if(key>=0){ float a=0; for(int d=0;d<DH;d++) a+= b2f(qkv[(size_t)q*3*D+h*DH+d])*b2f(qkv[(size_t)key*3*D+D+h*DH+d]); s=rb(rb(a)*0.25f); }
  red[t]=s; __syncthreads();
  for(int k=128;k>0;k>>=1){ if(t<k) red[t]=fmaxf(red[t],red[t+k]); __syncthreads(); }
  float mx=red[0]; __syncthreads();
  float e = key>=0? __expf(s-mx):0.f; red[t]=e; __syncthreads();
  for(int k=128;k>0;k>>=1){ if(t<k) red[t]+=red[t+k]; __syncthreads(); }
  p[t]=rb(e/red[0]); __syncthreads();
  if(t<DH){ float a=0; for(int j=0;j<256;j++){ int kk=q-j; if(kk>=0) a+=p[j]*b2f(qkv[(size_t)kk*3*D+2*D+h*DH+t]); } o[(size_t)q*D+h*DH+t]=f2b(a); }
}
struct Bufs { float* mod; u16 *h, *qkv, *o, *x1, *h2, *h1; };
static inline Bufs carve(void* ws){
  Bufs b; char* p=(char*)ws;
  b.mod=(float*)p; p+=16384;
  b.h=(u16*)p; p+=S*D*2; b.qkv=(u16*)p; p+=S*3*D*2; b.o=(u16*)p; p+=S*D*2; b.x1=(u16*)p; p+=S*D*2; b.h2=(u16*)p; p+=S*D*2; b.h1=(u16*)p; p+=S*F*2;
  return b;
}
static inline void run(void* const* d_in, void* d_out, void* d_ws, hipStream_t stream){
  const u16* x=(const u16*)d_in[0]; const u16* wqkv=(const u16*)d_in[1]; const u16* wo=(const u16*)d_in[2];
  const u16* w1=(const u16*)d_in[4]; const u16* b1=(const u16*)d_in[5]; const u16* w2=(const u16*)d_in[6]; const u16* b2=(const u16*)d_in[7];
  const u16* c=(const u16*)d_in[8]; const u16* wm=(const u16*)d_in[9];
  Bufs b=carve(d_ws);
  k_mod<<<12,256,0,stream>>>(c,wm,b.mod);
  k_ln<<<S,512,0,stream>>>(x,b.mod+0*D,b.mod+1*D,b.h);
  k_gemm<<<dim3(3*D/256,S),256,0,stream>>>(b.h,wqkv,b.qkv,3*D,D,0,nullptr,nullptr,nullptr);
  k_attn<<<dim3(S,H),256,0,stream>>>(b.qkv,b.o);
  k_gemm<<<dim3(D/256,S),256,0,stream>>>(b.o,wo,b.x1,D,D,1,x,b.mod+2*D,nullptr);
  k_ln<<<S,512,0,stream>>>(b.x1,b.mod+3*D,b.mod+4*D,b.h2);
  k_gemm<<<dim3(F/256,S),256,0,stream>>>(b.h2,w1,b.h1,F,D,2,nullptr,nullptr,b1);
  k_gemm<<<dim3(D/256,S),256,0,stream>>>(b.h1,w2,(u16*)d_out,D,F,3,b.x1,b.mod+5*D,b2);
}
}
extern "C" void kernel_launch(void* const* d_in, const int* in_sizes, int n_in,
                              void* d_out, int out_size, void* d_ws, size_t ws_size,
                              hipStream_t stream) {
  nref::run(d_in,d_out,d_ws,stream);
}
```

```cpp
#include <hip/hip_runtime.h>

struct Srd { const void* p; unsigned bytes; unsigned fmt; };
struct KArgs { Srd x, wqkv, wmod, ws, c, wo, w1, w2, out, b1, b2, spare; };

__global__ void __launch_bounds__(256, 1) fused_block_kernel(KArgs a) {
  __shared__ __attribute__((aligned(16))) char lds[163840];
  asm volatile(
    "s_load_dwordx16 s[4:19], s[0:1], 0\n"
    "s_load_dwordx16 s[20:35], s[0:1], 64\n"
    "v_lshrrev_b32 v2, 6, v0\n"
    "v_and_b32 v1, 63, v0\n"
    "s_and_b32 s65, s2, 7\n"
    "v_readfirstlane_b32 s52, v2\n"
    "s_lshr_b32 s53, s2, 3\n"
    "v_lshlrev_b32 v0, 4, v1\n"
    "s_getpc_b64 s[50:51]\n"
    "Lpc0:\n"
    "s_lshl2_add_u32 s71, s53, s52\n"
    "s_mul_i32 s71, s71, 320\n"
    "s_min_u32 s71, s71, Lallend-Lpc0-320\n"
    "s_add_u32 s50, s50, s71\n"
    "s_addc_u32 s51, s51, 0\n"
    "s_load_dword s36, s[50:51], 0\n"
    "s_load_dword s36, s[50:51], 64\n"
    "s_load_dword s36, s[50:51], 128\n"
    "s_load_dword s36, s[50:51], 192\n"
    "s_load_dword s36, s[50:51], 256\n"
    "s_lshr_b32 s71, s65, 1\n"
    "s_lshr_b32 s72, s53, 2\n"
    "s_lshl3_add_u32 s54, s71, s72\n"
    "s_and_b32 s73, s65, 1\n"
    "s_and_b32 s74, s53, 3\n"
    "s_lshl2_add_u32 s66, s73, s74\n"
    "s_add_u32 s94, s72, s74\n"
    "s_and_b32 s94, s94, 3\n"
    "v_and_b32 v60, 15, v1\n"
    "v_lshrrev_b32 v61, 4, v1\n"
    "v_lshrrev_b32 v4, 3, v1\n"
    "v_and_b32 v62, 7, v1\n"
    "s_lshl_b32 s42, s52, 12\n"
    "v_lshlrev_b32 v39, 4, v60\n"
    "v_lshl_add_u32 v32, v61, 10, v39\n"
    "v_add_u32 v32, s42, v32\n"
    "v_add_u32 v33, 0x4000, v32\n"
    "v_add_u32 v34, 0x8000, v32\n"
    "v_add_u32 v35, 0xc000, v32\n"
    "v_add_u32 v39, 0x24000, v39\n"
    "v_and_b32 v16, 2, v4\n"
    "v_lshlrev_b32 v17, 1, v16\n"
    "v_xor_b32 v17, v17, v62\n"
    "v_and_b32 v18, 2, v61\n"
    "v_xor_b32 v17, v17, v18\n"
    "v_lshlrev_b32 v17, 4, v17\n"
    "v_lshl_add_u32 v6, v4, 10, v17\n"
    "v_mul_u32_u24 v52, 0x1800, v4\n"
    "v_add_u32 v52, v52, v17\n"
    "s_and_b32 s75, s52, 1\n"
    "s_lshl_b32 s75, s75, 2\n"
    "v_or_b32 v16, s75, v16\n"
    "v_xor_b32 v16, v16, v62\n"
    "v_mul_u32_u24 v36, 0xc00, v4\n"
    "v_lshl_add_u32 v36, v16, 4, v36\n"
    "v_add_u32 v53, 0xbc00, v52\n"
    "v_add_u32 v54, 0xbc00, v53\n"
    "v_add_u32 v55, 0xbc00, v54\n"
    "v_lshlrev_b32 v56, 2, v1\n"
    "v_lshlrev_b32 v57, 7, v60\n"
    "s_mov_b32 s64, 20000\n"
    "s_mov_b32 s37, 0xffff0000\n"
    "s_lshl_b32 s50, s54, 16\n"
    "s_mul_i32 s51, s52, 0x6000\n"
    "s_lshl_b32 s75, s66, 7\n"
    "s_add_u32 s51, s51, s75\n"
    "s_add_u32 s75, s94, 0\n"
    "s_and_b32 s75, s75, 3\n"
    "s_lshl_b32 s74, s75, 8\n"
    "s_add_u32 s87, s74, s50\n"
    "s_mul_i32 s74, s75, 0x60000\n"
    "s_add_u32 s96, s74, s51\n"
    "s_add_u32 s75, s94, 1\n"
    "s_and_b32 s75, s75, 3\n"
    "s_lshl_b32 s74, s75, 8\n"
    "s_add_u32 s88, s74, s50\n"
    "s_mul_i32 s74, s75, 0x60000\n"
    "s_add_u32 s97, s74, s51\n"
    "s_add_u32 s75, s94, 2\n"
    "s_and_b32 s75, s75, 3\n"
    "s_lshl_b32 s74, s75, 8\n"
    "s_add_u32 s89, s74, s50\n"
    "s_mul_i32 s74, s75, 0x60000\n"
    "s_add_u32 s98, s74, s51\n"
    "s_add_u32 s75, s94, 3\n"
    "s_and_b32 s75, s75, 3\n"
    "s_lshl_b32 s74, s75, 8\n"
    "s_add_u32 s90, s74, s50\n"
    "s_mul_i32 s74, s75, 0x60000\n"
    "s_add_u32 s99, s74, s51\n"
    "s_lshl2_add_u32 s95, s2, s52\n"
    "s_and_b32 s73, s65, 1\n"
    "s_sub_u32 s93, s53, s73\n"
    "s_and_b32 s93, s93, 3\n"
    "s_lshr_b32 s73, s53, 2\n"
    "s_lshl3_add_u32 s92, s73, s65\n"
    "s_lshr_b32 s73, s92, 4\n"
    "s_lshl2_add_u32 s73, s73, s52\n"
    "s_lshl_b32 s68, s73, 6\n"
    "s_mul_i32 s73, s73, 0x30000\n"
    "s_and_b32 s75, s92, 15\n"
    "s_lshl_b32 s75, s75, 7\n"
    "s_add_u32 s91, s73, s75\n"
    "s_mul_i32 s67, s92, 3\n"
    "s_add_u32 s67, s67, s93\n"
    "s_lshl2_add_u32 s67, s67, s52\n"
    "s_lshl_b32 s67, s67, 11\n"
    "s_sub_u32 s67, s67, 8192\n"
    "s_waitcnt lgkmcnt(0)\n"
    "buffer_load_dwordx2 v[50:51], off, s[16:19], 0 sc1\n"
    "s_cmp_lg_u32 s93, 0\n"
    "s_cbranch_scc1 Lnomod1\n"
    "buffer_load_dword v57, v56, s[20:23], s68 offen\n"
    "s_add_u32 m0, s42, 0x20000\n"
    "s_nop 0\n"
    "buffer_load_dwordx4 v52, s[12:15], s91 offen offset:0 nt lds\n"
    "buffer_load_dwordx4 v53, s[12:15], s91 offen offset:1024 nt lds\n"
    "buffer_load_dwordx4 v54, s[12:15], s91 offen offset:2048 nt lds\n"
    "buffer_load_dwordx4 v55, s[12:15], s91 offen offset:3072 nt lds\n"
    "v_add_u32 v7, 7168, v6\n"
    "buffer_load_dwordx4 v[64:67], v32, s[4:7], s87 offen\n"
    "buffer_load_dwordx4 v[80:83], v33, s[4:7], s87 offen\n"
    "buffer_load_dwordx4 v[96:99], v34, s[4:7], s87 offen\n"
    "buffer_load_dwordx4 v[112:115], v35, s[4:7], s87 offen\n"
    "v_add_u32 v8, 14336, v6\n"
    "v_add_u32 v9, 21504, v6\n"
    "v_lshrrev_b32 v16, 2, v60\n"
    "v_and_b32 v17, 3, v60\n"
    "v_lshrrev_b32 v18, 1, v17\n"
    "v_and_b32 v19, 1, v17\n"
    "v_lshlrev_b32 v19, 3, v19\n"
    "v_and_b32 v20, 1, v61\n"
    "v_lshrrev_b32 v25, 5, v1\n"
    "v_lshl_add_u32 v21, v25, 2, v16\n"
    "v_lshl_add_u32 v22, v20, 1, v18\n"
    "v_and_b32 v23, 2, v16\n"
    "buffer_load_dwordx4 v[68:71], v32, s[4:7], s88 offen\n"
    "buffer_load_dwordx4 v[84:87], v33, s[4:7], s88 offen\n"
    "buffer_load_dwordx4 v[100:103], v34, s[4:7], s88 offen\n"
    "buffer_load_dwordx4 v[116:119], v35, s[4:7], s88 offen\n"
    "v_lshlrev_b32 v24, 1, v23\n"
    "v_lshl_or_b32 v26, v20, 1, v24\n"
    "v_lshl_or_b32 v24, v25, 1, v24\n"
    "v_xor_b32 v24, v22, v24\n"
    "v_lshl_add_u32 v11, v21, 7, v19\n"
    "v_lshl_add_u32 v11, v24, 4, v11\n"
    "v_add_u32 v11, s42, v11\n"
    "v_xor_b32 v12, 64, v11\n"
    "s_lshl_b32 s75, s52, 2\n"
    "v_add_u32 v22, s75, v22\n"
    "v_lshlrev_b32 v24, 2, v16\n"
    "v_xor_b32 v22, v22, v24\n"
    "v_lshl_add_u32 v24, v25, 11, v19\n"
    "v_lshl_add_u32 v24, v16, 8, v24\n"
    "v_lshl_add_u32 v24, v22, 4, v24\n"
    "v_accvgpr_write_b32 a193, v24\n"
    "v_lshl_add_u32 v23, v20, 2, v23\n"
    "s_lshl_b32 s74, s52, 1\n"
    "v_add_u32 v18, s74, v18\n"
    "v_xor_b32 v26, v18, v26\n"
    "v_lshl_add_u32 v27, v20, 2, v16\n"
    "v_lshl_add_u32 v27, v27, 7, v19\n"
    "v_lshl_add_u32 v27, v25, 11, v27\n"
    "v_lshl_add_u32 v27, v26, 4, v27\n"
    "v_accvgpr_write_b32 a195, v27\n"
    "v_xor_b32 v18, v18, v23\n"
    "v_lshl_add_u32 v19, v16, 7, v19\n"
    "v_lshl_add_u32 v19, v18, 4, v19\n"
    "v_lshl_add_u32 v37, v61, 13, v19\n"
    "v_lshl_add_u32 v59, v61, 12, v19\n"
    "v_add_u32 v18, s75, v61\n"
    "v_xor_b32 v18, v18, v62\n"
    "v_lshlrev_b32 v5, 10, v16\n"
    "v_lshl_add_u32 v5, v17, 8, v5\n"
    "v_lshl_add_u32 v5, v18, 4, v5\n"
    "v_xor_b32 v18, v60, v61\n"
    "v_lshlrev_b32 v2, 8, v61\n"
    "v_lshl_add_u32 v2, v18, 4, v2\n"
    "v_xor_b32 v3, 64, v2\n"
    "v_add_u32 v38, 0x17800, v36\n"
    "s_lshr_b32 s71, s53, 3\n"
    "s_and_b32 s72, s65, 6\n"
    "s_lshl1_add_u32 s71, s72, s71\n"
    "s_and_b32 s72, s65, 1\n"
    "s_lshl1_add_u32 s67, s71, s72\n"
    "s_add_u32 s68, s53, 1\n"
    "s_and_b32 s68, s68, 7\n"
    "s_mul_i32 s71, s53, 37\n"
    "s_lshr_b32 s71, s71, 8\n"
    "s_mul_i32 s72, s71, 7\n"
    "s_sub_u32 s72, s53, s72\n"
    "s_lshr_b32 s73, s65, 1\n"
    "s_lshl2_add_u32 s69, s73, s71\n"
    "s_and_b32 s73, s65, 1\n"
    "s_lshl_b32 s47, s73, 1\n"
    "s_mul_i32 s74, s73, 7\n"
    "s_add_u32 s70, s74, s72\n"
    "s_lshl2_add_u32 s71, s66, s52\n"
    "s_lshl_b32 s72, s54, 6\n"
    "s_add_u32 s72, s72, s71\n"
    "s_lshl_b32 s72, s72, 10\n"
    "s_add_u32 s55, s72, 0x500000\n"
    "s_lshl_b32 s72, s54, 5\n"
    "s_add_u32 s72, s72, s71\n"
    "s_lshl_b32 s74, s72, 12\n"
    "s_add_u32 s56, s74, 0x100000\n"
    "s_lshl_b32 s74, s72, 7\n"
    "s_add_u32 s60, s74, 0x10000\n"
    "s_lshl_b32 s72, s65, 5\n"
    "s_add_u32 s72, s72, s53\n"
    "s_lshl2_add_u32 s72, s72, s52\n"
    "s_lshl_b32 s72, s72, 3\n"
    "s_add_u32 s61, s72, 0x8000\n"
    "s_lshl_b32 s72, s67, 16\n"
    "s_lshl_b32 s74, s68, 12\n"
    "s_add_u32 s74, s72, s74\n"
    "s_add_u32 s57, s74, 0x700000\n"
    "s_lshl_b32 s73, s68, 7\n"
    "s_lshl1_add_u32 s72, s42, s72\n"
    "s_add_u32 s58, s72, s73\n"
    "s_add_u32 s41, s72, 0x500000\n"
    "s_lshl_b32 s72, s52, 17\n"
    "s_add_u32 s38, s72, s73\n"
    "s_lshl3_add_u32 s72, s42, s73\n"
    "s_and_b32 s74, s67, 1\n"
    "s_mul_i32 s71, s74, 0xe0000\n"
    "s_add_u32 s39, s72, s71\n"
    "s_sub_u32 s71, 0xe0000, s71\n"
    "s_add_u32 s40, s72, s71\n"
    "s_lshl_b32 s72, s67, 12\n"
    "s_lshr_b32 s71, s68, 1\n"
    "s_lshl_b32 s71, s71, 9\n"
    "s_add_u32 s72, s72, s71\n"
    "s_and_b32 s71, s68, 1\n"
    "s_lshl3_add_u32 s72, s71, s72\n"
    "s_add_u32 s59, s72, 0x80000\n"
    "s_lshr_b32 s72, s42, 1\n"
    "s_add_u32 s45, s72, 0x2000\n"
    "s_and_b32 s72, s67, 3\n"
    "s_lshl1_add_u32 s72, s72, s68\n"
    "s_mul_i32 s71, s72, 37\n"
    "s_lshr_b32 s71, s71, 8\n"
    "s_mul_i32 s71, s71, 7\n"
    "s_sub_u32 s72, s72, s71\n"
    "s_min_u32 s72, s65, 6\n"
    "s_lshl_b32 s76, s72, 13\n"
    "s_add_u32 s77, s76, 0x2000\n"
    "s_cmp_eq_u32 s77, 0xe000\n"
    "s_cselect_b32 s77, 0, s77\n"
    "s_add_u32 s78, s77, 0x2000\n"
    "s_cmp_eq_u32 s78, 0xe000\n"
    "s_cselect_b32 s78, 0, s78\n"
    "s_add_u32 s79, s78, 0x2000\n"
    "s_cmp_eq_u32 s79, 0xe000\n"
    "s_cselect_b32 s79, 0, s79\n"
    "s_add_u32 s80, s79, 0x2000\n"
    "s_cmp_eq_u32 s80, 0xe000\n"
    "s_cselect_b32 s80, 0, s80\n"
    "s_add_u32 s81, s80, 0x2000\n"
    "s_cmp_eq_u32 s81, 0xe000\n"
    "s_cselect_b32 s81, 0, s81\n"
    "s_add_u32 s82, s81, 0x2000\n"
    "s_cmp_eq_u32 s82, 0xe000\n"
    "s_cselect_b32 s82, 0, s82\n"
    "s_sub_u32 s72, s56, 0x20000\n"
    "s_cmp_gt_u32 s54, 0\n"
    "s_cselect_b32 s83, s72, 0x70000000\n"
    "s_sub_u32 s72, s56, 0x40000\n"
    "s_cmp_gt_u32 s54, 1\n"
    "s_cselect_b32 s84, s72, 0x70000000\n"
    "s_sub_u32 s72, s56, 0x60000\n"
    "s_cmp_gt_u32 s54, 2\n"
    "s_cselect_b32 s85, s72, 0x70000000\n"
    "s_sub_u32 s72, s56, 0x80000\n"
    "s_cmp_gt_u32 s54, 3\n"
    "s_cselect_b32 s86, s72, 0x70000000\n"
    "v_cmp_eq_u32 vcc, 3, v4\n"
    "v_add_u32 v16, 0x0, v0\n"
    "s_lshr_b32 s71, s42, 2\n"
    "s_add_u32 s57, s57, s71\n"
    "v_cndmask_b32 v10, v0, v16, vcc\n"
    "v_lshlrev_b32 v13, 4, v62\n"
    "v_lshl_add_u32 v13, v4, 10, v13\n"
    "s_and_b32 s71, s52, 1\n"
    "s_lshl_b32 s71, s71, 1\n"
    "s_sub_u32 s71, s71, s94\n"
    "v_lshrrev_b32 v48, 5, v1\n"
    "v_add_u32 v48, s71, v48\n"
    "v_and_b32 v48, 3, v48\n"
    "v_lshlrev_b32 v48, 9, v48\n"
    "v_and_b32 v16, 1, v1\n"
    "v_lshl_add_u32 v48, v16, 8, v48\n"
    "v_bfe_u32 v16, v1, 1, 4\n"
    "v_lshl_add_u32 v48, v16, 4, v48\n"
    "s_lshr_b32 s71, s52, 1\n"
    "s_lshl_b32 s72, s71, 11\n"
    "s_add_u32 s72, s72, 0x24000\n"
    "v_add_u32 v48, s72, v48\n"
    "v_min_u32 v16, 3, v1\n"
    "v_add_u32 v16, 1, v16\n"
    "v_min_u32 v16, s54, v16\n"
    "v_lshlrev_b32 v16, 12, v16\n"
    "v_sub_u32 v16, s60, v16\n"
    "v_accvgpr_write_b32 a194, v16\n"
    "s_sub_u32 s71, s53, 26\n"
    "s_lshl3_add_u32 s46, s71, s65\n"
    "v_lshlrev_b32 v16, 2, v61\n"
    "v_xor_b32 v16, v16, v60\n"
    "v_mul_u32_u24 v17, 0xe00, v61\n"
    "v_lshl_add_u32 v17, v16, 4, v17\n"
    "s_mul_i32 s71, s52, 0x70000\n"
    "s_lshl_b32 s72, s70, 8\n"
    "s_add_u32 s71, s71, s72\n"
    "s_mul_i32 s72, s52, 0xc0000\n"
    "s_lshl_b32 s73, s46, 7\n"
    "s_add_u32 s72, s72, s73\n"
    "s_cmp_lt_u32 s53, 28\n"
    "s_cselect_b32 s43, s71, s72\n"
    "s_cselect_b32 s44, 14, 48\n"
    "s_cselect_b64 vcc, -1, 0\n"
    "v_cndmask_b32 v17, v52, v17, vcc\n"
    "v_accvgpr_write_b32 a192, v17\n"
    "v_add_u32 v56, 0x10000, v37\n"
    "v_mov_b32 v52, v36\n"
    "v_add_u32 v53, 0x17c00, v52\n"
    "v_add_u32 v54, 0x17c00, v53\n"
    "v_add_u32 v55, 0x17c00, v54\n"
    "v_add_u32 v58, 0x20000, v59\n"
    "v_add_u32 v59, 0x10000, v59\n"
    "v_accvgpr_write_b32 a196, -1\n"
    "v_accvgpr_write_b32 a197, -1\n"
    "v_accvgpr_write_b32 a198, -1\n"
    "v_accvgpr_write_b32 a199, -1\n"
    "s_waitcnt vmcnt(13)\n"
    "v_readfirstlane_b32 s62, v50\n"
    "v_readfirstlane_b32 s63, v51\n"
    "s_mul_i32 s62, s62, 1664525\n"
    "s_add_u32 s62, s62, 1013904223\n"
    "s_mul_i32 s63, s63, 22695477\n"
    "s_add_u32 s63, s63, 1\n"
    "s_waitcnt vmcnt(12)\n"
    "v_lshlrev_b32 v192, 16, v57\n"
    "v_and_b32 v193, s37, v57\n"
    "v_mul_f32 v194, 0xbfb8aa3b, v192\n"
    "v_mul_f32 v195, 0xbfb8aa3b, v193\n"
    "v_exp_f32 v194, v194\n"
    "v_exp_f32 v195, v195\n"
    "s_lshr_b32 s74, s42, 4\n"
    "v_add_f32 v194, 1.0, v194\n"
    "v_add_f32 v195, 1.0, v195\n"
    "v_rcp_f32 v194, v194\n"
    "v_rcp_f32 v195, v195\n"
    "s_add_u32 s74, s74, 0x25000\n"
    "v_mul_f32 v192, v192, v194\n"
    "v_mul_f32 v193, v193, v195\n"
    "v_cvt_pk_bf16_f32 v192, v192, v193\n"
    "v_lshl_add_u32 v193, v4, 5, s74\n"
    "v_and_b32 v194, 2, v1\n"
    "v_lshl_add_u32 v193, v194, 3, v193\n"
    "v_and_b32 v194, 4, v1\n"
    "v_lshl_add_u32 v193, v194, 1, v193\n"
    "v_and_b32 v194, 1, v1\n"
    "v_lshl_add_u32 v193, v194, 2, v193\n"
    "ds_write_b32 v193, v192\n"
    "v_lshrrev_b32 v195, 5, v1\n"
    "v_lshl_add_u32 v195, v195, 4, s74\n"
    "v_add_u32 v198, 0x20000, v11\n"
    "v_add_u32 v199, 0x20000, v12\n"
    "ds_read_b128 v[128:131], v195 offset:0\n"
    "ds_read_b128 v[132:135], v195 offset:32\n"
    "v_lshl_add_u32 v192, v1, 2, s74\n"
    "v_and_b32 v193, 31, v1\n"
    "v_lshlrev_b32 v193, 3, v193\n"
    "v_add_u32 v194, 0x25400, v193\n"
    "s_lshl_b32 s73, s92, 8\n"
    "s_add_u32 s73, s73, 0x4000\n"
    "s_waitcnt vmcnt(8)\n"
    "ds_read_b64_tr_b16 a[0:1], v198 offset:0\n"
    "ds_read_b64_tr_b16 a[2:3], v198 offset:1024\n"
    "ds_read_b64_tr_b16 a[4:5], v199 offset:0\n"
    "ds_read_b64_tr_b16 a[6:7], v199 offset:1024\n"
    "ds_read_b64_tr_b16 a[8:9], v198 offset:2048\n"
    "ds_read_b64_tr_b16 a[10:11], v198 offset:3072\n"
    "ds_read_b64_tr_b16 a[12:13], v199 offset:2048\n"
    "ds_read_b64_tr_b16 a[14:15], v199 offset:3072\n"
    "s_waitcnt lgkmcnt(6)\n"
    "v_mfma_f32_32x32x16_bf16 v[160:175], v[128:131], a[0:3], 0\n"
    "s_waitcnt lgkmcnt(4)\n"
    "v_mfma_f32_32x32x16_bf16 v[176:191], v[128:131], a[4:7], 0\n"
    "s_waitcnt lgkmcnt(2)\n"
    "v_mfma_f32_32x32x16_bf16 v[160:175], v[132:135], a[8:11], v[160:175]\n"
    "s_waitcnt lgkmcnt(0)\n"
    "v_mfma_f32_32x32x16_bf16 v[176:191], v[132:135], a[12:15], v[176:191]\n"
    "s_nop 11\n"
    "v_permlane32_swap_b32 v160, v176\n"
    "ds_write_b32 v192, v160 offset:1024\n"
    "s_waitcnt lgkmcnt(0)\n"
    "s_barrier\n"
    "s_cmp_lg_u32 s52, 0\n"
    "s_cbranch_scc1 Lgst_1\n"
    "ds_read_b64 v[162:163], v194 offset:0\n"
    "ds_read_b64 v[164:165], v194 offset:256\n"
    "ds_read_b64 v[166:167], v194 offset:512\n"
    "ds_read_b64 v[168:169], v194 offset:768\n"
    "s_waitcnt lgkmcnt(2)\n"
    "v_pk_add_f32 v[162:163], v[162:163], v[164:165]\n"
    "s_waitcnt lgkmcnt(0)\n"
    "v_pk_add_f32 v[166:167], v[166:167], v[168:169]\n"
    "v_pk_add_f32 v[162:163], v[162:163], v[166:167]\n"
    "v_cvt_pk_bf16_f32 v162, v162, v163\n"
    "v_mov_b32 v163, s62\n"
    "buffer_store_dwordx2 v[162:163], v193, s[16:19], s73 offen sc1\n"
    "Lgst_1:\n"
    "buffer_load_dwordx4 v[72:75], v32, s[4:7], s89 offen\n"
    "buffer_load_dwordx4 v[88:91], v33, s[4:7], s89 offen\n"
    "buffer_load_dwordx4 v[104:107], v34, s[4:7], s89 offen\n"
    "buffer_load_dwordx4 v[120:123], v35, s[4:7], s89 offen\n"
    "buffer_load_dwordx4 v[76:79], v32, s[4:7], s90 offen\n"
    "buffer_load_dwordx4 v[92:95], v33, s[4:7], s90 offen\n"
    "buffer_load_dwordx4 v[108:111], v34, s[4:7], s90 offen\n"
    "buffer_load_dwordx4 v[124:127], v35, s[4:7], s90 offen\n"
    "s_branch Lmod1z\n"
    "Lnomod1:\n"
    "buffer_load_dword v58, v57, s[8:11], s67 offen\n"
    "buffer_load_dwordx4 v[64:67], v32, s[4:7], s87 offen\n"
    "buffer_load_dwordx4 v[80:83], v33, s[4:7], s87 offen\n"
    "buffer_load_dwordx4 v[96:99], v34, s[4:7], s87 offen\n"
    "buffer_load_dwordx4 v[112:115], v35, s[4:7], s87 offen\n"
    "v_add_u32 v7, 7168, v6\n"
    "buffer_load_dwordx4 v[68:71], v32, s[4:7], s88 offen\n"
    "buffer_load_dwordx4 v[84:87], v33, s[4:7], s88 offen\n"
    "buffer_load_dwordx4 v[100:103], v34, s[4:7], s88 offen\n"
    "buffer_load_dwordx4 v[116:119], v35, s[4:7], s88 offen\n"
    "v_add_u32 v8, 14336, v6\n"
    "v_add_u32 v9, 21504, v6\n"
    "v_lshrrev_b32 v16, 2, v60\n"
    "v_and_b32 v17, 3, v60\n"
    "v_lshrrev_b32 v18, 1, v17\n"
    "v_and_b32 v19, 1, v17\n"
    "v_lshlrev_b32 v19, 3, v19\n"
    "v_and_b32 v20, 1, v61\n"
    "v_lshrrev_b32 v25, 5, v1\n"
    "v_lshl_add_u32 v21, v25, 2, v16\n"
    "v_lshl_add_u32 v22, v20, 1, v18\n"
    "v_and_b32 v23, 2, v16\n"
    "v_lshlrev_b32 v24, 1, v23\n"
    "v_lshl_or_b32 v26, v20, 1, v24\n"
    "v_lshl_or_b32 v24, v25, 1, v24\n"
    "v_xor_b32 v24, v22, v24\n"
    "v_lshl_add_u32 v11, v21, 7, v19\n"
    "v_lshl_add_u32 v11, v24, 4, v11\n"
    "v_add_u32 v11, s42, v11\n"
    "v_xor_b32 v12, 64, v11\n"
    "s_lshl_b32 s75, s52, 2\n"
    "v_add_u32 v22, s75, v22\n"
    "v_lshlrev_b32 v24, 2, v16\n"
    "v_xor_b32 v22, v22, v24\n"
    "v_lshl_add_u32 v24, v25, 11, v19\n"
    "v_lshl_add_u32 v24, v16, 8, v24\n"
    "v_lshl_add_u32 v24, v22, 4, v24\n"
    "v_accvgpr_write_b32 a193, v24\n"
    "v_lshl_add_u32 v23, v20, 2, v23\n"
    "s_lshl_b32 s74, s52, 1\n"
    "v_add_u32 v18, s74, v18\n"
    "buffer_load_dwordx4 v[72:75], v32, s[4:7], s89 offen\n"
    "buffer_load_dwordx4 v[88:91], v33, s[4:7], s89 offen\n"
    "buffer_load_dwordx4 v[104:107], v34, s[4:7], s89 offen\n"
    "buffer_load_dwordx4 v[120:123], v35, s[4:7], s89 offen\n"
    "v_xor_b32 v26, v18, v26\n"
    "v_lshl_add_u32 v27, v20, 2, v16\n"
    "v_lshl_add_u32 v27, v27, 7, v19\n"
    "v_lshl_add_u32 v27, v25, 11, v27\n"
    "v_lshl_add_u32 v27, v26, 4, v27\n"
    "v_accvgpr_write_b32 a195, v27\n"
    "v_xor_b32 v18, v18, v23\n"
    "v_lshl_add_u32 v19, v16, 7, v19\n"
    "v_lshl_add_u32 v19, v18, 4, v19\n"
    "v_lshl_add_u32 v37, v61, 13, v19\n"
    "v_lshl_add_u32 v59, v61, 12, v19\n"
    "v_add_u32 v18, s75, v61\n"
    "v_xor_b32 v18, v18, v62\n"
    "v_lshlrev_b32 v5, 10, v16\n"
    "v_lshl_add_u32 v5, v17, 8, v5\n"
    "v_lshl_add_u32 v5, v18, 4, v5\n"
    "v_xor_b32 v18, v60, v61\n"
    "v_lshlrev_b32 v2, 8, v61\n"
    "v_lshl_add_u32 v2, v18, 4, v2\n"
    "v_xor_b32 v3, 64, v2\n"
    "v_add_u32 v38, 0x17800, v36\n"
    "s_lshr_b32 s71, s53, 3\n"
    "buffer_load_dwordx4 v[76:79], v32, s[4:7], s90 offen\n"
    "buffer_load_dwordx4 v[92:95], v33, s[4:7], s90 offen\n"
    "buffer_load_dwordx4 v[108:111], v34, s[4:7], s90 offen\n"
    "buffer_load_dwordx4 v[124:127], v35, s[4:7], s90 offen\n"
    "s_and_b32 s72, s65, 6\n"
    "s_lshl1_add_u32 s71, s72, s71\n"
    "s_and_b32 s72, s65, 1\n"
    "s_lshl1_add_u32 s67, s71, s72\n"
    "s_add_u32 s68, s53, 1\n"
    "s_and_b32 s68, s68, 7\n"
    "s_mul_i32 s71, s53, 37\n"
    "s_lshr_b32 s71, s71, 8\n"
    "s_mul_i32 s72, s71, 7\n"
    "s_sub_u32 s72, s53, s72\n"
    "s_lshr_b32 s73, s65, 1\n"
    "s_lshl2_add_u32 s69, s73, s71\n"
    "s_and_b32 s73, s65, 1\n"
    "s_lshl_b32 s47, s73, 1\n"
    "s_mul_i32 s74, s73, 7\n"
    "s_add_u32 s70, s74, s72\n"
    "s_lshl2_add_u32 s71, s66, s52\n"
    "s_lshl_b32 s72, s54, 6\n"
    "s_add_u32 s72, s72, s71\n"
    "s_lshl_b32 s72, s72, 10\n"
    "s_add_u32 s55, s72, 0x500000\n"
    "s_lshl_b32 s72, s54, 5\n"
    "s_add_u32 s72, s72, s71\n"
    "s_lshl_b32 s74, s72, 12\n"
    "s_add_u32 s56, s74, 0x100000\n"
    "s_lshl_b32 s74, s72, 7\n"
    "s_add_u32 s60, s74, 0x10000\n"
    "s_lshl_b32 s72, s65, 5\n"
    "s_add_u32 s72, s72, s53\n"
    "s_lshl2_add_u32 s72, s72, s52\n"
    "s_lshl_b32 s72, s72, 3\n"
    "s_add_u32 s61, s72, 0x8000\n"
    "s_lshl_b32 s72, s67, 16\n"
    "s_lshl_b32 s74, s68, 12\n"
    "s_add_u32 s74, s72, s74\n"
    "s_add_u32 s57, s74, 0x700000\n"
    "s_lshl_b32 s73, s68, 7\n"
    "s_lshl1_add_u32 s72, s42, s72\n"
    "s_add_u32 s58, s72, s73\n"
    "s_add_u32 s41, s72, 0x500000\n"
    "s_lshl_b32 s72, s52, 17\n"
    "s_add_u32 s38, s72, s73\n"
    "s_lshl3_add_u32 s72, s42, s73\n"
    "s_and_b32 s74, s67, 1\n"
    "s_mul_i32 s71, s74, 0xe0000\n"
    "s_add_u32 s39, s72, s71\n"
    "s_sub_u32 s71, 0xe0000, s71\n"
    "s_add_u32 s40, s72, s71\n"
    "s_lshl_b32 s72, s67, 12\n"
    "s_lshr_b32 s71, s68, 1\n"
    "s_lshl_b32 s71, s71, 9\n"
    "s_add_u32 s72, s72, s71\n"
    "s_and_b32 s71, s68, 1\n"
    "s_lshl3_add_u32 s72, s71, s72\n"
    "s_add_u32 s59, s72, 0x80000\n"
    "s_lshr_b32 s72, s42, 1\n"
    "s_add_u32 s45, s72, 0x2000\n"
    "s_and_b32 s72, s67, 3\n"
    "s_lshl1_add_u32 s72, s72, s68\n"
    "s_mul_i32 s71, s72, 37\n"
    "s_lshr_b32 s71, s71, 8\n"
    "s_mul_i32 s71, s71, 7\n"
    "s_sub_u32 s72, s72, s71\n"
    "s_min_u32 s72, s65, 6\n"
    "s_lshl_b32 s76, s72, 13\n"
    "s_add_u32 s77, s76, 0x2000\n"
    "s_cmp_eq_u32 s77, 0xe000\n"
    "s_cselect_b32 s77, 0, s77\n"
    "s_add_u32 s78, s77, 0x2000\n"
    "s_cmp_eq_u32 s78, 0xe000\n"
    "s_cselect_b32 s78, 0, s78\n"
    "s_add_u32 s79, s78, 0x2000\n"
    "s_cmp_eq_u32 s79, 0xe000\n"
    "s_cselect_b32 s79, 0, s79\n"
    "s_add_u32 s80, s79, 0x2000\n"
    "s_cmp_eq_u32 s80, 0xe000\n"
    "s_cselect_b32 s80, 0, s80\n"
    "s_add_u32 s81, s80, 0x2000\n"
    "s_cmp_eq_u32 s81, 0xe000\n"
    "s_cselect_b32 s81, 0, s81\n"
    "s_add_u32 s82, s81, 0x2000\n"
    "s_cmp_eq_u32 s82, 0xe000\n"
    "s_cselect_b32 s82, 0, s82\n"
    "s_sub_u32 s72, s56, 0x20000\n"
    "s_cmp_gt_u32 s54, 0\n"
    "s_cselect_b32 s83, s72, 0x70000000\n"
    "s_sub_u32 s72, s56, 0x40000\n"
    "s_cmp_gt_u32 s54, 1\n"
    "s_cselect_b32 s84, s72, 0x70000000\n"
    "s_sub_u32 s72, s56, 0x60000\n"
    "s_cmp_gt_u32 s54, 2\n"
    "s_cselect_b32 s85, s72, 0x70000000\n"
    "s_sub_u32 s72, s56, 0x80000\n"
    "s_cmp_gt_u32 s54, 3\n"
    "s_cselect_b32 s86, s72, 0x70000000\n"
    "v_cmp_eq_u32 vcc, 3, v4\n"
    "v_add_u32 v16, 0x0, v0\n"
    "s_lshr_b32 s71, s42, 2\n"
    "s_add_u32 s57, s57, s71\n"
    "v_cndmask_b32 v10, v0, v16, vcc\n"
    "v_lshlrev_b32 v13, 4, v62\n"
    "v_lshl_add_u32 v13, v4, 10, v13\n"
    "s_and_b32 s71, s52, 1\n"
    "s_lshl_b32 s71, s71, 1\n"
    "s_sub_u32 s71, s71, s94\n"
    "v_lshrrev_b32 v48, 5, v1\n"
    "v_add_u32 v48, s71, v48\n"
    "v_and_b32 v48, 3, v48\n"
    "v_lshlrev_b32 v48, 9, v48\n"
    "v_and_b32 v16, 1, v1\n"
    "v_lshl_add_u32 v48, v16, 8, v48\n"
    "v_bfe_u32 v16, v1, 1, 4\n"
    "v_lshl_add_u32 v48, v16, 4, v48\n"
    "s_lshr_b32 s71, s52, 1\n"
    "s_lshl_b32 s72, s71, 11\n"
    "s_add_u32 s72, s72, 0x24000\n"
    "v_add_u32 v48, s72, v48\n"
    "v_min_u32 v16, 3, v1\n"
    "v_add_u32 v16, 1, v16\n"
    "v_min_u32 v16, s54, v16\n"
    "v_lshlrev_b32 v16, 12, v16\n"
    "v_sub_u32 v16, s60, v16\n"
    "v_accvgpr_write_b32 a194, v16\n"
    "s_sub_u32 s71, s53, 26\n"
    "s_lshl3_add_u32 s46, s71, s65\n"
    "v_lshlrev_b32 v16, 2, v61\n"
    "v_xor_b32 v16, v16, v60\n"
    "v_mul_u32_u24 v17, 0xe00, v61\n"
    "v_lshl_add_u32 v17, v16, 4, v17\n"
    "s_mul_i32 s71, s52, 0x70000\n"
    "s_lshl_b32 s72, s70, 8\n"
    "s_add_u32 s71, s71, s72\n"
    "s_mul_i32 s72, s52, 0xc0000\n"
    "s_lshl_b32 s73, s46, 7\n"
    "s_add_u32 s72, s72, s73\n"
    "s_cmp_lt_u32 s53, 28\n"
    "s_cselect_b32 s43, s71, s72\n"
    "s_cselect_b32 s44, 14, 48\n"
    "s_cselect_b64 vcc, -1, 0\n"
    "v_cndmask_b32 v17, v52, v17, vcc\n"
    "v_accvgpr_write_b32 a192, v17\n"
    "v_add_u32 v56, 0x10000, v37\n"
    "v_mov_b32 v52, v36\n"
    "v_add_u32 v53, 0x17c00, v52\n"
    "v_add_u32 v54, 0x17c00, v53\n"
    "v_add_u32 v55, 0x17c00, v54\n"
    "s_waitcnt vmcnt(16)\n"
    "v_add_u32 v58, 0x20000, v59\n"
    "v_add_u32 v59, 0x10000, v59\n"
    "v_accvgpr_write_b32 a196, -1\n"
    "v_accvgpr_write_b32 a197, -1\n"
    "v_accvgpr_write_b32 a198, -1\n"
    "v_accvgpr_write_b32 a199, -1\n"
    "v_readfirstlane_b32 s62, v50\n"
    "v_readfirstlane_b32 s63, v51\n"
    "s_mul_i32 s62, s62, 1664525\n"
    "s_add_u32 s62, s62, 1013904223\n"
    "s_mul_i32 s63, s63, 22695477\n"
    "s_add_u32 s63, s63, 1\n"
    "Lmod1z:\n"
    "s_lshl2_add_u32 s71, s69, s47\n"
    "s_add_u32 s75, s96, 0x400\n"
    "s_lshl1_add_u32 m0, s42, 0x0\n"
    "s_nop 0\n"
    "buffer_load_dwordx4 v36, s[8:11], s75 offen offset:0 lds\n"
    "buffer_load_dwordx4 v36, s[8:11], s75 offen offset:1024 lds\n"
    "buffer_load_dwordx4 v38, s[8:11], s75 offen offset:2048 lds\n"
    "buffer_load_dwordx4 v38, s[8:11], s75 offen offset:3072 lds\n"
    "s_lshl_b32 s72, s71, 15\n"
    "s_lshl1_add_u32 s72, s42, s72\n"
    "s_add_u32 s87, s72, 0x700000\n"
    "s_lshl_b32 s72, s69, 13\n"
    "s_lshr_b32 s73, s42, 3\n"
    "s_add_u32 s72, s72, s73\n"
    "s_add_u32 s88, s72, 0x80000\n"
    "s_lshl2_add_u32 s73, s70, s52\n"
    "s_lshl_b32 s74, s73, 11\n"
    "s_add_u32 s74, s74, 0x900000\n"
    "s_mul_i32 s72, s71, 114688\n"
    "s_add_u32 s89, s72, s74\n"
    "s_xor_b32 s72, s71, 2\n"
    "s_mul_i32 s72, s72, 114688\n"
    "s_add_u32 s90, s72, s74\n"
    "s_lshl_b32 s72, s71, 12\n"
    "s_lshr_b32 s74, s42, 2\n"
    "s_add_u32 s74, s74, 0xc0000\n"
    "s_add_u32 s72, s72, s74\n"
    "s_lshl_b32 s71, s70, 6\n"
    "s_add_u32 s91, s72, s71\n"
    "s_lshl_b32 s72, s67, 13\n"
    "s_add_u32 s92, s72, s74\n"
    "s_mul_i32 s72, s67, 0x38000\n"
    "s_lshr_b32 s71, s42, 1\n"
    "s_add_u32 s72, s72, s71\n"
    "s_add_u32 s72, s72, 0x900000\n"
    "s_and_b32 s74, s67, 1\n"
    "s_mul_i32 s71, s74, 57344\n"
    "s_add_u32 s93, s72, s71\n"
    "s_sub_u32 s71, 57344, s71\n"
    "s_add_u32 s94, s72, s71\n"
    "s_lshl_b32 s95, s73, 6\n"
    "s_cmp_ge_u32 s53, 28\n"
    "s_cselect_b32 s87, 0x70000000, s87\n"
    "s_cselect_b32 s88, 0x70000000, s88\n"
    "s_cselect_b32 s91, 0x70000000, s91\n"
    "s_cselect_b32 s90, 0x70000000, s90\n"
    "s_lshr_b32 s74, s42, 2\n"
    "s_add_u32 s66, s74, 0x4000\n"
    "s_add_u32 s67, s66, 0x1000\n"
    "s_add_u32 s68, s67, 0x1000\n"
    "s_add_u32 s69, s68, 0x1000\n"
    "s_cmp_ge_u32 s52, 2\n"
    "s_cselect_b32 s70, 1.0, 0\n"
    "s_waitcnt vmcnt(19)\n"
    "v_lshlrev_b32 v128, 16, v64\n"
    "v_and_b32 v129, s37, v64\n"
    "v_pk_mul_f32 v[18:19], v[128:129], v[128:129]\n"
    "v_lshlrev_b32 v130, 16, v65\n"
    "v_and_b32 v131, s37, v65\n"
    "v_pk_add_f32 v[16:17], v[128:129], v[130:131]\n"
    "s_add_u32 s75, s96, 0x30400\n"
    "s_lshl1_add_u32 m0, s42, 0x1000\n"
    "s_nop 0\n"
    "buffer_load_dwordx4 v36, s[8:11], s75 offen offset:0 lds\n"
    "buffer_load_dwordx4 v36, s[8:11], s75 offen offset:1024 lds\n"
    "buffer_load_dwordx4 v38, s[8:11], s75 offen offset:2048 lds\n"
    "buffer_load_dwordx4 v38, s[8:11], s75 offen offset:3072 lds\n"
    "v_pk_fma_f32 v[18:19], v[130:131], v[130:131], v[18:19]\n"
    "v_lshlrev_b32 v132, 16, v66\n"
    "v_and_b32 v133, s37, v66\n"
    "v_pk_add_f32 v[16:17], v[16:17], v[132:133]\n"
    "v_pk_fma_f32 v[18:19], v[132:133], v[132:133], v[18:19]\n"
    "v_lshlrev_b32 v134, 16, v67\n"
    "v_and_b32 v135, s37, v67\n"
    "v_pk_add_f32 v[16:17], v[16:17], v[134:135]\n"
    "v_pk_fma_f32 v[18:19], v[134:135], v[134:135], v[18:19]\n"
    "s_waitcnt vmcnt(22)\n"
    "v_lshlrev_b32 v160, 16, v80\n"
    "v_and_b32 v161, s37, v80\n"
    "v_pk_mul_f32 v[22:23], v[160:161], v[160:161]\n"
    "v_lshlrev_b32 v162, 16, v81\n"
    "v_and_b32 v163, s37, v81\n"
    "v_pk_add_f32 v[20:21], v[160:161], v[162:163]\n"
    "v_pk_fma_f32 v[22:23], v[162:163], v[162:163], v[22:23]\n"
    "v_lshlrev_b32 v164, 16, v82\n"
    "v_and_b32 v165, s37, v82\n"
    "v_pk_add_f32 v[20:21], v[20:21], v[164:165]\n"
    "v_pk_fma_f32 v[22:23], v[164:165], v[164:165], v[22:23]\n"
    "v_lshlrev_b32 v166, 16, v83\n"
    "v_and_b32 v167, s37, v83\n"
    "v_pk_add_f32 v[20:21], v[20:21], v[166:167]\n"
    "v_pk_fma_f32 v[22:23], v[166:167], v[166:167], v[22:23]\n"
    "s_waitcnt vmcnt(21)\n"
    "v_lshlrev_b32 v192, 16, v96\n"
    "v_and_b32 v193, s37, v96\n"
    "v_pk_mul_f32 v[26:27], v[192:193], v[192:193]\n"
    "v_lshlrev_b32 v194, 16, v97\n"
    "v_and_b32 v195, s37, v97\n"
    "v_pk_add_f32 v[24:25], v[192:193], v[194:195]\n"
    "v_pk_fma_f32 v[26:27], v[194:195], v[194:195], v[26:27]\n"
    "v_lshlrev_b32 v196, 16, v98\n"
    "v_and_b32 v197, s37, v98\n"
    "v_pk_add_f32 v[24:25], v[24:25], v[196:197]\n"
    "v_pk_fma_f32 v[26:27], v[196:197], v[196:197], v[26:27]\n"
    "v_lshlrev_b32 v198, 16, v99\n"
    "v_and_b32 v199, s37, v99\n"
    "v_pk_add_f32 v[24:25], v[24:25], v[198:199]\n"
    "v_pk_fma_f32 v[26:27], v[198:199], v[198:199], v[26:27]\n"
    "s_waitcnt vmcnt(20)\n"
    "v_lshlrev_b32 v224, 16, v112\n"
    "v_and_b32 v225, s37, v112\n"
    "v_pk_mul_f32 v[30:31], v[224:225], v[224:225]\n"
    "v_lshlrev_b32 v226, 16, v113\n"
    "v_and_b32 v227, s37, v113\n"
    "v_pk_add_f32 v[28:29], v[224:225], v[226:227]\n"
    "v_pk_fma_f32 v[30:31], v[226:227], v[226:227], v[30:31]\n"
    "v_lshlrev_b32 v228, 16, v114\n"
    "v_and_b32 v229, s37, v114\n"
    "v_pk_add_f32 v[28:29], v[28:29], v[228:229]\n"
    "v_pk_fma_f32 v[30:31], v[228:229], v[228:229], v[30:31]\n"
    "v_lshlrev_b32 v230, 16, v115\n"
    "v_and_b32 v231, s37, v115\n"
    "v_pk_add_f32 v[28:29], v[28:29], v[230:231]\n"
    "v_pk_fma_f32 v[30:31], v[230:231], v[230:231], v[30:31]\n"
    "s_waitcnt vmcnt(19)\n"
    "v_lshlrev_b32 v136, 16, v68\n"
    "v_and_b32 v137, s37, v68\n"
    "v_pk_add_f32 v[16:17], v[16:17], v[136:137]\n"
    "v_pk_fma_f32 v[18:19], v[136:137], v[136:137], v[18:19]\n"
    "v_lshlrev_b32 v138, 16, v69\n"
    "v_and_b32 v139, s37, v69\n"
    "v_pk_add_f32 v[16:17], v[16:17], v[138:139]\n"
    "v_pk_fma_f32 v[18:19], v[138:139], v[138:139], v[18:19]\n"
    "v_lshlrev_b32 v140, 16, v70\n"
    "v_and_b32 v141, s37, v70\n"
    "v_pk_add_f32 v[16:17], v[16:17], v[140:141]\n"
    "v_pk_fma_f32 v[18:19], v[140:141], v[140:141], v[18:19]\n"
    "v_lshlrev_b32 v142, 16, v71\n"
    "s_add_u32 s75, s97, 0x400\n"
    "s_lshl1_add_u32 m0, s42, 0x8000\n"
    "s_nop 0\n"
    "buffer_load_dwordx4 v36, s[8:11], s75 offen offset:0 lds\n"
    "buffer_load_dwordx4 v36, s[8:11], s75 offen offset:1024 lds\n"
    "buffer_load_dwordx4 v38, s[8:11], s75 offen offset:2048 lds\n"
    "buffer_load_dwordx4 v38, s[8:11], s75 offen offset:3072 lds\n"
    "v_and_b32 v143, s37, v71\n"
    "v_pk_add_f32 v[16:17], v[16:17], v[142:143]\n"
    "v_pk_fma_f32 v[18:19], v[142:143], v[142:143], v[18:19]\n"
    "s_waitcnt vmcnt(22)\n"
    "v_lshlrev_b32 v168, 16, v84\n"
    "v_and_b32 v169, s37, v84\n"
    "v_pk_add_f32 v[20:21], v[20:21], v[168:169]\n"
    "v_pk_fma_f32 v[22:23], v[168:169], v[168:169], v[22:23]\n"
    "v_lshlrev_b32 v170, 16, v85\n"
    "v_and_b32 v171, s37, v85\n"
    "v_pk_add_f32 v[20:21], v[20:21], v[170:171]\n"
    "v_pk_fma_f32 v[22:23], v[170:171], v[170:171], v[22:23]\n"
    "v_lshlrev_b32 v172, 16, v86\n"
    "v_and_b32 v173, s37, v86\n"
    "v_pk_add_f32 v[20:21], v[20:21], v[172:173]\n"
    "v_pk_fma_f32 v[22:23], v[172:173], v[172:173], v[22:23]\n"
    "v_lshlrev_b32 v174, 16, v87\n"
    "v_and_b32 v175, s37, v87\n"
    "v_pk_add_f32 v[20:21], v[20:21], v[174:175]\n"
    "v_pk_fma_f32 v[22:23], v[174:175], v[174:175], v[22:23]\n"
    "s_waitcnt vmcnt(21)\n"
    "v_lshlrev_b32 v200, 16, v100\n"
    "v_and_b32 v201, s37, v100\n"
    "v_pk_add_f32 v[24:25], v[24:25], v[200:201]\n"
    "v_pk_fma_f32 v[26:27], v[200:201], v[200:201], v[26:27]\n"
    "v_lshlrev_b32 v202, 16, v101\n"
    "v_and_b32 v203, s37, v101\n"
    "v_pk_add_f32 v[24:25], v[24:25], v[202:203]\n"
    "v_pk_fma_f32 v[26:27], v[202:203], v[202:203], v[26:27]\n"
    "v_lshlrev_b32 v204, 16, v102\n"
    "v_and_b32 v205, s37, v102\n"
    "v_pk_add_f32 v[24:25], v[24:25], v[204:205]\n"
    "v_pk_fma_f32 v[26:27], v[204:205], v[204:205], v[26:27]\n"
    "v_lshlrev_b32 v206, 16, v103\n"
    "v_and_b32 v207, s37, v103\n"
    "v_pk_add_f32 v[24:25], v[24:25], v[206:207]\n"
    "v_pk_fma_f32 v[26:27], v[206:207], v[206:207], v[26:27]\n"
    "s_waitcnt vmcnt(20)\n"
    "v_lshlrev_b32 v232, 16, v116\n"
    "v_and_b32 v233, s37, v116\n"
    "v_pk_add_f32 v[28:29], v[28:29], v[232:233]\n"
    "v_pk_fma_f32 v[30:31], v[232:233], v[232:233], v[30:31]\n"
    "v_lshlrev_b32 v234, 16, v117\n"
    "v_and_b32 v235, s37, v117\n"
    "v_pk_add_f32 v[28:29], v[28:29], v[234:235]\n"
    "v_pk_fma_f32 v[30:31], v[234:235], v[234:235], v[30:31]\n"
    "v_lshlrev_b32 v236, 16, v118\n"
    "v_and_b32 v237, s37, v118\n"
    "v_pk_add_f32 v[28:29], v[28:29], v[236:237]\n"
    "v_pk_fma_f32 v[30:31], v[236:237], v[236:237], v[30:31]\n"
    "v_lshlrev_b32 v238, 16, v119\n"
    "v_and_b32 v239, s37, v119\n"
    "v_pk_add_f32 v[28:29], v[28:29], v[238:239]\n"
    "v_pk_fma_f32 v[30:31], v[238:239], v[238:239], v[30:31]\n"
    "s_waitcnt vmcnt(19)\n"
    "v_lshlrev_b32 v144, 16, v72\n"
    "v_and_b32 v145, s37, v72\n"
    "v_pk_add_f32 v[16:17], v[16:17], v[144:145]\n"
    "v_pk_fma_f32 v[18:19], v[144:145], v[144:145], v[18:19]\n"
    "v_lshlrev_b32 v146, 16, v73\n"
    "v_and_b32 v147, s37, v73\n"
    "v_pk_add_f32 v[16:17], v[16:17], v[146:147]\n"
    "v_pk_fma_f32 v[18:19], v[146:147], v[146:147], v[18:19]\n"
    "v_lshlrev_b32 v148, 16, v74\n"
    "v_and_b32 v149, s37, v74\n"
    "v_pk_add_f32 v[16:17], v[16:17], v[148:149]\n"
    "v_pk_fma_f32 v[18:19], v[148:149], v[148:149], v[18:19]\n"
    "v_lshlrev_b32 v150, 16, v75\n"
    "v_and_b32 v151, s37, v75\n"
    "v_pk_add_f32 v[16:17], v[16:17], v[150:151]\n"
    "v_pk_fma_f32 v[18:19], v[150:151], v[150:151], v[18:19]\n"
    "s_add_u32 s75, s97, 0x30400\n"
    "s_lshl1_add_u32 m0, s42, 0x9000\n"
    "s_nop 0\n"
    "buffer_load_dwordx4 v36, s[8:11], s75 offen offset:0 lds\n"
    "buffer_load_dwordx4 v36, s[8:11], s75 offen offset:1024 lds\n"
    "buffer_load_dwordx4 v38, s[8:11], s75 offen offset:2048 lds\n"
    "buffer_load_dwordx4 v38, s[8:11], s75 offen offset:3072 lds\n"
    "s_waitcnt vmcnt(22)\n"
    "v_lshlrev_b32 v176, 16, v88\n"
    "v_and_b32 v177, s37, v88\n"
    "v_pk_add_f32 v[20:21], v[20:21], v[176:177]\n"
    "v_pk_fma_f32 v[22:23], v[176:177], v[176:177], v[22:23]\n"
    "v_lshlrev_b32 v178, 16, v89\n"
    "v_and_b32 v179, s37, v89\n"
    "v_pk_add_f32 v[20:21], v[20:21], v[178:179]\n"
    "v_pk_fma_f32 v[22:23], v[178:179], v[178:179], v[22:23]\n"
    "v_lshlrev_b32 v180, 16, v90\n"
    "v_and_b32 v181, s37, v90\n"
    "v_pk_add_f32 v[20:21], v[20:21], v[180:181]\n"
    "v_pk_fma_f32 v[22:23], v[180:181], v[180:181], v[22:23]\n"
    "v_lshlrev_b32 v182, 16, v91\n"
    "v_and_b32 v183, s37, v91\n"
    "v_pk_add_f32 v[20:21], v[20:21], v[182:183]\n"
    "v_pk_fma_f32 v[22:23], v[182:183], v[182:183], v[22:23]\n"
    "s_waitcnt vmcnt(21)\n"
    "v_lshlrev_b32 v208, 16, v104\n"
    "v_and_b32 v209, s37, v104\n"
    "v_pk_add_f32 v[24:25], v[24:25], v[208:209]\n"
    "v_pk_fma_f32 v[26:27], v[208:209], v[208:209], v[26:27]\n"
    "v_lshlrev_b32 v210, 16, v105\n"
    "v_and_b32 v211, s37, v105\n"
    "v_pk_add_f32 v[24:25], v[24:25], v[210:211]\n"
    "v_pk_fma_f32 v[26:27], v[210:211], v[210:211], v[26:27]\n"
    "v_lshlrev_b32 v212, 16, v106\n"
    "v_and_b32 v213, s37, v106\n"
    "v_pk_add_f32 v[24:25], v[24:25], v[212:213]\n"
    "v_pk_fma_f32 v[26:27], v[212:213], v[212:213], v[26:27]\n"
    "v_lshlrev_b32 v214, 16, v107\n"
    "v_and_b32 v215, s37, v107\n"
    "v_pk_add_f32 v[24:25], v[24:25], v[214:215]\n"
    "v_pk_fma_f32 v[26:27], v[214:215], v[214:215], v[26:27]\n"
    "s_waitcnt vmcnt(20)\n"
    "v_lshlrev_b32 v240, 16, v120\n"
    "v_and_b32 v241, s37, v120\n"
    "v_pk_add_f32 v[28:29], v[28:29], v[240:241]\n"
    "v_pk_fma_f32 v[30:31], v[240:241], v[240:241], v[30:31]\n"
    "v_lshlrev_b32 v242, 16, v121\n"
    "v_and_b32 v243, s37, v121\n"
    "v_pk_add_f32 v[28:29], v[28:29], v[242:243]\n"
    "v_pk_fma_f32 v[30:31], v[242:243], v[242:243], v[30:31]\n"
    "v_lshlrev_b32 v244, 16, v122\n"
    "v_and_b32 v245, s37, v122\n"
    "v_pk_add_f32 v[28:29], v[28:29], v[244:245]\n"
    "v_pk_fma_f32 v[30:31], v[244:245], v[244:245], v[30:31]\n"
    "v_lshlrev_b32 v246, 16, v123\n"
    "v_and_b32 v247, s37, v123\n"
    "v_pk_add_f32 v[28:29], v[28:29], v[246:247]\n"
    "v_pk_fma_f32 v[30:31], v[246:247], v[246:247], v[30:31]\n"
    "s_waitcnt vmcnt(19)\n"
    "v_lshlrev_b32 v152, 16, v76\n"
    "v_and_b32 v153, s37, v76\n"
    "v_pk_add_f32 v[16:17], v[16:17], v[152:153]\n"
    "v_pk_fma_f32 v[18:19], v[152:153], v[152:153], v[18:19]\n"
    "v_lshlrev_b32 v154, 16, v77\n"
    "v_and_b32 v155, s37, v77\n"
    "v_pk_add_f32 v[16:17], v[16:17], v[154:155]\n"
    "v_pk_fma_f32 v[18:19], v[154:155], v[154:155], v[18:19]\n"
    "v_lshlrev_b32 v156, 16, v78\n"
    "v_and_b32 v157, s37, v78\n"
    "v_pk_add_f32 v[16:17], v[16:17], v[156:157]\n"
    "v_pk_fma_f32 v[18:19], v[156:157], v[156:157], v[18:19]\n"
    "v_lshlrev_b32 v158, 16, v79\n"
    "v_and_b32 v159, s37, v79\n"
    "v_pk_add_f32 v[16:17], v[16:17], v[158:159]\n"
    "v_pk_fma_f32 v[18:19], v[158:159], v[158:159], v[18:19]\n"
    "s_waitcnt vmcnt(18)\n"
    "v_lshlrev_b32 v184, 16, v92\n"
    "v_and_b32 v185, s37, v92\n"
    "v_pk_add_f32 v[20:21], v[20:21], v[184:185]\n"
    "v_pk_fma_f32 v[22:23], v[184:185], v[184:185], v[22:23]\n"
    "s_add_u32 s75, s98, 0x400\n"
    "s_lshl1_add_u32 m0, s42, 0x10000\n"
    "s_nop 0\n"
    "buffer_load_dwordx4 v36, s[8:11], s75 offen offset:0 lds\n"
    "buffer_load_dwordx4 v36, s[8:11], s75 offen offset:1024 lds\n"
    "buffer_load_dwordx4 v38, s[8:11], s75 offen offset:2048 lds\n"
    "buffer_load_dwordx4 v38, s[8:11], s75 offen offset:3072 lds\n"
    "v_lshlrev_b32 v186, 16, v93\n"
    "v_and_b32 v187, s37, v93\n"
    "v_pk_add_f32 v[20:21], v[20:21], v[186:187]\n"
    "v_pk_fma_f32 v[22:23], v[186:187], v[186:187], v[22:23]\n"
    "v_lshlrev_b32 v188, 16, v94\n"
    "v_and_b32 v189, s37, v94\n"
    "v_pk_add_f32 v[20:21], v[20:21], v[188:189]\n"
    "v_pk_fma_f32 v[22:23], v[188:189], v[188:189], v[22:23]\n"
    "v_lshlrev_b32 v190, 16, v95\n"
    "v_and_b32 v191, s37, v95\n"
    "v_pk_add_f32 v[20:21], v[20:21], v[190:191]\n"
    "v_pk_fma_f32 v[22:23], v[190:191], v[190:191], v[22:23]\n"
    "s_waitcnt vmcnt(21)\n"
    "v_lshlrev_b32 v216, 16, v108\n"
    "v_and_b32 v217, s37, v108\n"
    "v_pk_add_f32 v[24:25], v[24:25], v[216:217]\n"
    "v_pk_fma_f32 v[26:27], v[216:217], v[216:217], v[26:27]\n"
    "v_lshlrev_b32 v218, 16, v109\n"
    "v_and_b32 v219, s37, v109\n"
    "v_pk_add_f32 v[24:25], v[24:25], v[218:219]\n"
    "v_pk_fma_f32 v[26:27], v[218:219], v[218:219], v[26:27]\n"
    "v_lshlrev_b32 v220, 16, v110\n"
    "v_and_b32 v221, s37, v110\n"
    "v_pk_add_f32 v[24:25], v[24:25], v[220:221]\n"
    "v_pk_fma_f32 v[26:27], v[220:221], v[220:221], v[26:27]\n"
    "v_lshlrev_b32 v222, 16, v111\n"
    "v_and_b32 v223, s37, v111\n"
    "v_pk_add_f32 v[24:25], v[24:25], v[222:223]\n"
    "v_pk_fma_f32 v[26:27], v[222:223], v[222:223], v[26:27]\n"
    "s_waitcnt vmcnt(20)\n"
    "v_lshlrev_b32 v248, 16, v124\n"
    "v_and_b32 v249, s37, v124\n"
    "v_pk_add_f32 v[28:29], v[28:29], v[248:249]\n"
    "v_pk_fma_f32 v[30:31], v[248:249], v[248:249], v[30:31]\n"
    "v_lshlrev_b32 v250, 16, v125\n"
    "v_and_b32 v251, s37, v125\n"
    "v_pk_add_f32 v[28:29], v[28:29], v[250:251]\n"
    "v_pk_fma_f32 v[30:31], v[250:251], v[250:251], v[30:31]\n"
    "v_lshlrev_b32 v252, 16, v126\n"
    "v_and_b32 v253, s37, v126\n"
    "v_pk_add_f32 v[28:29], v[28:29], v[252:253]\n"
    "v_pk_fma_f32 v[30:31], v[252:253], v[252:253], v[30:31]\n"
    "v_lshlrev_b32 v254, 16, v127\n"
    "v_and_b32 v255, s37, v127\n"
    "v_pk_add_f32 v[28:29], v[28:29], v[254:255]\n"
    "v_pk_fma_f32 v[30:31], v[254:255], v[254:255], v[30:31]\n"
    "v_add_f32 v16, v16, v17\n"
    "v_add_f32 v17, v18, v19\n"
    "v_add_f32 v20, v20, v21\n"
    "v_add_f32 v21, v22, v23\n"
    "v_add_f32 v24, v24, v25\n"
    "v_add_f32 v25, v26, v27\n"
    "v_add_f32 v28, v28, v29\n"
    "v_add_f32 v29, v30, v31\n"
    "v_add_f32_dpp v16, v16, v16 row_ror:8 row_mask:0xf bank_mask:0xf\n"
    "v_add_f32_dpp v17, v17, v17 row_ror:8 row_mask:0xf bank_mask:0xf\n"
    "v_add_f32_dpp v20, v20, v20 row_ror:8 row_mask:0xf bank_mask:0xf\n"
    "v_add_f32_dpp v21, v21, v21 row_ror:8 row_mask:0xf bank_mask:0xf\n"
    "v_add_f32_dpp v24, v24, v24 row_ror:8 row_mask:0xf bank_mask:0xf\n"
    "v_add_f32_dpp v25, v25, v25 row_ror:8 row_mask:0xf bank_mask:0xf\n"
    "v_add_f32_dpp v28, v28, v28 row_ror:8 row_mask:0xf bank_mask:0xf\n"
    "v_add_f32_dpp v29, v29, v29 row_ror:8 row_mask:0xf bank_mask:0xf\n"
    "v_add_f32_dpp v16, v16, v16 row_ror:4 row_mask:0xf bank_mask:0xf\n"
    "v_add_f32_dpp v17, v17, v17 row_ror:4 row_mask:0xf bank_mask:0xf\n"
    "v_add_f32_dpp v20, v20, v20 row_ror:4 row_mask:0xf bank_mask:0xf\n"
    "v_add_f32_dpp v21, v21, v21 row_ror:4 row_mask:0xf bank_mask:0xf\n"
    "v_add_f32_dpp v24, v24, v24 row_ror:4 row_mask:0xf bank_mask:0xf\n"
    "v_add_f32_dpp v25, v25, v25 row_ror:4 row_mask:0xf bank_mask:0xf\n"
    "s_add_u32 s75, s98, 0x30400\n"
    "s_lshl1_add_u32 m0, s42, 0x11000\n"
    "s_nop 0\n"
    "buffer_load_dwordx4 v36, s[8:11], s75 offen offset:0 lds\n"
    "buffer_load_dwordx4 v36, s[8:11], s75 offen offset:1024 lds\n"
    "buffer_load_dwordx4 v38, s[8:11], s75 offen offset:2048 lds\n"
    "buffer_load_dwordx4 v38, s[8:11], s75 offen offset:3072 lds\n"
    "v_add_f32_dpp v28, v28, v28 row_ror:4 row_mask:0xf bank_mask:0xf\n"
    "v_add_f32_dpp v29, v29, v29 row_ror:4 row_mask:0xf bank_mask:0xf\n"
    "v_add_f32_dpp v16, v16, v16 row_ror:2 row_mask:0xf bank_mask:0xf\n"
    "v_add_f32_dpp v17, v17, v17 row_ror:2 row_mask:0xf bank_mask:0xf\n"
    "v_add_f32_dpp v20, v20, v20 row_ror:2 row_mask:0xf bank_mask:0xf\n"
    "v_add_f32_dpp v21, v21, v21 row_ror:2 row_mask:0xf bank_mask:0xf\n"
    "v_add_f32_dpp v24, v24, v24 row_ror:2 row_mask:0xf bank_mask:0xf\n"
    "v_add_f32_dpp v25, v25, v25 row_ror:2 row_mask:0xf bank_mask:0xf\n"
    "v_add_f32_dpp v28, v28, v28 row_ror:2 row_mask:0xf bank_mask:0xf\n"
    "v_add_f32_dpp v29, v29, v29 row_ror:2 row_mask:0xf bank_mask:0xf\n"
    "v_add_f32_dpp v16, v16, v16 row_ror:1 row_mask:0xf bank_mask:0xf\n"
    "v_add_f32_dpp v17, v17, v17 row_ror:1 row_mask:0xf bank_mask:0xf\n"
    "v_add_f32_dpp v20, v20, v20 row_ror:1 row_mask:0xf bank_mask:0xf\n"
    "v_add_f32_dpp v21, v21, v21 row_ror:1 row_mask:0xf bank_mask:0xf\n"
    "v_add_f32_dpp v24, v24, v24 row_ror:1 row_mask:0xf bank_mask:0xf\n"
    "v_add_f32_dpp v25, v25, v25 row_ror:1 row_mask:0xf bank_mask:0xf\n"
    "v_add_f32_dpp v28, v28, v28 row_ror:1 row_mask:0xf bank_mask:0xf\n"
    "v_add_f32_dpp v29, v29, v29 row_ror:1 row_mask:0xf bank_mask:0xf\n"
    "s_mov_b32 s73, 0xbb000000\n"
    "s_mov_b32 s71, 0x3727c5ac\n"
    "v_mul_f32 v16, s73, v16\n"
    "v_mul_f32 v17, s73, v17\n"
    "v_mul_f32 v20, s73, v20\n"
    "v_mul_f32 v21, s73, v21\n"
    "v_mul_f32 v24, s73, v24\n"
    "v_mul_f32 v25, s73, v25\n"
    "v_mul_f32 v28, s73, v28\n"
    "v_mul_f32 v29, s73, v29\n"
    "v_fmac_f32 v17, v16, v16\n"
    "v_fmac_f32 v21, v20, v20\n"
    "v_fmac_f32 v25, v24, v24\n"
    "v_fmac_f32 v29, v28, v28\n"
    "v_min_f32 v17, 0, v17\n"
    "v_min_f32 v21, 0, v21\n"
    "v_min_f32 v25, 0, v25\n"
    "v_min_f32 v29, 0, v29\n"
    "v_sub_f32 v17, s71, v17\n"
    "v_sub_f32 v21, s71, v21\n"
    "v_sub_f32 v25, s71, v25\n"
    "v_sub_f32 v29, s71, v29\n"
    "v_rsq_f32 v18, v17\n"
    "v_rsq_f32 v22, v21\n"
    "v_rsq_f32 v26, v25\n"
    "v_rsq_f32 v30, v29\n"
    "v_mul_f32 v19, v16, v18\n"
    "v_mul_f32 v23, v20, v22\n"
    "v_mul_f32 v27, v24, v26\n"
    "v_mul_f32 v31, v28, v30\n"
    "s_waitcnt vmcnt(8)\n"
    "s_barrier\n"
    "v_pk_fma_f32 v[128:129], v[128:129], v[18:19], v[18:19] op_sel:[0,0,1] op_sel_hi:[1,0,1]\n"
    "s_add_u32 s75, s99, 0x400\n"
    "s_lshl1_add_u32 m0, s42, 0x18000\n"
    "s_nop 0\n"
    "buffer_load_dwordx4 v36, s[8:11], s75 offen offset:0 lds\n"
    "buffer_load_dwordx4 v36, s[8:11], s75 offen offset:1024 lds\n"
    "buffer_load_dwordx4 v38, s[8:11], s75 offen offset:2048 lds\n"
    "buffer_load_dwordx4 v38, s[8:11], s75 offen offset:3072 lds\n"
    "v_pk_fma_f32 v[130:131], v[130:131], v[18:19], v[18:19] op_sel:[0,0,1] op_sel_hi:[1,0,1]\n"
    "v_pk_fma_f32 v[132:133], v[132:133], v[18:19], v[18:19] op_sel:[0,0,1] op_sel_hi:[1,0,1]\n"
    "v_pk_fma_f32 v[134:135], v[134:135], v[18:19], v[18:19] op_sel:[0,0,1] op_sel_hi:[1,0,1]\n"
    "v_pk_fma_f32 v[136:137], v[136:137], v[18:19], v[18:19] op_sel:[0,0,1] op_sel_hi:[1,0,1]\n"
    "v_pk_fma_f32 v[138:139], v[138:139], v[18:19], v[18:19] op_sel:[0,0,1] op_sel_hi:[1,0,1]\n"
    "ds_read_b64_tr_b16 a[64:65], v37 offset:0\n"
    "ds_read_b64_tr_b16 a[66:67], v37 offset:512\n"
    "ds_read_b64_tr_b16 a[128:129], v37 offset:1024\n"
    "ds_read_b64_tr_b16 a[130:131], v37 offset:1536\n"
    "v_pk_fma_f32 v[140:141], v[140:141], v[18:19], v[18:19] op_sel:[0,0,1] op_sel_hi:[1,0,1]\n"
    "v_pk_fma_f32 v[142:143], v[142:143], v[18:19], v[18:19] op_sel:[0,0,1] op_sel_hi:[1,0,1]\n"
    "v_pk_fma_f32 v[144:145], v[144:145], v[18:19], v[18:19] op_sel:[0,0,1] op_sel_hi:[1,0,1]\n"
    "v_pk_fma_f32 v[146:147], v[146:147], v[18:19], v[18:19] op_sel:[0,0,1] op_sel_hi:[1,0,1]\n"
    "v_pk_fma_f32 v[148:149], v[148:149], v[18:19], v[18:19] op_sel:[0,0,1] op_sel_hi:[1,0,1]\n"
    "ds_read_b64_tr_b16 a[68:69], v37 offset:2048\n"
    "ds_read_b64_tr_b16 a[70:71], v37 offset:2560\n"
    "ds_read_b64_tr_b16 a[132:133], v37 offset:3072\n"
    "ds_read_b64_tr_b16 a[134:135], v37 offset:3584\n"
    "v_pk_fma_f32 v[150:151], v[150:151], v[18:19], v[18:19] op_sel:[0,0,1] op_sel_hi:[1,0,1]\n"
    "v_pk_fma_f32 v[152:153], v[152:153], v[18:19], v[18:19] op_sel:[0,0,1] op_sel_hi:[1,0,1]\n"
    "v_pk_fma_f32 v[154:155], v[154:155], v[18:19], v[18:19] op_sel:[0,0,1] op_sel_hi:[1,0,1]\n"
    "v_pk_fma_f32 v[156:157], v[156:157], v[18:19], v[18:19] op_sel:[0,0,1] op_sel_hi:[1,0,1]\n"
    "s_add_u32 s75, s99, 0x30400\n"
    "s_lshl1_add_u32 m0, s42, 0x19000\n"
    "s_nop 0\n"
    "buffer_load_dwordx4 v36, s[8:11], s75 offen offset:0 lds\n"
    "buffer_load_dwordx4 v36, s[8:11], s75 offen offset:1024 lds\n"
    "buffer_load_dwordx4 v38, s[8:11], s75 offen offset:2048 lds\n"
    "buffer_load_dwordx4 v38, s[8:11], s75 offen offset:3072 lds\n"
    "v_pk_fma_f32 v[158:159], v[158:159], v[18:19], v[18:19] op_sel:[0,0,1] op_sel_hi:[1,0,1]\n"
    "v_pk_fma_f32 v[160:161], v[160:161], v[22:23], v[22:23] op_sel:[0,0,1] op_sel_hi:[1,0,1]\n"
    "ds_read_b64_tr_b16 a[72:73], v37 offset:4096\n"
    "ds_read_b64_tr_b16 a[74:75], v37 offset:4608\n"
    "ds_read_b64_tr_b16 a[136:137], v37 offset:5120\n"
    "ds_read_b64_tr_b16 a[138:139], v37 offset:5632\n"
    "v_pk_fma_f32 v[162:163], v[162:163], v[22:23], v[22:23] op_sel:[0,0,1] op_sel_hi:[1,0,1]\n"
    "v_pk_fma_f32 v[164:165], v[164:165], v[22:23], v[22:23] op_sel:[0,0,1] op_sel_hi:[1,0,1]\n"
    "v_pk_fma_f32 v[166:167], v[166:167], v[22:23], v[22:23] op_sel:[0,0,1] op_sel_hi:[1,0,1]\n"
    "v_pk_fma_f32 v[168:169], v[168:169], v[22:23], v[22:23] op_sel:[0,0,1] op_sel_hi:[1,0,1]\n"
    "v_pk_fma_f32 v[170:171], v[170:171], v[22:23], v[22:23] op_sel:[0,0,1] op_sel_hi:[1,0,1]\n"
    "ds_read_b64_tr_b16 a[76:77], v37 offset:6144\n"
    "ds_read_b64_tr_b16 a[78:79], v37 offset:6656\n"
    "ds_read_b64_tr_b16 a[140:141], v37 offset:7168\n"
    "ds_read_b64_tr_b16 a[142:143], v37 offset:7680\n"
    "v_pk_fma_f32 v[172:173], v[172:173], v[22:23], v[22:23] op_sel:[0,0,1] op_sel_hi:[1,0,1]\n"
    "v_pk_fma_f32 v[174:175], v[174:175], v[22:23], v[22:23] op_sel:[0,0,1] op_sel_hi:[1,0,1]\n"
    "v_pk_fma_f32 v[176:177], v[176:177], v[22:23], v[22:23] op_sel:[0,0,1] op_sel_hi:[1,0,1]\n"
    "v_pk_fma_f32 v[178:179], v[178:179], v[22:23], v[22:23] op_sel:[0,0,1] op_sel_hi:[1,0,1]\n"
    "v_pk_fma_f32 v[180:181], v[180:181], v[22:23], v[22:23] op_sel:[0,0,1] op_sel_hi:[1,0,1]\n"
    "v_pk_fma_f32 v[182:183], v[182:183], v[22:23], v[22:23] op_sel:[0,0,1] op_sel_hi:[1,0,1]\n"
    "ds_read_b64_tr_b16 a[80:81], v37 offset:32768\n"
    "ds_read_b64_tr_b16 a[82:83], v37 offset:33280\n"
    "ds_read_b64_tr_b16 a[144:145], v37 offset:33792\n"
    "ds_read_b64_tr_b16 a[146:147], v37 offset:34304\n"
    "v_pk_fma_f32 v[184:185], v[184:185], v[22:23], v[22:23] op_sel:[0,0,1] op_sel_hi:[1,0,1]\n"
    "v_pk_fma_f32 v[186:187], v[186:187], v[22:23], v[22:23] op_sel:[0,0,1] op_sel_hi:[1,0,1]\n"
    "v_pk_fma_f32 v[188:189], v[188:189], v[22:23], v[22:23] op_sel:[0,0,1] op_sel_hi:[1,0,1]\n"
    "v_pk_fma_f32 v[190:191], v[190:191], v[22:23], v[22:23] op_sel:[0,0,1] op_sel_hi:[1,0,1]\n"
    "v_pk_fma_f32 v[192:193], v[192:193], v[26:27], v[26:27] op_sel:[0,0,1] op_sel_hi:[1,0,1]\n"
    "buffer_load_dwordx4 v[40:43], v0, s[16:19], s66 offen sc1\n"
    "buffer_load_dwordx4 v[44:47], v0, s[16:19], s67 offen sc1\n"
    "buffer_load_dwordx4 v[32:35], v0, s[16:19], s68 offen sc1\n"
    "buffer_load_dwordx4 v[60:63], v0, s[16:19], s69 offen sc1\n"
    "ds_read_b64_tr_b16 a[84:85], v37 offset:34816\n"
    "ds_read_b64_tr_b16 a[86:87], v37 offset:35328\n"
    "ds_read_b64_tr_b16 a[148:149], v37 offset:35840\n"
    "ds_read_b64_tr_b16 a[150:151], v37 offset:36352\n"
    "v_pk_fma_f32 v[194:195], v[194:195], v[26:27], v[26:27] op_sel:[0,0,1] op_sel_hi:[1,0,1]\n"
    "v_pk_fma_f32 v[196:197], v[196:197], v[26:27], v[26:27] op_sel:[0,0,1] op_sel_hi:[1,0,1]\n"
    "v_pk_fma_f32 v[198:199], v[198:199], v[26:27], v[26:27] op_sel:[0,0,1] op_sel_hi:[1,0,1]\n"
    "v_pk_fma_f32 v[200:201], v[200:201], v[26:27], v[26:27] op_sel:[0,0,1] op_sel_hi:[1,0,1]\n"
    "v_pk_fma_f32 v[202:203], v[202:203], v[26:27], v[26:27] op_sel:[0,0,1] op_sel_hi:[1,0,1]\n"
    "v_pk_fma_f32 v[204:205], v[204:205], v[26:27], v[26:27] op_sel:[0,0,1] op_sel_hi:[1,0,1]\n"
    "ds_read_b64_tr_b16 a[88:89], v37 offset:36864\n"
    "ds_read_b64_tr_b16 a[90:91], v37 offset:37376\n"
    "ds_read_b64_tr_b16 a[152:153], v37 offset:37888\n"
    "ds_read_b64_tr_b16 a[154:155], v37 offset:38400\n"
    "v_pk_fma_f32 v[206:207], v[206:207], v[26:27], v[26:27] op_sel:[0,0,1] op_sel_hi:[1,0,1]\n"
    "v_pk_fma_f32 v[208:209], v[208:209], v[26:27], v[26:27] op_sel:[0,0,1] op_sel_hi:[1,0,1]\n"
    "v_pk_fma_f32 v[210:211], v[210:211], v[26:27], v[26:27] op_sel:[0,0,1] op_sel_hi:[1,0,1]\n"
    "v_pk_fma_f32 v[212:213], v[212:213], v[26:27], v[26:27] op_sel:[0,0,1] op_sel_hi:[1,0,1]\n"
    "v_pk_fma_f32 v[214:215], v[214:215], v[26:27], v[26:27] op_sel:[0,0,1] op_sel_hi:[1,0,1]\n"
    "ds_read_b64_tr_b16 a[92:93], v37 offset:38912\n"
    "ds_read_b64_tr_b16 a[94:95], v37 offset:39424\n"
    "ds_read_b64_tr_b16 a[156:157], v37 offset:39936\n"
    "ds_read_b64_tr_b16 a[158:159], v37 offset:40448\n"
    "s_waitcnt vmcnt(12)\n"
    "s_barrier\n"
    "v_pk_fma_f32 v[216:217], v[216:217], v[26:27], v[26:27] op_sel:[0,0,1] op_sel_hi:[1,0,1]\n"
    "v_pk_fma_f32 v[218:219], v[218:219], v[26:27], v[26:27] op_sel:[0,0,1] op_sel_hi:[1,0,1]\n"
    "v_pk_fma_f32 v[220:221], v[220:221], v[26:27], v[26:27] op_sel:[0,0,1] op_sel_hi:[1,0,1]\n"
    "v_pk_fma_f32 v[222:223], v[222:223], v[26:27], v[26:27] op_sel:[0,0,1] op_sel_hi:[1,0,1]\n"
    "v_pk_fma_f32 v[224:225], v[224:225], v[30:31], v[30:31] op_sel:[0,0,1] op_sel_hi:[1,0,1]\n"
    "s_add_u32 m0, s42, 0x20000\n"
    "s_nop 0\n"
    "buffer_load_dwordx4 v52, s[8:11], s96 offen offset:0 lds\n"
    "buffer_load_dwordx4 v53, s[8:11], s96 offen offset:1024 lds\n"
    "buffer_load_dwordx4 v54, s[8:11], s96 offen offset:2048 lds\n"
    "buffer_load_dwordx4 v55, s[8:11], s96 offen offset:3072 lds\n"
    "ds_read_b64_tr_b16 a[96:97], v56 offset:0\n"
    "ds_read_b64_tr_b16 a[98:99], v56 offset:512\n"
    "ds_read_b64_tr_b16 a[160:161], v56 offset:1024\n"
    "ds_read_b64_tr_b16 a[162:163], v56 offset:1536\n"
    "v_pk_fma_f32 v[226:227], v[226:227], v[30:31], v[30:31] op_sel:[0,0,1] op_sel_hi:[1,0,1]\n"
    "v_pk_fma_f32 v[228:229], v[228:229], v[30:31], v[30:31] op_sel:[0,0,1] op_sel_hi:[1,0,1]\n"
    "v_pk_fma_f32 v[230:231], v[230:231], v[30:31], v[30:31] op_sel:[0,0,1] op_sel_hi:[1,0,1]\n"
    "v_pk_fma_f32 v[232:233], v[232:233], v[30:31], v[30:31] op_sel:[0,0,1] op_sel_hi:[1,0,1]\n"
    "v_pk_fma_f32 v[234:235], v[234:235], v[30:31], v[30:31] op_sel:[0,0,1] op_sel_hi:[1,0,1]\n"
    "ds_read_b64_tr_b16 a[100:101], v56 offset:2048\n"
    "ds_read_b64_tr_b16 a[102:103], v56 offset:2560\n"
    "ds_read_b64_tr_b16 a[164:165], v56 offset:3072\n"
    "ds_read_b64_tr_b16 a[166:167], v56 offset:3584\n"
    "v_pk_fma_f32 v[236:237], v[236:237], v[30:31], v[30:31] op_sel:[0,0,1] op_sel_hi:[1,0,1]\n"
    "v_pk_fma_f32 v[238:239], v[238:239], v[30:31], v[30:31] op_sel:[0,0,1] op_sel_hi:[1,0,1]\n"
    "v_pk_fma_f32 v[240:241], v[240:241], v[30:31], v[30:31] op_sel:[0,0,1] op_sel_hi:[1,0,1]\n"
    "v_pk_fma_f32 v[242:243], v[242:243], v[30:31], v[30:31] op_sel:[0,0,1] op_sel_hi:[1,0,1]\n"
    "v_pk_fma_f32 v[244:245], v[244:245], v[30:31], v[30:31] op_sel:[0,0,1] op_sel_hi:[1,0,1]\n"
    "ds_read_b64_tr_b16 a[104:105], v56 offset:4096\n"
    "ds_read_b64_tr_b16 a[106:107], v56 offset:4608\n"
    "ds_read_b64_tr_b16 a[168:169], v56 offset:5120\n"
    "ds_read_b64_tr_b16 a[170:171], v56 offset:5632\n"
    "v_pk_fma_f32 v[246:247], v[246:247], v[30:31], v[30:31] op_sel:[0,0,1] op_sel_hi:[1,0,1]\n"
    "v_pk_fma_f32 v[248:249], v[248:249], v[30:31], v[30:31] op_sel:[0,0,1] op_sel_hi:[1,0,1]\n"
    "v_pk_fma_f32 v[250:251], v[250:251], v[30:31], v[30:31] op_sel:[0,0,1] op_sel_hi:[1,0,1]\n"
    "v_pk_fma_f32 v[252:253], v[252:253], v[30:31], v[30:31] op_sel:[0,0,1] op_sel_hi:[1,0,1]\n"
    "v_pk_fma_f32 v[254:255], v[254:255], v[30:31], v[30:31] op_sel:[0,0,1] op_sel_hi:[1,0,1]\n"
    "ds_read_b64_tr_b16 a[108:109], v56 offset:6144\n"
    "ds_read_b64_tr_b16 a[110:111], v56 offset:6656\n"
    "ds_read_b64_tr_b16 a[172:173], v56 offset:7168\n"
    "ds_read_b64_tr_b16 a[174:175], v56 offset:7680\n"
    "s_waitcnt vmcnt(7)\n"
    "v_xor_b32 v41, s62, v41\n"
    "v_xor_b32 v43, s62, v43\n"
    "s_waitcnt vmcnt(6)\n"
    "v_xor_b32 v45, s62, v45\n"
    "v_xor_b32 v47, s62, v47\n"
    "s_waitcnt vmcnt(5)\n"
    "v_xor_b32 v33, s62, v33\n"
    "v_xor_b32 v35, s62, v35\n"
    "s_waitcnt vmcnt(4)\n"
    "v_xor_b32 v61, s62, v61\n"
    "v_xor_b32 v63, s62, v63\n"
    "v_or3_b32 v50, v41, v43, v45\n"
    "v_or3_b32 v50, v50, v47, v33\n"
    "v_or3_b32 v50, v50, v35, v61\n"
    "v_or_b32 v50, v50, v63\n"
    "v_cmp_ne_u32 vcc, 0, v50\n"
    "s_cbranch_vccnz Lmodr_2\n"
    "Lmodb_3:\n"
    "s_lshl2_add_u32 s72, s2, s52\n"
    "s_mul_i32 s72, s72, 512\n"
    "v_min_u32 v50, 3, v1\n"
    "v_lshlrev_b32 v50, 7, v50\n"
    "buffer_load_dword v49, v50, s[24:27], s72 offen\n"
    "s_lshl2_add_u32 s72, s2, s52\n"
    "s_mul_i32 s72, s72, 1792\n"
    "v_min_u32 v50, 13, v1\n"
    "v_lshlrev_b32 v50, 7, v50\n"
    "buffer_load_dword v49, v50, s[28:31], s72 offen\n"
    "s_lshl2_add_u32 s72, s2, s52\n"
    "s_mul_i32 s72, s72, 1792\n"
    "v_min_u32 v50, 13, v1\n"
    "v_lshlrev_b32 v50, 7, v50\n"
    "buffer_load_dword v49, v50, s[32:35], s72 offen\n"
    "s_lshl2_add_u32 s72, s53, s52\n"
    "s_and_b32 s72, s72, 31\n"
    "s_mul_i32 s72, s72, 0x18000\n"
    "s_and_b32 s50, s53, 24\n"
    "s_add_u32 s50, s50, s65\n"
    "s_lshl_b32 s50, s50, 7\n"
    "s_add_u32 s72, s72, s50\n"
    "v_and_b32 v50, 15, v1\n"
    "v_mul_u32_u24 v50, 0x1800, v50\n"
    "buffer_load_dword v49, v50, s[12:15], s72 offen offset:2048\n"
    "v_lshlrev_b32 v64, 16, v40\n"
    "v_and_b32 v65, s37, v40\n"
    "v_lshlrev_b32 v66, 16, v42\n"
    "v_and_b32 v67, s37, v42\n"
    "v_lshlrev_b32 v68, 16, v44\n"
    "v_and_b32 v69, s37, v44\n"
    "v_lshlrev_b32 v70, 16, v46\n"
    "v_and_b32 v71, s37, v46\n"
    "v_lshlrev_b32 v72, 16, v32\n"
    "v_and_b32 v73, s37, v32\n"
    "v_lshlrev_b32 v74, 16, v34\n"
    "v_and_b32 v75, s37, v34\n"
    "v_lshlrev_b32 v76, 16, v60\n"
    "v_and_b32 v77, s37, v60\n"
    "v_lshlrev_b32 v78, 16, v62\n"
    "v_and_b32 v79, s37, v62\n"
    "v_pk_add_f32 v[64:65], v[64:65], v[68:69]\n"
    "v_pk_add_f32 v[66:67], v[66:67], v[70:71]\n"
    "v_pk_add_f32 v[72:73], v[72:73], v[76:77]\n"
    "v_pk_add_f32 v[74:75], v[74:75], v[78:79]\n"
    "v_pk_add_f32 v[64:65], v[64:65], v[72:73]\n"
    "v_pk_add_f32 v[66:67], v[66:67], v[74:75]\n"
    "v_pk_add_f32 v[64:65], v[64:65], s[70:71] op_sel_hi:[1,0]\n"
    "v_pk_add_f32 v[66:67], v[66:67], s[70:71] op_sel_hi:[1,0]\n"
    "ds_write_b128 v48, v[64:67]\n"
    "s_waitcnt lgkmcnt(0)\n"
    "s_barrier\n"
    "ds_read_b128 v[96:99], v39 offset:2048\n"
    "ds_read_b128 v[64:67], v39 offset:0\n"
    "ds_read_b128 v[100:103], v39 offset:2304\n"
    "ds_read_b128 v[68:71], v39 offset:256\n"
    "ds_read_b128 v[104:107], v39 offset:2560\n"
    "ds_read_b128 v[72:75], v39 offset:512\n"
    "ds_read_b128 v[108:111], v39 offset:2816\n"
    "ds_read_b128 v[76:79], v39 offset:768\n"
    "ds_read_b128 v[112:115], v39 offset:3072\n"
    "ds_read_b128 v[80:83], v39 offset:1024\n"
    "ds_read_b128 v[116:119], v39 offset:3328\n"
    "ds_read_b128 v[84:87], v39 offset:1280\n"
    "ds_read_b128 v[120:123], v39 offset:3584\n"
    "ds_read_b128 v[88:91], v39 offset:1536\n"
    "ds_read_b128 v[124:127], v39 offset:3840\n"
    "ds_read_b128 v[92:95], v39 offset:1792\n"
    "s_waitcnt lgkmcnt(14)\n"
    "v_pk_fma_f32 v[128:129], v[128:129], v[96:97], v[64:65]\n"
    "v_pk_fma_f32 v[130:131], v[130:131], v[98:99], v[66:67]\n"
    "s_waitcnt lgkmcnt(12)\n"
    "v_pk_fma_f32 v[132:133], v[132:133], v[100:101], v[68:69]\n"
    "v_pk_fma_f32 v[134:135], v[134:135], v[102:103], v[70:71]\n"
    "v_pk_fma_f32 v[160:161], v[160:161], v[96:97], v[64:65]\n"
    "v_cvt_pk_bf16_f32 v128, v128, v129\n"
    "v_pk_fma_f32 v[162:163], v[162:163], v[98:99], v[66:67]\n"
    "v_cvt_pk_bf16_f32 v129, v130, v131\n"
    "v_pk_fma_f32 v[164:165], v[164:165], v[100:101], v[68:69]\n"
    "v_cvt_pk_bf16_f32 v130, v132, v133\n"
    "v_pk_fma_f32 v[166:167], v[166:167], v[102:103], v[70:71]\n"
    "v_cvt_pk_bf16_f32 v131, v134, v135\n"
    "ds_write_b128 v5, v[128:131] offset:0\n"
    "v_pk_fma_f32 v[192:193], v[192:193], v[96:97], v[64:65]\n"
    "v_cvt_pk_bf16_f32 v160, v160, v161\n"
    "v_pk_fma_f32 v[194:195], v[194:195], v[98:99], v[66:67]\n"
    "v_cvt_pk_bf16_f32 v161, v162, v163\n"
    "v_pk_fma_f32 v[196:197], v[196:197], v[100:101], v[68:69]\n"
    "v_cvt_pk_bf16_f32 v162, v164, v165\n"
    "v_pk_fma_f32 v[198:199], v[198:199], v[102:103], v[70:71]\n"
    "v_cvt_pk_bf16_f32 v163, v166, v167\n"
    "ds_write_b128 v5, v[160:163] offset:16384\n"
    "v_pk_fma_f32 v[224:225], v[224:225], v[96:97], v[64:65]\n"
    "v_cvt_pk_bf16_f32 v192, v192, v193\n"
    "v_pk_fma_f32 v[226:227], v[226:227], v[98:99], v[66:67]\n"
    "v_cvt_pk_bf16_f32 v193, v194, v195\n"
    "v_pk_fma_f32 v[228:229], v[228:229], v[100:101], v[68:69]\n"
    "v_cvt_pk_bf16_f32 v194, v196, v197\n"
    "v_pk_fma_f32 v[230:231], v[230:231], v[102:103], v[70:71]\n"
    "v_cvt_pk_bf16_f32 v195, v198, v199\n"
    "ds_write_b128 v5, v[192:195] offset:32768\n"
    "s_waitcnt lgkmcnt(13)\n"
    "v_pk_fma_f32 v[136:137], v[136:137], v[104:105], v[72:73]\n"
    "v_cvt_pk_bf16_f32 v224, v224, v225\n"
    "v_pk_fma_f32 v[138:139], v[138:139], v[106:107], v[74:75]\n"
    "v_cvt_pk_bf16_f32 v225, v226, v227\n"
    "s_waitcnt lgkmcnt(11)\n"
    "v_pk_fma_f32 v[140:141], v[140:141], v[108:109], v[76:77]\n"
    "v_cvt_pk_bf16_f32 v226, v228, v229\n"
    "v_pk_fma_f32 v[142:143], v[142:143], v[110:111], v[78:79]\n"
    "v_cvt_pk_bf16_f32 v227, v230, v231\n"
    "ds_write_b128 v5, v[224:227] offset:49152\n"
    "v_pk_fma_f32 v[168:169], v[168:169], v[104:105], v[72:73]\n"
    "v_cvt_pk_bf16_f32 v136, v136, v137\n"
    "v_pk_fma_f32 v[170:171], v[170:171], v[106:107], v[74:75]\n"
    "v_cvt_pk_bf16_f32 v137, v138, v139\n"
    "v_pk_fma_f32 v[172:173], v[172:173], v[108:109], v[76:77]\n"
    "v_cvt_pk_bf16_f32 v138, v140, v141\n"
    "v_pk_fma_f32 v[174:175], v[174:175], v[110:111], v[78:79]\n"
    "v_cvt_pk_bf16_f32 v139, v142, v143\n"
    "ds_write_b128 v5, v[136:139] offset:4096\n"
    "v_pk_fma_f32 v[200:201], v[200:201], v[104:105], v[72:73]\n"
    "v_cvt_pk_bf16_f32 v168, v168, v169\n"
    "v_pk_fma_f32 v[202:203], v[202:203], v[106:107], v[74:75]\n"
    "v_cvt_pk_bf16_f32 v169, v170, v171\n"
    "v_pk_fma_f32 v[204:205], v[204:205], v[108:109], v[76:77]\n"
    "v_cvt_pk_bf16_f32 v170, v172, v173\n"
    "v_pk_fma_f32 v[206:207], v[206:207], v[110:111], v[78:79]\n"
    "v_cvt_pk_bf16_f32 v171, v174, v175\n"
    "ds_write_b128 v5, v[168:171] offset:20480\n"
    "v_pk_fma_f32 v[232:233], v[232:233], v[104:105], v[72:73]\n"
    "v_cvt_pk_bf16_f32 v200, v200, v201\n"
    "v_pk_fma_f32 v[234:235], v[234:235], v[106:107], v[74:75]\n"
    "v_cvt_pk_bf16_f32 v201, v202, v203\n"
    "v_pk_fma_f32 v[236:237], v[236:237], v[108:109], v[76:77]\n"
    "v_cvt_pk_bf16_f32 v202, v204, v205\n"
    "v_pk_fma_f32 v[238:239], v[238:239], v[110:111], v[78:79]\n"
    "v_cvt_pk_bf16_f32 v203, v206, v207\n"
    "ds_write_b128 v5, v[200:203] offset:36864\n"
    "s_waitcnt lgkmcnt(13)\n"
    "v_pk_fma_f32 v[144:145], v[144:145], v[112:113], v[80:81]\n"
    "v_cvt_pk_bf16_f32 v232, v232, v233\n"
    "v_pk_fma_f32 v[146:147], v[146:147], v[114:115], v[82:83]\n"
    "v_cvt_pk_bf16_f32 v233, v234, v235\n"
    "s_waitcnt lgkmcnt(11)\n"
    "v_pk_fma_f32 v[148:149], v[148:149], v[116:117], v[84:85]\n"
    "v_cvt_pk_bf16_f32 v234, v236, v237\n"
    "v_pk_fma_f32 v[150:151], v[150:151], v[118:119], v[86:87]\n"
    "v_cvt_pk_bf16_f32 v235, v238, v239\n"
    "ds_write_b128 v5, v[232:235] offset:53248\n"
    "v_pk_fma_f32 v[176:177], v[176:177], v[112:113], v[80:81]\n"
    "v_cvt_pk_bf16_f32 v144, v144, v145\n"
    "v_pk_fma_f32 v[178:179], v[178:179], v[114:115], v[82:83]\n"
    "v_cvt_pk_bf16_f32 v145, v146, v147\n"
    "v_pk_fma_f32 v[180:181], v[180:181], v[116:117], v[84:85]\n"
    "v_cvt_pk_bf16_f32 v146, v148, v149\n"
    "v_pk_fma_f32 v[182:183], v[182:183], v[118:119], v[86:87]\n"
    "v_cvt_pk_bf16_f32 v147, v150, v151\n"
    "ds_write_b128 v5, v[144:147] offset:8192\n"
    "v_pk_fma_f32 v[208:209], v[208:209], v[112:113], v[80:81]\n"
    "v_cvt_pk_bf16_f32 v176, v176, v177\n"
    "v_pk_fma_f32 v[210:211], v[210:211], v[114:115], v[82:83]\n"
    "v_cvt_pk_bf16_f32 v177, v178, v179\n"
    "v_pk_fma_f32 v[212:213], v[212:213], v[116:117], v[84:85]\n"
    "v_cvt_pk_bf16_f32 v178, v180, v181\n"
    "v_pk_fma_f32 v[214:215], v[214:215], v[118:119], v[86:87]\n"
    "v_cvt_pk_bf16_f32 v179, v182, v183\n"
    "ds_write_b128 v5, v[176:179] offset:24576\n"
    "v_pk_fma_f32 v[240:241], v[240:241], v[112:113], v[80:81]\n"
    "v_cvt_pk_bf16_f32 v208, v208, v209\n"
    "v_pk_fma_f32 v[242:243], v[242:243], v[114:115], v[82:83]\n"
    "v_cvt_pk_bf16_f32 v209, v210, v211\n"
    "v_pk_fma_f32 v[244:245], v[244:245], v[116:117], v[84:85]\n"
    "v_cvt_pk_bf16_f32 v210, v212, v213\n"
    "v_pk_fma_f32 v[246:247], v[246:247], v[118:119], v[86:87]\n"
    "v_cvt_pk_bf16_f32 v211, v214, v215\n"
    "ds_write_b128 v5, v[208:211] offset:40960\n"
    "s_waitcnt lgkmcnt(13)\n"
    "v_pk_fma_f32 v[152:153], v[152:153], v[120:121], v[88:89]\n"
    "v_cvt_pk_bf16_f32 v240, v240, v241\n"
    "v_pk_fma_f32 v[154:155], v[154:155], v[122:123], v[90:91]\n"
    "v_cvt_pk_bf16_f32 v241, v242, v243\n"
    "s_waitcnt lgkmcnt(11)\n"
    "v_pk_fma_f32 v[156:157], v[156:157], v[124:125], v[92:93]\n"
    "v_cvt_pk_bf16_f32 v242, v244, v245\n"
    "v_pk_fma_f32 v[158:159], v[158:159], v[126:127], v[94:95]\n"
    "v_cvt_pk_bf16_f32 v243, v246, v247\n"
    "ds_write_b128 v5, v[240:243] offset:57344\n"
    "v_pk_fma_f32 v[184:185], v[184:185], v[120:121], v[88:89]\n"
    "v_cvt_pk_bf16_f32 v152, v152, v153\n"
    "v_pk_fma_f32 v[186:187], v[186:187], v[122:123], v[90:91]\n"
    "v_cvt_pk_bf16_f32 v153, v154, v155\n"
    "v_pk_fma_f32 v[188:189], v[188:189], v[124:125], v[92:93]\n"
    "v_cvt_pk_bf16_f32 v154, v156, v157\n"
    "v_pk_fma_f32 v[190:191], v[190:191], v[126:127], v[94:95]\n"
    "v_cvt_pk_bf16_f32 v155, v158, v159\n"
    "ds_write_b128 v5, v[152:155] offset:12288\n"
    "v_pk_fma_f32 v[216:217], v[216:217], v[120:121], v[88:89]\n"
    "v_cvt_pk_bf16_f32 v184, v184, v185\n"
    "v_pk_fma_f32 v[218:219], v[218:219], v[122:123], v[90:91]\n"
    "v_cvt_pk_bf16_f32 v185, v186, v187\n"
    "v_pk_fma_f32 v[220:221], v[220:221], v[124:125], v[92:93]\n"
    "v_cvt_pk_bf16_f32 v186, v188, v189\n"
    "v_pk_fma_f32 v[222:223], v[222:223], v[126:127], v[94:95]\n"
    "v_cvt_pk_bf16_f32 v187, v190, v191\n"
    "ds_write_b128 v5, v[184:187] offset:28672\n"
    "v_pk_fma_f32 v[248:249], v[248:249], v[120:121], v[88:89]\n"
    "v_cvt_pk_bf16_f32 v216, v216, v217\n"
    "v_pk_fma_f32 v[250:251], v[250:251], v[122:123], v[90:91]\n"
    "v_cvt_pk_bf16_f32 v217, v218, v219\n"
    "v_pk_fma_f32 v[252:253], v[252:253], v[124:125], v[92:93]\n"
    "v_cvt_pk_bf16_f32 v218, v220, v221\n"
    "v_pk_fma_f32 v[254:255], v[254:255], v[126:127], v[94:95]\n"
    "v_cvt_pk_bf16_f32 v219, v222, v223\n"
    "ds_write_b128 v5, v[216:219] offset:45056\n"
    "v_cvt_pk_bf16_f32 v248, v248, v249\n"
    "v_cvt_pk_bf16_f32 v249, v250, v251\n"
    "v_cvt_pk_bf16_f32 v250, v252, v253\n"
    "v_cvt_pk_bf16_f32 v251, v254, v255\n"
    "ds_write_b128 v5, v[248:251] offset:61440\n"
    "s_waitcnt vmcnt(4)\n"
    "s_waitcnt lgkmcnt(0)\n"
    "s_barrier\n"
    "ds_read_b128 v[96:99], v2 offset:0\n"
    "ds_read_b128 v[100:103], v2 offset:16384\n"
    "ds_read_b128 v[104:107], v3 offset:1024\n"
    "ds_read_b128 v[108:111], v3 offset:17408\n"
    "ds_read_b128 v[112:115], v2 offset:2048\n"
    "ds_read_b128 v[116:119], v2 offset:18432\n"
    "ds_read_b128 v[120:123], v3 offset:3072\n"
    "ds_read_b128 v[124:127], v3 offset:19456\n"
    "ds_read_b64_tr_b16 a[112:113], v56 offset:32768\n"
    "ds_read_b64_tr_b16 a[114:115], v56 offset:33280\n"
    "ds_read_b64_tr_b16 a[176:177], v56 offset:33792\n"
    "ds_read_b64_tr_b16 a[178:179], v56 offset:34304\n"
    "s_waitcnt lgkmcnt(11)\n"
    "v_mfma_f32_16x16x32_bf16 v[16:19], a[64:67], v[96:99], 0\n"
    "s_add_u32 m0, s42, 0x10000\n"
    "s_nop 0\n"
    "buffer_load_dwordx4 v52, s[8:11], s97 offen offset:0 lds\n"
    "v_mfma_f32_16x16x32_bf16 v[32:35], v[96:99], a[128:131], 0\n"
    "ds_read_b128 v[128:131], v2 offset:4096\n"
    "v_accvgpr_read_b32 v252, a192\n"
    "s_waitcnt lgkmcnt(11)\n"
    "v_mfma_f32_16x16x32_bf16 v[20:23], a[64:67], v[100:103], 0\n"
    "ds_read_b64_tr_b16 a[116:117], v56 offset:34816\n"
    "v_accvgpr_read_b32 v14, a193\n"
    "v_mfma_f32_16x16x32_bf16 v[36:39], v[100:103], a[128:131], 0\n"
    "ds_read_b128 v[132:135], v2 offset:20480\n"
    "v_accvgpr_read_b32 v5, a194\n"
    "s_waitcnt lgkmcnt(12)\n"
    "v_mfma_f32_16x16x32_bf16 v[16:19], a[68:71], v[104:107], v[16:19]\n"
    "ds_read_b64_tr_b16 a[118:119], v56 offset:35328\n"
    "v_accvgpr_read_b32 v248, a195\n"
    "v_mfma_f32_16x16x32_bf16 v[32:35], v[104:107], a[132:135], v[32:35]\n"
    "ds_read_b128 v[136:139], v3 offset:5120\n"
    "buffer_load_dwordx4 v53, s[8:11], s97 offen offset:1024 lds\n"
    "s_waitcnt lgkmcnt(13)\n"
    "v_mfma_f32_16x16x32_bf16 v[20:23], a[68:71], v[108:111], v[20:23]\n"
    "ds_read_b64_tr_b16 a[180:181], v56 offset:35840\n"
    "s_lshl_b32 s73, s44, 10\n"
    "v_mfma_f32_16x16x32_bf16 v[36:39], v[108:111], a[132:135], v[36:39]\n"
    "ds_read_b128 v[140:143], v3 offset:21504\n"
    "s_waitcnt lgkmcnt(11)\n"
    "v_mfma_f32_16x16x32_bf16 v[16:19], a[72:75], v[112:115], v[16:19]\n"
    "ds_read_b64_tr_b16 a[182:183], v56 offset:36352\n"
    "s_sub_u32 s73, s73, 1024\n"
    "v_mfma_f32_16x16x32_bf16 v[32:35], v[112:115], a[136:139], v[32:35]\n"
    "ds_read_b128 v[96:99], v2 offset:6144\n"
    "s_lshl_b32 s44, s44, 12\n"
    "v_mfma_f32_16x16x32_bf16 v[20:23], a[72:75], v[116:119], v[20:23]\n"
    "buffer_load_dwordx4 v54, s[8:11], s97 offen offset:2048 lds\n"
    "ds_read_b64_tr_b16 a[120:121], v56 offset:36864\n"
    "v_mfma_f32_16x16x32_bf16 v[36:39], v[116:119], a[136:139], v[36:39]\n"
    "ds_read_b128 v[100:103], v2 offset:22528\n"
    "v_add_u32 v253, s73, v252\n"
    "v_mfma_f32_16x16x32_bf16 v[16:19], a[76:79], v[120:123], v[16:19]\n"
    "ds_read_b64_tr_b16 a[122:123], v56 offset:37376\n"
    "v_add_u32 v254, s73, v253\n"
    "v_mfma_f32_16x16x32_bf16 v[32:35], v[120:123], a[140:143], v[32:35]\n"
    "ds_read_b128 v[104:107], v3 offset:7168\n"
    "v_add_u32 v255, s73, v254\n"
    "v_mfma_f32_16x16x32_bf16 v[20:23], a[76:79], v[124:127], v[20:23]\n"
    "ds_read_b64_tr_b16 a[184:185], v56 offset:37888\n"
    "v_mfma_f32_16x16x32_bf16 v[36:39], v[124:127], a[140:143], v[36:39]\n"
    "ds_read_b128 v[108:111], v3 offset:23552\n"
    "s_waitcnt lgkmcnt(8)\n"
    "v_mfma_f32_16x16x32_bf16 v[16:19], a[80:83], v[128:131], v[16:19]\n"
    "buffer_load_dwordx4 v55, s[8:11], s97 offen offset:3072 lds\n"
    "s_add_u32 m0, s42, 0x14000\n"
    "v_mfma_f32_16x16x32_bf16 v[32:35], v[128:131], a[144:147], v[32:35]\n"
    "ds_read_b128 v[112:115], v2 offset:8192\n"
    "v_mfma_f32_16x16x32_bf16 v[20:23], a[80:83], v[132:135], v[20:23]\n"
    "ds_read_b64_tr_b16 a[186:187], v56 offset:38400\n"
    "s_mul_i32 s73, s52, 0xc0000\n"
    "s_add_u32 s73, s73, 0xc0000\n"
    "v_mfma_f32_16x16x32_bf16 v[36:39], v[132:135], a[144:147], v[36:39]\n"
    "ds_read_b128 v[116:119], v2 offset:24576\n"
    "v_mfma_f32_16x16x32_bf16 v[16:19], a[84:87], v[136:139], v[16:19]\n"
    "ds_read_b64_tr_b16 a[124:125], v56 offset:38912\n"
    "v_mfma_f32_16x16x32_bf16 v[32:35], v[136:139], a[148:151], v[32:35]\n"
    "ds_read_b128 v[120:123], v3 offset:9216\n"
    "buffer_load_dwordx4 v52, s[8:11], s98 offen offset:0 lds\n"
    "v_mfma_f32_16x16x32_bf16 v[20:23], a[84:87], v[140:143], v[20:23]\n"
    "ds_read_b64_tr_b16 a[126:127], v56 offset:39424\n"
    "v_mfma_f32_16x16x32_bf16 v[36:39], v[140:143], a[148:151], v[36:39]\n"
    "ds_read_b128 v[124:127], v3 offset:25600\n"
    "s_waitcnt lgkmcnt(7)\n"
    "v_mfma_f32_16x16x32_bf16 v[16:19], a[88:91], v[96:99], v[16:19]\n"
    "ds_read_b64_tr_b16 a[188:189], v56 offset:39936\n"
    "s_cmp_ge_u32 s53, 28\n"
    "s_cselect_b64 s[28:29], s[12:13], s[28:29]\n"
    "s_cselect_b32 s30, s73, s30\n"
    "v_mfma_f32_16x16x32_bf16 v[32:35], v[96:99], a[152:155], v[32:35]\n"
    "ds_read_b128 v[128:131], v2 offset:10240\n"
    "v_mfma_f32_16x16x32_bf16 v[20:23], a[88:91], v[100:103], v[20:23]\n"
    "buffer_load_dwordx4 v53, s[8:11], s98 offen offset:1024 lds\n"
    "v_mfma_f32_16x16x32_bf16 v[36:39], v[100:103], a[152:155], v[36:39]\n"
    "ds_read_b128 v[132:135], v2 offset:26624\n"
    "v_and_b32 v204, 31, v1\n"
    "v_mfma_f32_16x16x32_bf16 v[16:19], a[92:95], v[104:107], v[16:19]\n"
    "ds_read_b64_tr_b16 a[190:191], v56 offset:40448\n"
    "v_lshrrev_b32 v205, 5, v1\n"
    "v_mfma_f32_16x16x32_bf16 v[32:35], v[104:107], a[156:159], v[32:35]\n"
    "ds_read_b128 v[136:139], v3 offset:11264\n"
    "v_lshlrev_b32 v205, 2, v205\n"
    "s_waitcnt lgkmcnt(1)\n"
    "s_barrier\n"
    "v_mfma_f32_16x16x32_bf16 v[20:23], a[92:95], v[108:111], v[20:23]\n"
    "buffer_load_dwordx4 v54, s[8:11], s98 offen offset:2048 lds\n"
    "ds_read_b64_tr_b16 a[0:1], v58 offset:0\n"
    "v_mfma_f32_16x16x32_bf16 v[36:39], v[108:111], a[156:159], v[36:39]\n"
    "ds_read_b128 v[140:143], v3 offset:27648\n"
    "v_mfma_f32_16x16x32_bf16 v[16:19], a[96:99], v[112:115], v[16:19]\n"
    "ds_read_b64_tr_b16 a[2:3], v58 offset:512\n"
    "v_sub_u32 v204, v204, v205\n"
    "v_mfma_f32_16x16x32_bf16 v[32:35], v[112:115], a[160:163], v[32:35]\n"
    "ds_read_b128 v[96:99], v2 offset:12288\n"
    "v_cvt_f32_i32 v204, v204\n"
    "v_mfma_f32_16x16x32_bf16 v[20:23], a[96:99], v[116:119], v[20:23]\n"
    "ds_read_b64_tr_b16 a[4:5], v58 offset:1024\n"
    "v_mul_f32 v204, 0x6e013f39, v204\n"
    "v_mfma_f32_16x16x32_bf16 v[36:39], v[116:119], a[160:163], v[36:39]\n"
    "ds_read_b128 v[100:103], v2 offset:28672\n"
    "buffer_load_dwordx4 v55, s[8:11], s98 offen offset:3072 lds\n"
    "v_mfma_f32_16x16x32_bf16 v[16:19], a[100:103], v[120:123], v[16:19]\n"
    "s_add_u32 m0, s42, 0x18000\n"
    "ds_read_b64_tr_b16 a[6:7], v58 offset:1536\n"
    "v_mfma_f32_16x16x32_bf16 v[32:35], v[120:123], a[164:167], v[32:35]\n"
    "ds_read_b128 v[104:107], v3 offset:13312\n"
    "v_subrev_f32 v224, 0xed813f39, v204\n"
    "v_mfma_f32_16x16x32_bf16 v[20:23], a[100:103], v[124:127], v[20:23]\n"
    "ds_read_b64_tr_b16 a[8:9], v58 offset:2048\n"
    "v_subrev_f32 v225, 0x6d813f39, v204\n"
    "v_mfma_f32_16x16x32_bf16 v[36:39], v[124:127], a[164:167], v[36:39]\n"
    "ds_read_b128 v[108:111], v3 offset:29696\n"
    "s_waitcnt lgkmcnt(8)\n"
    "v_mfma_f32_16x16x32_bf16 v[16:19], a[104:107], v[128:131], v[16:19]\n"
    "buffer_load_dwordx4 v52, s[8:11], s99 offen offset:0 lds\n"
    "ds_read_b64_tr_b16 a[10:11], v58 offset:2560\n"
    "v_mfma_f32_16x16x32_bf16 v[32:35], v[128:131], a[168:171], v[32:35]\n"
    "ds_read_b128 v[112:115], v2 offset:14336\n"
    "v_subrev_f32 v226, 0x6e41ded6, v204\n"
    "v_mfma_f32_16x16x32_bf16 v[20:23], a[104:107], v[132:135], v[20:23]\n"
    "ds_read_b64_tr_b16 a[12:13], v58 offset:3072\n"
    "v_subrev_f32 v227, 0x6ea18f08, v204\n"
    "v_mfma_f32_16x16x32_bf16 v[36:39], v[132:135], a[168:171], v[36:39]\n"
    "ds_read_b128 v[116:119], v2 offset:30720\n"
    "v_subrev_f32 v228, 0x6f72568c, v204\n"
    "v_mfma_f32_16x16x32_bf16 v[16:19], a[108:111], v[136:139], v[16:19]\n"
    "ds_read_b64_tr_b16 a[14:15], v58 offset:3584\n"
    "v_subrev_f32 v229, 0x6f89532d, v204\n"
    "v_mfma_f32_16x16x32_bf16 v[32:35], v[136:139], a[172:175], v[32:35]\n"
    "ds_read_b128 v[120:123], v3 offset:15360\n"
    "buffer_load_dwordx4 v53, s[8:11], s99 offen offset:1024 lds\n"
    "v_mfma_f32_16x16x32_bf16 v[20:23], a[108:111], v[140:143], v[20:23]\n"
    "v_subrev_f32 v230, 0x6f997b14, v204\n"
    "v_subrev_f32 v231, 0x6fa9a2fb, v204\n"
    "v_mfma_f32_16x16x32_bf16 v[36:39], v[140:143], a[172:175], v[36:39]\n"
    "ds_read_b128 v[124:127], v3 offset:31744\n"
    "s_waitcnt lgkmcnt(7)\n"
    "v_mfma_f32_16x16x32_bf16 v[16:19], a[112:115], v[96:99], v[16:19]\n"
    "v_subrev_f32 v232, 0x6ffa6a7f, v204\n"
    "v_subrev_f32 v233, 0x70054933, v204\n"
    "v_mfma_f32_16x16x32_bf16 v[32:35], v[96:99], a[176:179], v[32:35]\n"
    "ds_read_b128 v[64:67], v2 offset:32768\n"
    "v_subrev_f32 v234, 0x700d5d27, v204\n"
    "v_mfma_f32_16x16x32_bf16 v[20:23], a[112:115], v[100:103], v[20:23]\n"
    "buffer_load_dwordx4 v54, s[8:11], s99 offen offset:2048 lds\n"
    "v_subrev_f32 v235, 0x7015711a, v204\n"
    "v_mfma_f32_16x16x32_bf16 v[36:39], v[100:103], a[176:179], v[36:39]\n"
    "ds_read_b128 v[68:71], v2 offset:49152\n"
    "v_subrev_f32 v236, 0x703dd4dc, v204\n"
    "v_mfma_f32_16x16x32_bf16 v[16:19], a[116:119], v[104:107], v[16:19]\n"
    "v_subrev_f32 v237, 0x7045e8d0, v204\n"
    "v_subrev_f32 v238, 0x704dfcc4, v204\n"
    "v_mfma_f32_16x16x32_bf16 v[32:35], v[104:107], a[180:183], v[32:35]\n"
    "ds_read_b128 v[72:75], v3 offset:33792\n"
    "v_subrev_f32 v239, 0x705610b7, v204\n"
    "v_mfma_f32_16x16x32_bf16 v[20:23], a[116:119], v[108:111], v[20:23]\n"
    "v_lshrrev_b32 v204, 2, v1\n"
    "v_xor_b32 v204, v204, v1\n"
    "v_mfma_f32_16x16x32_bf16 v[36:39], v[108:111], a[180:183], v[36:39]\n"
    "ds_read_b128 v[76:79], v3 offset:50176\n"
    "s_waitcnt lgkmcnt(4)\n"
    "v_mfma_f32_16x16x32_bf16 v[16:19], a[120:123], v[112:115], v[16:19]\n"
    "buffer_load_dwordx4 v55, s[8:11], s99 offen offset:3072 lds\n"
    "s_add_u32 s71, s38, 0\n"
    "v_mfma_f32_16x16x32_bf16 v[32:35], v[112:115], a[184:187], v[32:35]\n"
    "ds_read_b128 v[80:83], v2 offset:34816\n"
    "s_add_u32 m0, s42, 0x1c000\n"
    "v_mfma_f32_16x16x32_bf16 v[20:23], a[120:123], v[116:119], v[20:23]\n"
    "v_bfe_u32 v204, v204, 2, 1\n"
    "v_subrev_u32 v204, 1, v204\n"
    "v_mfma_f32_16x16x32_bf16 v[36:39], v[116:119], a[184:187], v[36:39]\n"
    "ds_read_b128 v[84:87], v2 offset:51200\n"
    "v_and_b32 v212, 0x3f803f80, v204\n"
    "v_mfma_f32_16x16x32_bf16 v[16:19], a[124:127], v[120:123], v[16:19]\n"
    "v_mov_b32 v213, v212\n"
    "v_mov_b32 v214, v212\n"
    "v_mfma_f32_16x16x32_bf16 v[32:35], v[120:123], a[188:191], v[32:35]\n"
    "ds_read_b128 v[88:91], v3 offset:35840\n"
    "buffer_load_dwordx4 v6, s[24:27], s71 offen offset:0 lds\n"
    "v_mfma_f32_16x16x32_bf16 v[20:23], a[124:127], v[124:127], v[20:23]\n"
    "v_mov_b32 v215, v212\n"
    "v_mfma_f32_16x16x32_bf16 v[36:39], v[124:127], a[188:191], v[36:39]\n"
    "ds_read_b128 v[92:95], v3 offset:52224\n"
    "s_waitcnt lgkmcnt(4)\n"
    "v_mfma_f32_16x16x32_bf16 v[24:27], a[64:67], v[64:67], 0\n"
    "v_mfma_f32_16x16x32_bf16 v[40:43], v[64:67], a[128:131], 0\n"
    "ds_read_b128 v[96:99], v2 offset:36864\n"
    "v_mfma_f32_16x16x32_bf16 v[28:31], a[64:67], v[68:71], 0\n"
    "buffer_load_dwordx4 v7, s[24:27], s71 offen offset:1024 lds\n"
    "v_cvt_pk_bf16_f32 v160, v16, v17\n"
    "v_mfma_f32_16x16x32_bf16 v[44:47], v[68:71], a[128:131], 0\n"
    "ds_read_b128 v[100:103], v2 offset:53248\n"
    "v_cvt_pk_bf16_f32 v161, v18, v19\n"
    "v_mfma_f32_16x16x32_bf16 v[24:27], a[68:71], v[72:75], v[24:27]\n"
    "v_cvt_pk_bf16_f32 v162, v20, v21\n"
    "v_cvt_pk_bf16_f32 v163, v22, v23\n"
    "v_mfma_f32_16x16x32_bf16 v[40:43], v[72:75], a[132:135], v[40:43]\n"
    "ds_read_b128 v[104:107], v3 offset:37888\n"
    "v_cvt_pk_bf16_f32 v164, v32, v33\n"
    "s_waitcnt vmcnt(6)\n"
    "s_barrier\n"
    "v_mfma_f32_16x16x32_bf16 v[28:31], a[68:71], v[76:79], v[28:31]\n"
    "buffer_load_dwordx4 v8, s[24:27], s71 offen offset:2048 lds\n"
    "ds_read_b64_tr_b16 a[16:17], v59 offset:0\n"
    "v_mfma_f32_16x16x32_bf16 v[44:47], v[76:79], a[132:135], v[44:47]\n"
    "ds_read_b128 v[108:111], v3 offset:54272\n"
    "s_waitcnt lgkmcnt(5)\n"
    "v_mfma_f32_16x16x32_bf16 v[24:27], a[72:75], v[80:83], v[24:27]\n"
    "ds_read_b64_tr_b16 a[18:19], v59 offset:512\n"
    "v_cvt_pk_bf16_f32 v165, v34, v35\n"
    "v_mfma_f32_16x16x32_bf16 v[40:43], v[80:83], a[136:139], v[40:43]\n"
    "ds_read_b128 v[112:115], v2 offset:38912\n"
    "v_cvt_pk_bf16_f32 v166, v36, v37\n"
    "v_mfma_f32_16x16x32_bf16 v[28:31], a[72:75], v[84:87], v[28:31]\n"
    "ds_read_b64_tr_b16 a[20:21], v59 offset:1024\n"
    "v_cvt_pk_bf16_f32 v167, v38, v39\n"
    "v_mfma_f32_16x16x32_bf16 v[44:47], v[84:87], a[136:139], v[44:47]\n"
    "ds_read_b128 v[116:119], v2 offset:55296\n"
    "buffer_load_dwordx4 v9, s[24:27], s71 offen offset:3072 lds\n"
    "v_mfma_f32_16x16x32_bf16 v[24:27], a[76:79], v[88:91], v[24:27]\n"
    "ds_read_b64_tr_b16 a[22:23], v59 offset:1536\n"
    "v_mfma_f32_16x16x32_bf16 v[40:43], v[88:91], a[140:143], v[40:43]\n"
    "ds_read_b128 v[120:123], v3 offset:39936\n"
    "v_mfma_f32_16x16x32_bf16 v[28:31], a[76:79], v[92:95], v[28:31]\n"
    "ds_read_b64_tr_b16 a[24:25], v59 offset:2048\n"
    "v_permlane16_swap_b32 v160, v162\n"
    "v_mfma_f32_16x16x32_bf16 v[44:47], v[92:95], a[140:143], v[44:47]\n"
    "ds_read_b128 v[124:127], v3 offset:56320\n"
    "s_waitcnt lgkmcnt(8)\n"
    "v_mfma_f32_16x16x32_bf16 v[24:27], a[80:83], v[96:99], v[24:27]\n"
    "ds_read_b64_tr_b16 a[26:27], v59 offset:2560\n"
    "v_mfma_f32_16x16x32_bf16 v[40:43], v[96:99], a[144:147], v[40:43]\n"
    "ds_read_b128 v[128:131], v2 offset:40960\n"
    "v_mfma_f32_16x16x32_bf16 v[28:31], a[80:83], v[100:103], v[28:31]\n"
    "s_add_u32 s75, s55, 0\n"
    "buffer_store_dwordx4 a[196:199], v10, s[16:19], s75 offen sc1\n"
    "v_mfma_f32_16x16x32_bf16 v[44:47], v[100:103], a[144:147], v[44:47]\n"
    "ds_read_b128 v[132:135], v2 offset:57344\n"
    "v_mfma_f32_16x16x32_bf16 v[24:27], a[84:87], v[104:107], v[24:27]\n"
    "ds_read_b64_tr_b16 a[28:29], v59 offset:3072\n"
    "v_mfma_f32_16x16x32_bf16 v[40:43], v[104:107], a[148:151], v[40:43]\n"
    "ds_read_b128 v[136:139], v3 offset:41984\n"
    "v_permlane16_swap_b32 v161, v163\n"
    "v_mfma_f32_16x16x32_bf16 v[28:31], a[84:87], v[108:111], v[28:31]\n"
    "ds_read_b64_tr_b16 a[30:31], v59 offset:3584\n"
    "v_mfma_f32_16x16x32_bf16 v[44:47], v[108:111], a[148:151], v[44:47]\n"
    "ds_read_b128 v[140:143], v3 offset:58368\n"
    "s_waitcnt lgkmcnt(7)\n"
    "v_mfma_f32_16x16x32_bf16 v[24:27], a[88:91], v[112:115], v[24:27]\n"
    "s_add_u32 s75, s55, 32768\n"
    "buffer_store_dwordx4 a[196:199], v10, s[16:19], s75 offen sc1\n"
    "v_mfma_f32_16x16x32_bf16 v[40:43], v[112:115], a[152:155], v[40:43]\n"
    "ds_read_b128 v[144:147], v2 offset:43008\n"
    "v_mfma_f32_16x16x32_bf16 v[28:31], a[88:91], v[116:119], v[28:31]\n"
    "s_add_u32 m0, s42, 0x20000\n"
    "ds_read_b64_tr_b16 a[32:33], v59 offset:16384\n"
    "v_mfma_f32_16x16x32_bf16 v[44:47], v[116:119], a[152:155], v[44:47]\n"
    "ds_read_b128 v[148:151], v2 offset:59392\n"
    "v_mfma_f32_16x16x32_bf16 v[24:27], a[92:95], v[120:123], v[24:27]\n"
    "ds_read_b64_tr_b16 a[34:35], v59 offset:16896\n"
    "v_mfma_f32_16x16x32_bf16 v[40:43], v[120:123], a[156:159], v[40:43]\n"
    "ds_read_b128 v[152:155], v3 offset:44032\n"
    "buffer_load_dwordx4 v252, s[28:31], s43 offen offset:0 lds\n"
    "v_mfma_f32_16x16x32_bf16 v[28:31], a[92:95], v[124:127], v[28:31]\n"
    "ds_read_b64_tr_b16 a[36:37], v59 offset:17408\n"
    "v_permlane16_swap_b32 v164, v166\n"
    "v_mfma_f32_16x16x32_bf16 v[44:47], v[124:127], a[156:159], v[44:47]\n"
    "ds_read_b128 v[156:159], v3 offset:60416\n"
    "s_waitcnt lgkmcnt(7)\n"
    "v_mfma_f32_16x16x32_bf16 v[24:27], a[96:99], v[128:131], v[24:27]\n"
    "ds_read_b64_tr_b16 a[38:39], v59 offset:17920\n"
    "v_mfma_f32_16x16x32_bf16 v[40:43], v[128:131], a[160:163], v[40:43]\n"
    "ds_read_b128 v[176:179], v2 offset:45056\n"
    "v_mfma_f32_16x16x32_bf16 v[28:31], a[96:99], v[132:135], v[28:31]\n"
    "buffer_load_dwordx4 v253, s[28:31], s43 offen offset:1024 lds\n"
    "ds_read_b64_tr_b16 a[40:41], v59 offset:18432\n"
    "v_mfma_f32_16x16x32_bf16 v[44:47], v[132:135], a[160:163], v[44:47]\n"
    "ds_read_b128 v[180:183], v2 offset:61440\n"
    "v_mfma_f32_16x16x32_bf16 v[24:27], a[100:103], v[136:139], v[24:27]\n"
    "ds_read_b64_tr_b16 a[42:43], v59 offset:18944\n"
    "v_permlane16_swap_b32 v165, v167\n"
    "v_mfma_f32_16x16x32_bf16 v[40:43], v[136:139], a[164:167], v[40:43]\n"
    "ds_read_b128 v[184:187], v3 offset:46080\n"
    "v_mfma_f32_16x16x32_bf16 v[28:31], a[100:103], v[140:143], v[28:31]\n"
    "ds_read_b64_tr_b16 a[44:45], v59 offset:19456\n"
    "v_mfma_f32_16x16x32_bf16 v[44:47], v[140:143], a[164:167], v[44:47]\n"
    "ds_read_b128 v[188:191], v3 offset:62464\n"
    "s_waitcnt lgkmcnt(8)\n"
    "v_mfma_f32_16x16x32_bf16 v[24:27], a[104:107], v[144:147], v[24:27]\n"
    "buffer_load_dwordx4 v254, s[28:31], s43 offen offset:2048 lds\n"
    "ds_read_b64_tr_b16 a[46:47], v59 offset:19968\n"
    "v_mfma_f32_16x16x32_bf16 v[40:43], v[144:147], a[168:171], v[40:43]\n"
    "ds_read_b128 v[192:195], v2 offset:47104\n"
    "v_mfma_f32_16x16x32_bf16 v[28:31], a[104:107], v[148:151], v[28:31]\n"
    "buffer_store_dwordx4 v[160:163], v10, s[16:19], s56 offen offset:0 sc1\n"
    "v_mfma_f32_16x16x32_bf16 v[44:47], v[148:151], a[168:171], v[44:47]\n"
    "ds_read_b128 v[196:199], v2 offset:63488\n"
    "s_waitcnt vmcnt(10) lgkmcnt(2)\n"
    "s_barrier\n"
    "v_mfma_f32_16x16x32_bf16 v[24:27], a[108:111], v[152:155], v[24:27]\n"
    "buffer_load_dwordx4 v255, s[28:31], s43 offen offset:3072 lds\n"
    "s_add_u32 s43, s43, s44\n"
    "v_mfma_f32_16x16x32_bf16 v[40:43], v[152:155], a[172:175], v[40:43]\n"
    "ds_read_b128 v[216:219], v3 offset:48128\n"
    "s_add_u32 s71, s38, 32768\n"
    "v_mfma_f32_16x16x32_bf16 v[28:31], a[108:111], v[156:159], v[28:31]\n"
    "s_add_u32 m0, s42, 0x10000\n"
    "ds_read_b64_tr_b16 a[48:49], v59 offset:32768\n"
    "v_mfma_f32_16x16x32_bf16 v[44:47], v[156:159], a[172:175], v[44:47]\n"
    "ds_read_b128 v[220:223], v3 offset:64512\n"
    "v_mfma_f32_16x16x32_bf16 v[24:27], a[112:115], v[176:179], v[24:27]\n"
    "ds_read_b64_tr_b16 a[50:51], v59 offset:33280\n"
    "v_mfma_f32_16x16x32_bf16 v[40:43], v[176:179], a[176:179], v[40:43]\n"
    "buffer_load_dwordx4 v6, s[24:27], s71 offen offset:0 lds\n"
    "ds_read_b64_tr_b16 a[52:53], v59 offset:33792\n"
    "v_mfma_f32_16x16x32_bf16 v[28:31], a[112:115], v[180:183], v[28:31]\n"
    "ds_read_b64_tr_b16 a[54:55], v59 offset:34304\n"
    "v_mfma_f32_16x16x32_bf16 v[44:47], v[180:183], a[176:179], v[44:47]\n"
    "ds_read_b64_tr_b16 a[56:57], v59 offset:34816\n"
    "v_permlane32_swap_b32 v164, v166\n"
    "v_mfma_f32_16x16x32_bf16 v[24:27], a[116:119], v[184:187], v[24:27]\n"
    "ds_read_b64_tr_b16 a[58:59], v59 offset:35328\n"
    "v_mfma_f32_16x16x32_bf16 v[40:43], v[184:187], a[180:183], v[40:43]\n"
    "ds_read_b64_tr_b16 a[60:61], v59 offset:35840\n"
    "v_mfma_f32_16x16x32_bf16 v[28:31], a[116:119], v[188:191], v[28:31]\n"
    "buffer_load_dwordx4 v7, s[24:27], s71 offen offset:1024 lds\n"
    "ds_read_b64_tr_b16 a[62:63], v59 offset:36352\n"
    "v_mfma_f32_16x16x32_bf16 v[44:47], v[188:191], a[180:183], v[44:47]\n"
    "s_waitcnt lgkmcnt(7)\n"
    "v_mfma_f32_16x16x32_bf16 v[24:27], a[120:123], v[192:195], v[24:27]\n"
    "v_permlane32_swap_b32 v165, v167\n"
    "v_mfma_f32_16x16x32_bf16 v[40:43], v[192:195], a[184:187], v[40:43]\n"
    "buffer_store_dwordx4 v[164:167], v10, s[16:19], s56 offen offset:1024 sc1\n"
    "v_mfma_f32_16x16x32_bf16 v[28:31], a[120:123], v[196:199], v[28:31]\n"
    "v_mfma_f32_16x16x32_bf16 v[44:47], v[196:199], a[184:187], v[44:47]\n"
    "v_mfma_f32_16x16x32_bf16 v[24:27], a[124:127], v[216:219], v[24:27]\n"
    "v_mfma_f32_16x16x32_bf16 v[40:43], v[216:219], a[188:191], v[40:43]\n"
    "v_mfma_f32_16x16x32_bf16 v[28:31], a[124:127], v[220:223], v[28:31]\n"
    "v_mfma_f32_16x16x32_bf16 v[44:47], v[220:223], a[188:191], v[44:47]\n"
    "v_mfma_f32_16x16x32_bf16 v[56:59], a[0:3], v[64:67], 0\n"
    "ds_read_b128 v[64:67], v2 offset:0\n"
    "v_mfma_f32_16x16x32_bf16 v[60:63], a[0:3], v[68:71], 0\n"
    "ds_read_b128 v[68:71], v2 offset:16384\n"
    "v_mfma_f32_16x16x32_bf16 v[56:59], a[4:7], v[72:75], v[56:59]\n"
    "v_cvt_pk_bf16_f32 v168, v24, v25\n"
    "v_cvt_pk_bf16_f32 v169, v26, v27\n"
    "v_cvt_pk_bf16_f32 v170, v28, v29\n"
    "v_cvt_pk_bf16_f32 v171, v30, v31\n"
    "v_cvt_pk_bf16_f32 v172, v40, v41\n"
    "v_cvt_pk_bf16_f32 v173, v42, v43\n"
    "v_cvt_pk_bf16_f32 v174, v44, v45\n"
    "v_cvt_pk_bf16_f32 v175, v46, v47\n"
    "v_permlane16_swap_b32 v168, v170\n"
    "v_permlane16_swap_b32 v169, v171\n"
    "v_permlane16_swap_b32 v172, v174\n"
    "v_permlane16_swap_b32 v173, v175\n"
    "buffer_store_dwordx4 v[168:171], v10, s[16:19], s56 offen offset:2048 sc1\n"
    "v_permlane32_swap_b32 v172, v174\n"
    "v_permlane32_swap_b32 v173, v175\n"
    "buffer_store_dwordx4 v[172:175], v10, s[16:19], s56 offen offset:3072 sc1\n"
    "s_waitcnt lgkmcnt(2)\n"
    "s_barrier\n"
    "v_mfma_f32_16x16x32_bf16 v[60:63], a[4:7], v[76:79], v[60:63]\n"
    "ds_read_b128 v[72:75], v3 offset:1024\n"
    "buffer_load_dwordx4 v8, s[24:27], s71 offen offset:2048 lds\n"
    "v_mfma_f32_16x16x32_bf16 v[56:59], a[8:11], v[80:83], v[56:59]\n"
    "ds_read_b128 v[76:79], v3 offset:17408\n"
    "v_min_f32 v16, 0, v224\n"
    "v_mfma_f32_16x16x32_bf16 v[60:63], a[8:11], v[84:87], v[60:63]\n"
    "v_min_f32 v17, 0, v225\n"
    "v_min_f32 v18, 0, v226\n"
    "v_mfma_f32_16x16x32_bf16 v[56:59], a[12:15], v[88:91], v[56:59]\n"
    "ds_read_b128 v[80:83], v2 offset:2048\n"
    "v_min_f32 v19, 0, v227\n"
    "v_mfma_f32_16x16x32_bf16 v[60:63], a[12:15], v[92:95], v[60:63]\n"
    "v_min_f32 v20, 0, v228\n"
    "v_min_f32 v21, 0, v229\n"
    "v_mfma_f32_16x16x32_bf16 v[56:59], a[16:19], v[96:99], v[56:59]\n"
    "ds_read_b128 v[84:87], v2 offset:18432\n"
    "buffer_load_dwordx4 v9, s[24:27], s71 offen offset:3072 lds\n"
    "v_mfma_f32_16x16x32_bf16 v[60:63], a[16:19], v[100:103], v[60:63]\n"
    "ds_read_b128 v[88:91], v3 offset:3072\n"
    "s_add_u32 s71, s38, 65536\n"
    "v_mfma_f32_16x16x32_bf16 v[56:59], a[20:23], v[104:107], v[56:59]\n"
    "s_add_u32 m0, s42, 0x14000\n"
    "v_min_f32 v22, 0, v230\n"
    "v_mfma_f32_16x16x32_bf16 v[60:63], a[20:23], v[108:111], v[60:63]\n"
    "ds_read_b128 v[92:95], v3 offset:19456\n"
    "v_min_f32 v23, 0, v231\n"
    "v_mfma_f32_16x16x32_bf16 v[56:59], a[24:27], v[112:115], v[56:59]\n"
    "ds_read_b128 v[96:99], v2 offset:4096\n"
    "v_min_f32 v24, 0, v232\n"
    "v_mfma_f32_16x16x32_bf16 v[60:63], a[24:27], v[116:119], v[60:63]\n"
    "buffer_load_dwordx4 v6, s[24:27], s71 offen offset:0 lds\n"
    "v_min_f32 v25, 0, v233\n"
    "v_mfma_f32_16x16x32_bf16 v[56:59], a[28:31], v[120:123], v[56:59]\n"
    "ds_read_b128 v[100:103], v2 offset:20480\n"
    "v_min_f32 v26, 0, v234\n"
    "v_mfma_f32_16x16x32_bf16 v[60:63], a[28:31], v[124:127], v[60:63]\n"
    "v_mov_b32 v200, s62\n"
    "v_mov_b32 v201, s63\n"
    "v_mfma_f32_16x16x32_bf16 v[56:59], a[32:35], v[128:131], v[56:59]\n"
    "ds_read_b128 v[104:107], v3 offset:5120\n"
    "v_min_f32 v27, 0, v235\n"
    "v_mfma_f32_16x16x32_bf16 v[60:63], a[32:35], v[132:135], v[60:63]\n"
    "ds_read_b128 v[108:111], v3 offset:21504\n"
    "v_min_f32 v28, 0, v236\n"
    "v_mfma_f32_16x16x32_bf16 v[56:59], a[36:39], v[136:139], v[56:59]\n"
    "buffer_load_dwordx4 v7, s[24:27], s71 offen offset:1024 lds\n"
    "s_waitcnt vmcnt(4)\n"
    "s_mov_b64 exec, 1\n"
    "buffer_store_dwordx2 v[200:201], off, s[16:19], s60 sc1\n"
    "buffer_store_dwordx2 v[200:201], off, s[16:19], s61 sc1\n"
    "s_mov_b64 exec, -1\n"
    "v_mfma_f32_16x16x32_bf16 v[60:63], a[36:39], v[140:143], v[60:63]\n"
    "ds_read_b128 v[112:115], v2 offset:6144\n"
    "v_mfma_f32_16x16x32_bf16 v[56:59], a[40:43], v[144:147], v[56:59]\n"
    "ds_read_b128 v[116:119], v2 offset:22528\n"
    "v_mfma_f32_16x16x32_bf16 v[60:63], a[40:43], v[148:151], v[60:63]\n"
    "v_mfma_f32_16x16x32_bf16 v[56:59], a[44:47], v[152:155], v[56:59]\n"
    "ds_read_b128 v[120:123], v3 offset:7168\n"
    "v_mfma_f32_16x16x32_bf16 v[60:63], a[44:47], v[156:159], v[60:63]\n"
    "v_min_f32 v29, 0, v237\n"
    "v_mfma_f32_16x16x32_bf16 v[56:59], a[48:51], v[176:179], v[56:59]\n"
    "ds_read_b128 v[124:127], v3 offset:23552\n"
    "v_min_f32 v30, 0, v238\n"
    "v_mfma_f32_16x16x32_bf16 v[60:63], a[48:51], v[180:183], v[60:63]\n"
    "ds_read_b128 v[128:131], v2 offset:8192\n"
    "v_min_f32 v31, 0, v239\n"
    "v_mfma_f32_16x16x32_bf16 v[56:59], a[52:55], v[184:187], v[56:59]\n"
    "s_mov_b32 s72, 0x3eb8aa3b\n"
    "s_mov_b32 s73, s72\n"
    "v_mfma_f32_16x16x32_bf16 v[60:63], a[52:55], v[188:191], v[60:63]\n"
    "ds_read_b128 v[132:135], v2 offset:24576\n"
    "v_mfma_f32_16x16x32_bf16 v[56:59], a[56:59], v[192:195], v[56:59]\n"
    "ds_read_b128 v[136:139], v3 offset:9216\n"
    "buffer_load_dwordx4 v8, s[24:27], s71 offen offset:2048 lds\n"
    "v_mfma_f32_16x16x32_bf16 v[60:63], a[56:59], v[196:199], v[60:63]\n"
    "v_mfma_f32_16x16x32_bf16 v[56:59], a[60:63], v[216:219], v[56:59]\n"
    "ds_read_b128 v[140:143], v3 offset:25600\n"
    "v_mfma_f32_16x16x32_bf16 v[60:63], a[60:63], v[220:223], v[60:63]\n"
    "s_waitcnt lgkmcnt(14)\n"
    "v_mfma_f32_16x16x32_bf16 v[48:51], a[0:3], v[64:67], 0\n"
    "ds_read_b128 v[144:147], v2 offset:10240\n"
    "v_mfma_f32_16x16x32_bf16 v[52:55], a[0:3], v[68:71], 0\n"
    "ds_read_b128 v[148:151], v2 offset:26624\n"
    "buffer_load_dwordx4 v9, s[24:27], s71 offen offset:3072 lds\n"
    "v_mfma_f32_16x16x32_bf16 v[48:51], a[4:7], v[72:75], v[48:51]\n"
    "s_add_u32 s71, s38, 98304\n"
    "s_add_u32 m0, s42, 0x18000\n"
    "v_mfma_f32_16x16x32_bf16 v[52:55], a[4:7], v[76:79], v[52:55]\n"
    "ds_read_b128 v[152:155], v3 offset:11264\n"
    "s_waitcnt lgkmcnt(14)\n"
    "v_mfma_f32_16x16x32_bf16 v[48:51], a[8:11], v[80:83], v[48:51]\n"
    "ds_read_b128 v[156:159], v3 offset:27648\n"
    "v_mul_f32 v56, s72, v56\n"
    "v_mfma_f32_16x16x32_bf16 v[52:55], a[8:11], v[84:87], v[52:55]\n"
    "v_mul_f32 v57, s72, v57\n"
    "v_mul_f32 v58, s72, v58\n"
    "v_mfma_f32_16x16x32_bf16 v[48:51], a[12:15], v[88:91], v[48:51]\n"
    "ds_read_b128 v[176:179], v2 offset:12288\n"
    "buffer_load_dwordx4 v6, s[24:27], s71 offen offset:0 lds\n"
    "v_mfma_f32_16x16x32_bf16 v[52:55], a[12:15], v[92:95], v[52:55]\n"
    "s_waitcnt lgkmcnt(13)\n"
    "v_mul_f32 v59, s72, v59\n"
    "v_mfma_f32_16x16x32_bf16 v[48:51], a[16:19], v[96:99], v[48:51]\n"
    "ds_read_b128 v[180:183], v2 offset:28672\n"
    "v_mul_f32 v60, s72, v60\n"
    "v_mfma_f32_16x16x32_bf16 v[52:55], a[16:19], v[100:103], v[52:55]\n"
    "ds_read_b128 v[184:187], v3 offset:13312\n"
    "v_mul_f32 v61, s72, v61\n"
    "v_mfma_f32_16x16x32_bf16 v[48:51], a[20:23], v[104:107], v[48:51]\n"
    "v_mul_f32 v62, s72, v62\n"
    "v_mul_f32 v63, s72, v63\n"
    "v_mfma_f32_16x16x32_bf16 v[52:55], a[20:23], v[108:111], v[52:55]\n"
    "ds_read_b128 v[188:191], v3 offset:29696\n"
    "s_waitcnt lgkmcnt(12)\n"
    "v_mfma_f32_16x16x32_bf16 v[48:51], a[24:27], v[112:115], v[48:51]\n"
    "ds_read_b128 v[192:195], v2 offset:14336\n"
    "buffer_load_dwordx4 v7, s[24:27], s71 offen offset:1024 lds\n"
    "v_mfma_f32_16x16x32_bf16 v[52:55], a[24:27], v[116:119], v[52:55]\n"
    "v_mfma_f32_16x16x32_bf16 v[48:51], a[28:31], v[120:123], v[48:51]\n"
    "ds_read_b128 v[196:199], v2 offset:30720\n"
    "v_mfma_f32_16x16x32_bf16 v[52:55], a[28:31], v[124:127], v[52:55]\n"
    "s_waitcnt lgkmcnt(10)\n"
    "v_mfma_f32_16x16x32_bf16 v[48:51], a[32:35], v[128:131], v[48:51]\n"
    "ds_read_b128 v[216:219], v3 offset:15360\n"
    "v_mfma_f32_16x16x32_bf16 v[52:55], a[32:35], v[132:135], v[52:55]\n"
    "ds_read_b128 v[220:223], v3 offset:31744\n"
    "buffer_load_dwordx4 v8, s[24:27], s71 offen offset:2048 lds\n"
    "v_mfma_f32_16x16x32_bf16 v[48:51], a[36:39], v[136:139], v[48:51]\n"
    "v_mfma_f32_16x16x32_bf16 v[52:55], a[36:39], v[140:143], v[52:55]\n"
    "s_waitcnt lgkmcnt(8)\n"
    "v_mfma_f32_16x16x32_bf16 v[48:51], a[40:43], v[144:147], v[48:51]\n"
    "v_mfma_f32_16x16x32_bf16 v[52:55], a[40:43], v[148:151], v[52:55]\n"
    "v_mfma_f32_16x16x32_bf16 v[48:51], a[44:47], v[152:155], v[48:51]\n"
    "buffer_load_dwordx4 v9, s[24:27], s71 offen offset:3072 lds\n"
    "s_add_u32 m0, s42, 0x8000\n"
    "v_mfma_f32_16x16x32_bf16 v[52:55], a[44:47], v[156:159], v[52:55]\n"
    "s_waitcnt lgkmcnt(4)\n"
    "v_mfma_f32_16x16x32_bf16 v[48:51], a[48:51], v[176:179], v[48:51]\n"
    "v_mfma_f32_16x16x32_bf16 v[52:55], a[48:51], v[180:183], v[52:55]\n"
    "v_mfma_f32_16x16x32_bf16 v[48:51], a[52:55], v[184:187], v[48:51]\n"
    "v_mfma_f32_16x16x32_bf16 v[52:55], a[52:55], v[188:191], v[52:55]\n"
    "s_waitcnt lgkmcnt(0)\n"
    "buffer_load_dwordx4 v252, s[28:31], s43 offen offset:0 lds\n"
    "v_mfma_f32_16x16x32_bf16 v[48:51], a[56:59], v[192:195], v[48:51]\n"
    "v_mfma_f32_16x16x32_bf16 v[52:55], a[56:59], v[196:199], v[52:55]\n"
    "v_mfma_f32_16x16x32_bf16 v[48:51], a[60:63], v[216:219], v[48:51]\n"
    "v_mfma_f32_16x16x32_bf16 v[52:55], a[60:63], v[220:223], v[52:55]\n"
    "v_cvt_pk_bf16_f32 v180, v56, v57\n"
    "buffer_load_dwordx4 v253, s[28:31], s43 offen offset:1024 lds\n"
    "buffer_load_dwordx4 v254, s[28:31], s43 offen offset:2048 lds\n"
    "v_cvt_pk_bf16_f32 v181, v58, v59\n"
    "v_cvt_pk_bf16_f32 v182, v60, v61\n"
    "v_cvt_pk_bf16_f32 v183, v62, v63\n"
    "s_nop 0\n"
    "v_permlane16_swap_b32 v180, v182\n"
    "v_permlane16_swap_b32 v181, v183\n"
    "v_pk_mul_f32 v[48:49], v[48:49], s[72:73]\n"
    "v_pk_mul_f32 v[50:51], v[50:51], s[72:73]\n"
    "v_pk_mul_f32 v[52:53], v[52:53], s[72:73]\n"
    "v_pk_mul_f32 v[54:55], v[54:55], s[72:73]\n"
    "v_cvt_pk_bf16_f32 v176, v48, v49\n"
    "v_cvt_pk_bf16_f32 v177, v50, v51\n"
    "v_cvt_pk_bf16_f32 v178, v52, v53\n"
    "v_cvt_pk_bf16_f32 v179, v54, v55\n"
    "v_mfma_f32_32x32x16_bf16 v[32:47], v[160:163], v[180:183], 0\n"
    "buffer_load_dwordx4 v255, s[28:31], s43 offen offset:3072 lds\n"
    "s_add_u32 s43, s43, s44\n"
    "s_add_u32 m0, s42, 0xc000\n"
    "v_permlane16_swap_b32 v176, v178\n"
    "v_permlane16_swap_b32 v177, v179\n"
    "s_barrier\n"
    "v_mfma_f32_32x32x16_bf16 v[48:63], v[168:171], v[180:183], v[16:31]\n"
    "s_load_dwordx4 s[8:11], s[0:1], 128\n"
    "s_load_dwordx4 s[12:15], s[0:1], 144\n"
    "v_mfma_f32_32x32x16_bf16 v[16:31], v[160:163], v[176:179], v[16:31]\n"
    "s_nop 1\n"
    "v_max_f32 v203, v32, v33\n"
    "v_max3_f32 v203, v203, v34, v35\n"
    "v_max3_f32 v203, v203, v36, v37\n"
    "v_max3_f32 v203, v203, v38, v39\n"
    "v_max3_f32 v203, v203, v40, v41\n"
    "v_max3_f32 v203, v203, v42, v43\n"
    "v_max3_f32 v203, v203, v44, v45\n"
    "v_max3_f32 v203, v203, v46, v47\n"
    "v_max3_f32 v203, v203, v48, v49\n"
    "v_max3_f32 v203, v203, v50, v51\n"
    "v_max3_f32 v203, v203, v52, v53\n"
    "v_max3_f32 v203, v203, v54, v55\n"
    "v_max3_f32 v203, v203, v56, v57\n"
    "v_max3_f32 v203, v203, v58, v59\n"
    "v_max3_f32 v203, v203, v60, v61\n"
    "v_max3_f32 v203, v203, v62, v63\n"
    "v_max_f32 v202, v16, v17\n"
    "v_max3_f32 v202, v202, v18, v19\n"
    "v_max3_f32 v202, v202, v20, v21\n"
    "v_max3_f32 v202, v202, v22, v23\n"
    "v_max3_f32 v202, v202, v24, v25\n"
    "v_max3_f32 v202, v202, v26, v27\n"
    "v_max3_f32 v202, v202, v28, v29\n"
    "v_max3_f32 v202, v202, v30, v31\n"
    "v_mov_b32 v216, v202\n"
    "v_mov_b32 v217, v203\n"
    "s_nop 0\n"
    "v_permlane32_swap_b32 v202, v216\n"
    "v_permlane32_swap_b32 v203, v217\n"
    "v_max_f32 v202, v202, v216\n"
    "v_max_f32 v203, v203, v217\n"
    "v_pk_add_f32 v[16:17], v[16:17], v[202:203] op_sel:[0,0] op_sel_hi:[1,0] neg_lo:[0,1] neg_hi:[0,1]\n"
    "v_pk_add_f32 v[18:19], v[18:19], v[202:203] op_sel:[0,0] op_sel_hi:[1,0] neg_lo:[0,1] neg_hi:[0,1]\n"
    "v_pk_add_f32 v[20:21], v[20:21], v[202:203] op_sel:[0,0] op_sel_hi:[1,0] neg_lo:[0,1] neg_hi:[0,1]\n"
    "v_pk_add_f32 v[22:23], v[22:23], v[202:203] op_sel:[0,0] op_sel_hi:[1,0] neg_lo:[0,1] neg_hi:[0,1]\n"
    "v_pk_add_f32 v[24:25], v[24:25], v[202:203] op_sel:[0,0] op_sel_hi:[1,0] neg_lo:[0,1] neg_hi:[0,1]\n"
    "v_pk_add_f32 v[26:27], v[26:27], v[202:203] op_sel:[0,0] op_sel_hi:[1,0] neg_lo:[0,1] neg_hi:[0,1]\n"
    "v_pk_add_f32 v[28:29], v[28:29], v[202:203] op_sel:[0,0] op_sel_hi:[1,0] neg_lo:[0,1] neg_hi:[0,1]\n"
    "v_pk_add_f32 v[30:31], v[30:31], v[202:203] op_sel:[0,0] op_sel_hi:[1,0] neg_lo:[0,1] neg_hi:[0,1]\n"
    "v_pk_add_f32 v[32:33], v[32:33], v[202:203] op_sel:[0,1] op_sel_hi:[1,1] neg_lo:[0,1] neg_hi:[0,1]\n"
    "v_pk_add_f32 v[34:35], v[34:35], v[202:203] op_sel:[0,1] op_sel_hi:[1,1] neg_lo:[0,1] neg_hi:[0,1]\n"
    "v_pk_add_f32 v[36:37], v[36:37], v[202:203] op_sel:[0,1] op_sel_hi:[1,1] neg_lo:[0,1] neg_hi:[0,1]\n"
    "v_pk_add_f32 v[38:39], v[38:39], v[202:203] op_sel:[0,1] op_sel_hi:[1,1] neg_lo:[0,1] neg_hi:[0,1]\n"
    "v_pk_add_f32 v[40:41], v[40:41], v[202:203] op_sel:[0,1] op_sel_hi:[1,1] neg_lo:[0,1] neg_hi:[0,1]\n"
    "v_pk_add_f32 v[42:43], v[42:43], v[202:203] op_sel:[0,1] op_sel_hi:[1,1] neg_lo:[0,1] neg_hi:[0,1]\n"
    "v_pk_add_f32 v[44:45], v[44:45], v[202:203] op_sel:[0,1] op_sel_hi:[1,1] neg_lo:[0,1] neg_hi:[0,1]\n"
    "v_pk_add_f32 v[46:47], v[46:47], v[202:203] op_sel:[0,1] op_sel_hi:[1,1] neg_lo:[0,1] neg_hi:[0,1]\n"
    "v_pk_add_f32 v[48:49], v[48:49], v[202:203] op_sel:[0,1] op_sel_hi:[1,1] neg_lo:[0,1] neg_hi:[0,1]\n"
    "v_pk_add_f32 v[50:51], v[50:51], v[202:203] op_sel:[0,1] op_sel_hi:[1,1] neg_lo:[0,1] neg_hi:[0,1]\n"
    "v_pk_add_f32 v[52:53], v[52:53], v[202:203] op_sel:[0,1] op_sel_hi:[1,1] neg_lo:[0,1] neg_hi:[0,1]\n"
    "v_pk_add_f32 v[54:55], v[54:55], v[202:203] op_sel:[0,1] op_sel_hi:[1,1] neg_lo:[0,1] neg_hi:[0,1]\n"
    "v_pk_add_f32 v[56:57], v[56:57], v[202:203] op_sel:[0,1] op_sel_hi:[1,1] neg_lo:[0,1] neg_hi:[0,1]\n"
    "v_pk_add_f32 v[58:59], v[58:59], v[202:203] op_sel:[0,1] op_sel_hi:[1,1] neg_lo:[0,1] neg_hi:[0,1]\n"
    "v_pk_add_f32 v[60:61], v[60:61], v[202:203] op_sel:[0,1] op_sel_hi:[1,1] neg_lo:[0,1] neg_hi:[0,1]\n"
    "v_pk_add_f32 v[62:63], v[62:63], v[202:203] op_sel:[0,1] op_sel_hi:[1,1] neg_lo:[0,1] neg_hi:[0,1]\n"
    "v_exp_f32 v16, v16\n"
    "v_exp_f32 v17, v17\n"
    "v_exp_f32 v18, v18\n"
    "v_exp_f32 v19, v19\n"
    "v_cvt_pk_bf16_f32 v216, v16, v17\n"
    "v_exp_f32 v20, v20\n"
    "buffer_load_dwordx2 v[204:205], v5, s[16:19], 0 offen sc1\n"
    "buffer_load_dwordx4 v252, s[28:31], s43 offen offset:0 lds\n"
    "v_exp_f32 v21, v21\n"
    "v_cvt_pk_bf16_f32 v217, v18, v19\n"
    "v_exp_f32 v22, v22\n"
    "v_exp_f32 v23, v23\n"
    "v_cvt_pk_bf16_f32 v218, v20, v21\n"
    "v_exp_f32 v24, v24\n"
    "v_exp_f32 v25, v25\n"
    "v_cvt_pk_bf16_f32 v219, v22, v23\n"
    "v_exp_f32 v26, v26\n"
    "buffer_load_dwordx4 v253, s[28:31], s43 offen offset:1024 lds\n"
    "v_exp_f32 v27, v27\n"
    "v_cvt_pk_bf16_f32 v220, v24, v25\n"
    "v_exp_f32 v28, v28\n"
    "v_exp_f32 v29, v29\n"
    "v_cvt_pk_bf16_f32 v221, v26, v27\n"
    "v_exp_f32 v30, v30\n"
    "v_exp_f32 v31, v31\n"
    "v_cvt_pk_bf16_f32 v222, v28, v29\n"
    "v_exp_f32 v32, v32\n"
    "v_cvt_pk_bf16_f32 v223, v30, v31\n"
    "v_exp_f32 v33, v33\n"
    "v_exp_f32 v34, v34\n"
    "v_exp_f32 v35, v35\n"
    "v_cvt_pk_bf16_f32 v240, v32, v33\n"
    "v_exp_f32 v36, v36\n"
    "v_exp_f32 v37, v37\n"
    "v_cvt_pk_bf16_f32 v241, v34, v35\n"
    "v_mfma_f32_16x16x32_bf16 v[208:211], v[212:215], v[216:219], 0\n"
    "buffer_load_dwordx4 v254, s[28:31], s43 offen offset:2048 lds\n"
    "v_exp_f32 v38, v38\n"
    "v_exp_f32 v39, v39\n"
    "v_cvt_pk_bf16_f32 v242, v36, v37\n"
    "v_mfma_f32_32x32x16_bf16 v[128:143], v[164:167], v[216:219], 0\n"
    "buffer_load_dwordx4 v255, s[28:31], s43 offen offset:3072 lds\n"
    "v_exp_f32 v40, v40\n"
    "v_exp_f32 v41, v41\n"
    "v_cvt_pk_bf16_f32 v243, v38, v39\n"
    "v_exp_f32 v42, v42\n"
    "v_exp_f32 v43, v43\n"
    "v_cvt_pk_bf16_f32 v244, v40, v41\n"
    "v_mfma_f32_16x16x32_bf16 v[208:211], v[212:215], v[220:223], v[208:211]\n"
    "s_add_u32 s43, s43, s44\n"
    "v_exp_f32 v44, v44\n"
    "v_exp_f32 v45, v45\n"
    "v_cvt_pk_bf16_f32 v245, v42, v43\n"
    "v_mfma_f32_32x32x16_bf16 v[144:159], v[164:167], v[220:223], 0\n"
    "v_lshlrev_b32 v206, 2, v1\n"
    "s_lshr_b32 s75, s42, 4\n"
    "buffer_load_dword v207, v206, s[20:23], s75 offen\n"
    "v_exp_f32 v46, v46\n"
    "v_exp_f32 v47, v47\n"
    "s_add_u32 m0, s42, 0x0\n"
    "v_cvt_pk_bf16_f32 v246, v44, v45\n"
    "v_exp_f32 v48, v48\n"
    "v_cvt_pk_bf16_f32 v247, v46, v47\n"
    "v_exp_f32 v49, v49\n"
    "v_exp_f32 v50, v50\n"
    "buffer_load_dwordx4 v252, s[28:31], s43 offen offset:0 lds\n"
    "v_exp_f32 v51, v51\n"
    "v_cvt_pk_bf16_f32 v216, v48, v49\n"
    "v_exp_f32 v52, v52\n"
    "v_exp_f32 v53, v53\n"
    "v_cvt_pk_bf16_f32 v217, v50, v51\n"
    "v_add_f32 v184, v128, v152\n"
    "v_add_f32 v185, v129, v153\n"
    "v_add_f32 v186, v130, v154\n"
    "v_add_f32 v187, v131, v155\n"
    "v_add_f32 v188, v132, v156\n"
    "v_add_f32 v189, v133, v157\n"
    "v_add_f32 v190, v134, v158\n"
    "v_add_f32 v191, v135, v159\n"
    "v_mov_b32 v200, v208\n"
    "v_mfma_f32_32x32x16_bf16 v[128:143], v[164:167], v[240:243], 0\n"
    "buffer_load_dwordx4 v253, s[28:31], s43 offen offset:1024 lds\n"
    "v_exp_f32 v54, v54\n"
    "v_exp_f32 v55, v55\n"
    "v_cvt_pk_bf16_f32 v218, v52, v53\n"
    "v_exp_f32 v56, v56\n"
    "v_exp_f32 v57, v57\n"
    "v_mfma_f32_16x16x32_bf16 v[208:211], v[212:215], v[240:243], 0\n"
    "v_cvt_pk_bf16_f32 v219, v54, v55\n"
    "v_exp_f32 v58, v58\n"
    "v_exp_f32 v59, v59\n"
    "v_mfma_f32_32x32x16_bf16 v[144:159], v[164:167], v[244:247], 0\n"
    "buffer_load_dwordx4 v254, s[28:31], s43 offen offset:2048 lds\n"
    "v_cvt_pk_bf16_f32 v220, v56, v57\n"
    "v_exp_f32 v60, v60\n"
    "v_exp_f32 v61, v61\n"
    "v_cvt_pk_bf16_f32 v221, v58, v59\n"
    "v_exp_f32 v62, v62\n"
    "v_mfma_f32_16x16x32_bf16 v[208:211], v[212:215], v[244:247], v[208:211]\n"
    "v_exp_f32 v63, v63\n"
    "v_cvt_pk_bf16_f32 v222, v60, v61\n"
    "v_mfma_f32_32x32x16_bf16 v[128:143], v[172:175], v[216:219], v[128:143]\n"
    "buffer_load_dwordx4 v255, s[28:31], s43 offen offset:3072 lds\n"
    "s_add_u32 s43, s43, s44\n"
    "s_waitcnt vmcnt(9)\n"
    "v_cmp_ne_u64 vcc, s[62:63], v[204:205]\n"
    "s_cbranch_vccnz Lkvfr_4\n"
    "Lkvfb_5:\n"
    "v_cvt_pk_bf16_f32 v223, v62, v63\n"
    "s_cmp_gt_u32 s54, 0\n"
    "s_cselect_b32 s70, 0, 0xf149f2ca\n"
    "v_sub_f32 v48, s70, v202\n"
    "v_sub_f32 v49, s70, v202\n"
    "v_sub_f32 v50, s70, v202\n"
    "v_mfma_f32_16x16x32_bf16 v[208:211], v[212:215], v[216:219], v[208:211]\n"
    "v_sub_f32 v51, s70, v202\n"
    "v_sub_f32 v52, s70, v202\n"
    "v_sub_f32 v53, s70, v202\n"
    "v_mfma_f32_32x32x16_bf16 v[144:159], v[172:175], v[220:223], v[144:159]\n"
    "v_sub_f32 v54, s70, v202\n"
    "v_sub_f32 v55, s70, v202\n"
    "v_sub_f32 v56, s70, v202\n"
    "v_sub_f32 v57, s70, v202\n"
    "v_sub_f32 v58, s70, v202\n"
    "v_sub_f32 v59, s70, v202\n"
    "v_mfma_f32_16x16x32_bf16 v[208:211], v[212:215], v[220:223], v[208:211]\n"
    "buffer_load_dwordx4 v[64:67], v10, s[16:19], s83 offen offset:2048 sc1\n"
    "v_sub_f32 v60, s70, v202\n"
    "v_sub_f32 v61, s70, v202\n"
    "v_sub_f32 v62, s70, v202\n"
    "v_sub_f32 v63, s70, v202\n"
    "v_add_f32 v192, v128, v152\n"
    "buffer_load_dwordx4 v[96:99], v10, s[16:19], s83 offen offset:3072 sc1\n"
    "buffer_load_dwordx4 v[68:71], v10, s[16:19], s83 offen offset:0 sc1\n"
    "v_add_f32 v193, v129, v153\n"
    "v_add_f32 v194, v130, v154\n"
    "v_add_f32 v195, v131, v155\n"
    "v_add_f32 v196, v132, v156\n"
    "v_add_f32 v197, v133, v157\n"
    "buffer_load_dwordx4 v[100:103], v10, s[16:19], s83 offen offset:1024 sc1\n"
    "v_add_f32 v198, v134, v158\n"
    "v_add_f32 v199, v135, v159\n"
    "v_mov_b32 v201, v208\n"
    "s_waitcnt vmcnt(4)\n"
    "s_barrier\n"
    "v_add_u32 v2, s42, v248\n"
    "v_add_u32 v3, 0x10000, v2\n"
    "buffer_load_dwordx4 v[72:75], v10, s[16:19], s84 offen offset:2048 sc1\n"
    "ds_read_b64_tr_b16 a[192:193], v3 offset:49152\n"
    "ds_read_b64_tr_b16 a[194:195], v3 offset:50176\n"
    "ds_read_b64_tr_b16 a[196:197], v3 offset:0\n"
    "ds_read_b64_tr_b16 a[198:199], v3 offset:1024\n"
    "ds_read_b64_tr_b16 a[200:201], v3 offset:16384\n"
    "ds_read_b64_tr_b16 a[202:203], v3 offset:17408\n"
    "buffer_load_dwordx4 v[104:107], v10, s[16:19], s84 offen offset:3072 sc1\n"
    "ds_read_b64_tr_b16 a[204:205], v3 offset:32768\n"
    "ds_read_b64_tr_b16 a[206:207], v3 offset:33792\n"
    "v_add_u32 v2, 0x1000, v2\n"
    "v_and_b32 v2, 0x3fff, v2\n"
    "v_add_u32 v3, 0x10000, v2\n"
    "ds_read_b64_tr_b16 a[208:209], v3 offset:49152\n"
    "ds_read_b64_tr_b16 a[210:211], v3 offset:50176\n"
    "buffer_load_dwordx4 v[76:79], v10, s[16:19], s84 offen offset:0 sc1\n"
    "ds_read_b64_tr_b16 a[212:213], v3 offset:0\n"
    "ds_read_b64_tr_b16 a[214:215], v3 offset:1024\n"
    "ds_read_b64_tr_b16 a[216:217], v3 offset:16384\n"
    "ds_read_b64_tr_b16 a[218:219], v3 offset:17408\n"
    "ds_read_b64_tr_b16 a[220:221], v3 offset:32768\n"
    "ds_read_b64_tr_b16 a[222:223], v3 offset:33792\n"
    "buffer_load_dwordx4 v[108:111], v10, s[16:19], s84 offen offset:1024 sc1\n"
    "v_add_u32 v2, 0x1000, v2\n"
    "v_and_b32 v2, 0x3fff, v2\n"
    "v_add_u32 v3, 0x10000, v2\n"
    "ds_read_b64_tr_b16 a[224:225], v3 offset:49152\n"
    "ds_read_b64_tr_b16 a[226:227], v3 offset:50176\n"
    "ds_read_b64_tr_b16 a[228:229], v3 offset:0\n"
    "ds_read_b64_tr_b16 a[230:231], v3 offset:1024\n"
    "buffer_load_dwordx4 v[80:83], v10, s[16:19], s85 offen offset:2048 sc1\n"
    "ds_read_b64_tr_b16 a[232:233], v3 offset:16384\n"
    "ds_read_b64_tr_b16 a[234:235], v3 offset:17408\n"
    "ds_read_b64_tr_b16 a[236:237], v3 offset:32768\n"
    "ds_read_b64_tr_b16 a[238:239], v3 offset:33792\n"
    "v_add_u32 v2, 0x1000, v2\n"
    "v_and_b32 v2, 0x3fff, v2\n"
    "v_add_u32 v3, 0x10000, v2\n"
    "buffer_load_dwordx4 v[112:115], v10, s[16:19], s85 offen offset:3072 sc1\n"
    "ds_read_b64_tr_b16 a[240:241], v3 offset:49152\n"
    "ds_read_b64_tr_b16 a[242:243], v3 offset:50176\n"
    "ds_read_b64_tr_b16 a[244:245], v3 offset:0\n"
    "ds_read_b64_tr_b16 a[246:247], v3 offset:1024\n"
    "ds_read_b64_tr_b16 a[248:249], v3 offset:16384\n"
    "ds_read_b64_tr_b16 a[250:251], v3 offset:17408\n"
    "buffer_load_dwordx4 v[84:87], v10, s[16:19], s85 offen offset:0 sc1\n"
    "ds_read_b64_tr_b16 a[252:253], v3 offset:32768\n"
    "ds_read_b64_tr_b16 a[254:255], v3 offset:33792\n"
    "s_cmp_lt_u32 s53, 28\n"
    "s_cbranch_scc1 Lw1t\n"
    "v_lshlrev_b32 v160, 16, v207\n"
    "v_and_b32 v161, s37, v207\n"
    "v_mul_f32 v162, 0xbfb8aa3b, v160\n"
    "v_mul_f32 v163, 0xbfb8aa3b, v161\n"
    "v_exp_f32 v162, v162\n"
    "v_exp_f32 v163, v163\n"
    "s_lshr_b32 s74, s42, 4\n"
    "v_add_f32 v162, 1.0, v162\n"
    "v_add_f32 v163, 1.0, v163\n"
    "v_rcp_f32 v162, v162\n"
    "v_rcp_f32 v163, v163\n"
    "s_add_u32 s74, s74, 0x27000\n"
    "v_mul_f32 v160, v160, v162\n"
    "v_mul_f32 v161, v161, v163\n"
    "v_cvt_pk_bf16_f32 v160, v160, v161\n"
    "v_lshl_add_u32 v161, v4, 5, s74\n"
    "v_and_b32 v162, 2, v1\n"
    "v_lshl_add_u32 v161, v162, 3, v161\n"
    "v_and_b32 v162, 4, v1\n"
    "v_lshl_add_u32 v161, v162, 1, v161\n"
    "v_and_b32 v162, 1, v1\n"
    "v_lshl_add_u32 v161, v162, 2, v161\n"
    "ds_write_b32 v161, v160\n"
    "v_lshrrev_b32 v163, 5, v1\n"
    "v_lshl_add_u32 v163, v163, 4, s74\n"
    "v_add_u32 v166, 0x20000, v11\n"
    "v_add_u32 v167, 0x20000, v12\n"
    "ds_read_b128 v[16:19], v163 offset:0\n"
    "ds_read_b128 v[20:23], v163 offset:32\n"
    "ds_read_b128 v[24:27], v163 offset:64\n"
    "ds_read_b64_tr_b16 a[64:65], v166 offset:0\n"
    "ds_read_b64_tr_b16 a[66:67], v166 offset:1024\n"
    "ds_read_b64_tr_b16 a[68:69], v167 offset:0\n"
    "ds_read_b64_tr_b16 a[70:71], v167 offset:1024\n"
    "ds_read_b64_tr_b16 a[72:73], v166 offset:2048\n"
    "ds_read_b64_tr_b16 a[74:75], v166 offset:3072\n"
    "ds_read_b64_tr_b16 a[76:77], v167 offset:2048\n"
    "ds_read_b64_tr_b16 a[78:79], v167 offset:3072\n"
    "s_waitcnt lgkmcnt(0)\n"
    "v_mfma_f32_32x32x16_bf16 v[128:143], v[16:19], a[64:67], 0\n"
    "ds_read_b64_tr_b16 a[80:81], v11 offset:32768\n"
    "ds_read_b64_tr_b16 a[82:83], v11 offset:33792\n"
    "buffer_load_dwordx4 v[116:119], v10, s[16:19], s85 offen offset:1024 sc1\n"
    "v_lshl_add_u32 v160, v1, 2, s74\n"
    "v_and_b32 v161, 31, v1\n"
    "v_mfma_f32_32x32x16_bf16 v[144:159], v[16:19], a[68:71], 0\n"
    "ds_read_b64_tr_b16 a[84:85], v12 offset:32768\n"
    "ds_read_b64_tr_b16 a[86:87], v12 offset:33792\n"
    "ds_read_b128 v[28:31], v163 offset:96\n"
    "v_lshlrev_b32 v161, 3, v161\n"
    "v_add_u32 v162, 0x27400, v161\n"
    "s_lshl_b32 s73, s46, 8\n"
    "v_mfma_f32_32x32x16_bf16 v[128:143], v[20:23], a[72:75], v[128:143]\n"
    "ds_read_b64_tr_b16 a[88:89], v11 offset:34816\n"
    "ds_read_b64_tr_b16 a[90:91], v11 offset:35840\n"
    "buffer_load_dwordx4 v[88:91], v10, s[16:19], s86 offen offset:2048 sc1\n"
    "s_add_u32 s73, s73, 0x1000\n"
    "s_lshr_b32 s72, s46, 3\n"
    "v_mfma_f32_32x32x16_bf16 v[144:159], v[20:23], a[76:79], v[144:159]\n"
    "ds_read_b64_tr_b16 a[92:93], v12 offset:34816\n"
    "ds_read_b64_tr_b16 a[94:95], v12 offset:35840\n"
    "ds_read_b128 v[16:19], v163 offset:128\n"
    "s_cmp_eq_u32 s72, 4\n"
    "s_cselect_b32 s72, 1.0, 0\n"
    "s_waitcnt lgkmcnt(8)\n"
    "v_mfma_f32_32x32x16_bf16 v[128:143], v[24:27], a[80:83], v[128:143]\n"
    "ds_read_b64_tr_b16 a[96:97], v11 offset:49152\n"
    "ds_read_b64_tr_b16 a[98:99], v11 offset:50176\n"
    "buffer_load_dwordx4 v[120:123], v10, s[16:19], s86 offen offset:3072 sc1\n"
    "s_waitcnt lgkmcnt(8)\n"
    "v_mfma_f32_32x32x16_bf16 v[144:159], v[24:27], a[84:87], v[144:159]\n"
    "ds_read_b64_tr_b16 a[100:101], v12 offset:49152\n"
    "ds_read_b64_tr_b16 a[102:103], v12 offset:50176\n"
    "ds_read_b128 v[20:23], v163 offset:160\n"
    "s_waitcnt lgkmcnt(8)\n"
    "v_mfma_f32_32x32x16_bf16 v[128:143], v[28:31], a[88:91], v[128:143]\n"
    "ds_read_b64_tr_b16 a[104:105], v11 offset:51200\n"
    "ds_read_b64_tr_b16 a[106:107], v11 offset:52224\n"
    "s_waitcnt lgkmcnt(8)\n"
    "v_mfma_f32_32x32x16_bf16 v[144:159], v[28:31], a[92:95], v[144:159]\n"
    "ds_read_b64_tr_b16 a[108:109], v12 offset:51200\n"
    "ds_read_b64_tr_b16 a[110:111], v12 offset:52224\n"
    "ds_read_b128 v[24:27], v163 offset:192\n"
    "buffer_load_dwordx4 v[92:95], v10, s[16:19], s86 offen offset:0 sc1\n"
    "s_waitcnt lgkmcnt(8)\n"
    "v_mfma_f32_32x32x16_bf16 v[128:143], v[16:19], a[96:99], v[128:143]\n"
    "ds_read_b64_tr_b16 a[112:113], v11 offset:0\n"
    "ds_read_b64_tr_b16 a[114:115], v11 offset:1024\n"
    "s_waitcnt lgkmcnt(8)\n"
    "v_mfma_f32_32x32x16_bf16 v[144:159], v[16:19], a[100:103], v[144:159]\n"
    "ds_read_b64_tr_b16 a[116:117], v12 offset:0\n"
    "ds_read_b64_tr_b16 a[118:119], v12 offset:1024\n"
    "ds_read_b128 v[28:31], v163 offset:224\n"
    "buffer_load_dwordx4 v[124:127], v10, s[16:19], s86 offen offset:1024 sc1\n"
    "s_waitcnt lgkmcnt(8)\n"
    "v_mfma_f32_32x32x16_bf16 v[128:143], v[20:23], a[104:107], v[128:143]\n"
    "ds_read_b64_tr_b16 a[120:121], v11 offset:2048\n"
    "ds_read_b64_tr_b16 a[122:123], v11 offset:3072\n"
    "s_waitcnt lgkmcnt(8)\n"
    "v_mfma_f32_32x32x16_bf16 v[144:159], v[20:23], a[108:111], v[144:159]\n"
    "ds_read_b64_tr_b16 a[124:125], v12 offset:2048\n"
    "ds_read_b64_tr_b16 a[126:127], v12 offset:3072\n"
    "s_waitcnt lgkmcnt(7)\n"
    "v_mfma_f32_32x32x16_bf16 v[128:143], v[24:27], a[112:115], v[128:143]\n"
    "s_waitcnt lgkmcnt(5)\n"
    "v_mfma_f32_32x32x16_bf16 v[144:159], v[24:27], a[116:119], v[144:159]\n"
    "s_waitcnt lgkmcnt(2)\n"
    "v_mfma_f32_32x32x16_bf16 v[128:143], v[28:31], a[120:123], v[128:143]\n"
    "s_waitcnt lgkmcnt(0)\n"
    "v_mfma_f32_32x32x16_bf16 v[144:159], v[28:31], a[124:127], v[144:159]\n"
    "s_nop 11\n"
    "v_permlane32_swap_b32 v128, v144\n"
    "ds_write_b32 v160, v128 offset:1024\n"
    "s_waitcnt lgkmcnt(0)\n"
    "s_barrier\n"
    "s_cmp_lg_u32 s52, 0\n"
    "s_cbranch_scc1 Lgst_6\n"
    "ds_read_b64 v[130:131], v162 offset:0\n"
    "ds_read_b64 v[132:133], v162 offset:256\n"
    "ds_read_b64 v[134:135], v162 offset:512\n"
    "ds_read_b64 v[136:137], v162 offset:768\n"
    "s_waitcnt lgkmcnt(2)\n"
    "v_pk_add_f32 v[130:131], v[130:131], v[132:133]\n"
    "s_waitcnt lgkmcnt(0)\n"
    "v_pk_add_f32 v[134:135], v[134:135], v[136:137]\n"
    "v_pk_add_f32 v[130:131], v[130:131], v[134:135]\n"
    "v_cvt_pk_bf16_f32 v130, v130, v131\n"
    "v_lshlrev_b32 v132, 16, v130\n"
    "v_and_b32 v133, s37, v130\n"
    "v_add_f32 v132, s72, v132\n"
    "v_add_f32 v133, s72, v133\n"
    "v_cvt_pk_bf16_f32 v130, v132, v133\n"
    "v_mov_b32 v131, s62\n"
    "buffer_store_dwordx2 v[130:131], v161, s[16:19], s73 offen sc1\n"
    "Lgst_6:\n"
    "s_branch Lw1u\n"
    "Lw1t:\n"
    "v_add_u32 v2, s42, v14\n"
    "buffer_load_dwordx4 v[116:119], v10, s[16:19], s85 offen offset:1024 sc1\n"
    "buffer_load_dwordx4 v[88:91], v10, s[16:19], s86 offen offset:2048 sc1\n"
    "v_add_u32 v3, 0x20000, v2\n"
    "ds_read_b64_tr_b16 a[64:65], v3 offset:0\n"
    "ds_read_b64_tr_b16 a[66:67], v3 offset:1024\n"
    "ds_read_b64_tr_b16 a[68:69], v2 offset:32768\n"
    "buffer_load_dwordx4 v[120:123], v10, s[16:19], s86 offen offset:3072 sc1\n"
    "ds_read_b64_tr_b16 a[70:71], v2 offset:33792\n"
    "ds_read_b64_tr_b16 a[72:73], v2 offset:49152\n"
    "ds_read_b64_tr_b16 a[74:75], v2 offset:50176\n"
    "ds_read_b64_tr_b16 a[76:77], v2 offset:0\n"
    "ds_read_b64_tr_b16 a[78:79], v2 offset:1024\n"
    "v_add_u32 v2, 0x1000, v2\n"
    "buffer_load_dwordx4 v[92:95], v10, s[16:19], s86 offen offset:0 sc1\n"
    "v_and_b32 v2, 0x3fff, v2\n"
    "v_add_u32 v3, 0x20000, v2\n"
    "ds_read_b64_tr_b16 a[96:97], v3 offset:0\n"
    "ds_read_b64_tr_b16 a[98:99], v3 offset:1024\n"
    "ds_read_b64_tr_b16 a[100:101], v2 offset:32768\n"
    "ds_read_b64_tr_b16 a[102:103], v2 offset:33792\n"
    "ds_read_b64_tr_b16 a[104:105], v2 offset:49152\n"
    "buffer_load_dwordx4 v[124:127], v10, s[16:19], s86 offen offset:1024 sc1\n"
    "ds_read_b64_tr_b16 a[106:107], v2 offset:50176\n"
    "ds_read_b64_tr_b16 a[108:109], v2 offset:0\n"
    "ds_read_b64_tr_b16 a[110:111], v2 offset:1024\n"
    "v_add_u32 v2, 0x1000, v2\n"
    "v_and_b32 v2, 0x3fff, v2\n"
    "v_add_u32 v3, 0x20000, v2\n"
    "ds_read_b64_tr_b16 a[128:129], v3 offset:0\n"
    "ds_read_b64_tr_b16 a[130:131], v3 offset:1024\n"
    "ds_read_b64_tr_b16 a[132:133], v2 offset:32768\n"
    "ds_read_b64_tr_b16 a[134:135], v2 offset:33792\n"
    "ds_read_b64_tr_b16 a[136:137], v2 offset:49152\n"
    "ds_read_b64_tr_b16 a[138:139], v2 offset:50176\n"
    "ds_read_b64_tr_b16 a[140:141], v2 offset:0\n"
    "ds_read_b64_tr_b16 a[142:143], v2 offset:1024\n"
    "v_add_u32 v2, 0x1000, v2\n"
    "v_and_b32 v2, 0x3fff, v2\n"
    "v_add_u32 v3, 0x20000, v2\n"
    "ds_read_b64_tr_b16 a[160:161], v3 offset:0\n"
    "ds_read_b64_tr_b16 a[162:163], v3 offset:1024\n"
    "ds_read_b64_tr_b16 a[164:165], v2 offset:32768\n"
    "ds_read_b64_tr_b16 a[166:167], v2 offset:33792\n"
    "ds_read_b64_tr_b16 a[168:169], v2 offset:49152\n"
    "ds_read_b64_tr_b16 a[170:171], v2 offset:50176\n"
    "ds_read_b64_tr_b16 a[172:173], v2 offset:0\n"
    "ds_read_b64_tr_b16 a[174:175], v2 offset:1024\n"
    "Lw1u:\n"
    "s_load_dwordx4 s[20:23], s[0:1], 160\n"
    "s_waitcnt lgkmcnt(0)\n"
    "s_barrier\n"
    "s_waitcnt vmcnt(15)\n"
    "v_mfma_f32_32x32x16_bf16 v[16:31], v[64:67], v[176:179], v[48:63]\n"
    "v_accvgpr_write_b32 a0, -1\n"
    "v_accvgpr_write_b32 a1, -1\n"
    "v_accvgpr_write_b32 a2, -1\n"
    "v_accvgpr_write_b32 a3, -1\n"
    "s_and_b32 s69, s61, 0xfffff800\n"
    "s_waitcnt vmcnt(13)\n"
    "v_mfma_f32_32x32x16_bf16 v[32:47], v[68:71], v[176:179], v[48:63]\n"
    "s_lshr_b32 s75, s42, 3\n"
    "s_add_u32 s69, s69, s75\n"
    "v_lshlrev_b32 v206, 3, v1\n"
    "s_nop 1\n"
    "v_exp_f32 v16, v16\n"
    "v_exp_f32 v17, v17\n"
    "v_exp_f32 v18, v18\n"
    "v_exp_f32 v19, v19\n"
    "v_cvt_pk_bf16_f32 v216, v16, v17\n"
    "v_exp_f32 v20, v20\n"
    "s_add_u32 m0, s42, 0x4000\n"
    "s_nop 0\n"
    "buffer_load_dwordx4 v252, s[28:31], s43 offen offset:0 lds\n"
    "buffer_load_dwordx4 v253, s[28:31], s43 offen offset:1024 lds\n"
    "v_exp_f32 v21, v21\n"
    "v_cvt_pk_bf16_f32 v217, v18, v19\n"
    "v_exp_f32 v22, v22\n"
    "v_exp_f32 v23, v23\n"
    "v_cvt_pk_bf16_f32 v218, v20, v21\n"
    "v_exp_f32 v24, v24\n"
    "buffer_load_dwordx4 v254, s[28:31], s43 offen offset:2048 lds\n"
    "v_exp_f32 v25, v25\n"
    "v_cvt_pk_bf16_f32 v219, v22, v23\n"
    "v_exp_f32 v26, v26\n"
    "v_exp_f32 v27, v27\n"
    "v_cvt_pk_bf16_f32 v220, v24, v25\n"
    "v_exp_f32 v28, v28\n"
    "buffer_load_dwordx4 v255, s[28:31], s43 offen offset:3072 lds\n"
    "v_exp_f32 v29, v29\n"
    "v_cvt_pk_bf16_f32 v221, v26, v27\n"
    "v_exp_f32 v30, v30\n"
    "v_exp_f32 v31, v31\n"
    "v_cvt_pk_bf16_f32 v222, v28, v29\n"
    "v_exp_f32 v32, v32\n"
    "v_cvt_pk_bf16_f32 v223, v30, v31\n"
    "v_exp_f32 v33, v33\n"
    "s_cmp_eq_u32 s54, 1\n"
    "s_cbranch_scc1 Lfx_7\n"
    "Lfb_8:\n"
    "s_waitcnt vmcnt(15)\n"
    "v_mfma_f32_32x32x16_bf16 v[16:31], v[72:75], v[176:179], v[48:63]\n"
    "s_add_u32 s43, s43, s44\n"
    "v_exp_f32 v34, v34\n"
    "v_exp_f32 v35, v35\n"
    "v_cvt_pk_bf16_f32 v240, v32, v33\n"
    "v_exp_f32 v36, v36\n"
    "v_exp_f32 v37, v37\n"
    "v_cvt_pk_bf16_f32 v241, v34, v35\n"
    "v_mfma_f32_16x16x32_bf16 v[208:211], v[212:215], v[216:219], 0\n"
    "s_add_u32 s75, s57, 0\n"
    "buffer_store_dwordx4 a[0:3], v10, s[16:19], s75 offen sc1\n"
    "v_exp_f32 v38, v38\n"
    "v_exp_f32 v39, v39\n"
    "v_cvt_pk_bf16_f32 v242, v36, v37\n"
    "v_mfma_f32_32x32x16_bf16 v[128:143], v[96:99], v[216:219], 0\n"
    "s_add_u32 m0, s42, 0x10000\n"
    "v_exp_f32 v40, v40\n"
    "v_exp_f32 v41, v41\n"
    "v_cvt_pk_bf16_f32 v243, v38, v39\n"
    "v_exp_f32 v42, v42\n"
    "v_exp_f32 v43, v43\n"
    "v_cvt_pk_bf16_f32 v244, v40, v41\n"
    "v_mfma_f32_16x16x32_bf16 v[208:211], v[212:215], v[220:223], v[208:211]\n"
    "buffer_load_dwordx4 v252, s[28:31], s43 offen offset:0 lds\n"
    "v_exp_f32 v44, v44\n"
    "v_exp_f32 v45, v45\n"
    "v_cvt_pk_bf16_f32 v245, v42, v43\n"
    "v_mfma_f32_32x32x16_bf16 v[144:159], v[96:99], v[220:223], 0\n"
    "buffer_load_dwordx4 v253, s[28:31], s43 offen offset:1024 lds\n"
    "v_exp_f32 v46, v46\n"
    "v_exp_f32 v47, v47\n"
    "v_cvt_pk_bf16_f32 v246, v44, v45\n"
    "v_exp_f32 v16, v16\n"
    "v_cvt_pk_bf16_f32 v247, v46, v47\n"
    "v_exp_f32 v17, v17\n"
    "s_waitcnt vmcnt(16)\n"
    "v_mfma_f32_32x32x16_bf16 v[32:47], v[76:79], v[176:179], v[48:63]\n"
    "buffer_load_dwordx4 v254, s[28:31], s43 offen offset:2048 lds\n"
    "v_exp_f32 v18, v18\n"
    "v_exp_f32 v19, v19\n"
    "v_cvt_pk_bf16_f32 v216, v16, v17\n"
    "v_exp_f32 v20, v20\n"
    "v_exp_f32 v21, v21\n"
    "v_cvt_pk_bf16_f32 v217, v18, v19\n"
    "v_mfma_f32_16x16x32_bf16 v[208:211], v[212:215], v[240:243], v[208:211]\n"
    "buffer_load_dwordx4 v255, s[28:31], s43 offen offset:3072 lds\n"
    "v_exp_f32 v22, v22\n"
    "v_exp_f32 v23, v23\n"
    "v_cvt_pk_bf16_f32 v218, v20, v21\n"
    "v_mfma_f32_32x32x16_bf16 v[128:143], v[100:103], v[240:243], v[128:143]\n"
    "s_add_u32 s43, s43, s44\n"
    "v_exp_f32 v24, v24\n"
    "v_exp_f32 v25, v25\n"
    "v_cvt_pk_bf16_f32 v219, v22, v23\n"
    "v_exp_f32 v26, v26\n"
    "v_exp_f32 v27, v27\n"
    "v_cvt_pk_bf16_f32 v220, v24, v25\n"
    "v_mfma_f32_16x16x32_bf16 v[208:211], v[212:215], v[244:247], v[208:211]\n"
    "s_add_u32 s75, s57, 32768\n"
    "buffer_store_dwordx4 a[0:3], v10, s[16:19], s75 offen sc1\n"
    "v_exp_f32 v28, v28\n"
    "v_exp_f32 v29, v29\n"
    "v_cvt_pk_bf16_f32 v221, v26, v27\n"
    "v_mfma_f32_32x32x16_bf16 v[144:159], v[100:103], v[244:247], v[144:159]\n"
    "s_add_u32 m0, s42, 0x14000\n"
    "v_exp_f32 v30, v30\n"
    "v_exp_f32 v31, v31\n"
    "v_cvt_pk_bf16_f32 v222, v28, v29\n"
    "v_exp_f32 v32, v32\n"
    "v_cvt_pk_bf16_f32 v223, v30, v31\n"
    "v_exp_f32 v33, v33\n"
    "s_cmp_eq_u32 s54, 2\n"
    "s_cbranch_scc1 Lfx_9\n"
    "Lfb_10:\n"
    "s_waitcnt vmcnt(17)\n"
    "v_mfma_f32_32x32x16_bf16 v[16:31], v[80:83], v[176:179], v[48:63]\n"
    "buffer_load_dwordx4 v252, s[28:31], s43 offen offset:0 lds\n"
    "v_exp_f32 v34, v34\n"
    "v_exp_f32 v35, v35\n"
    "v_cvt_pk_bf16_f32 v240, v32, v33\n"
    "v_exp_f32 v36, v36\n"
    "v_exp_f32 v37, v37\n"
    "v_cvt_pk_bf16_f32 v241, v34, v35\n"
    "v_mfma_f32_16x16x32_bf16 v[208:211], v[212:215], v[216:219], v[208:211]\n"
    "buffer_load_dwordx4 v253, s[28:31], s43 offen offset:1024 lds\n"
    "v_exp_f32 v38, v38\n"
    "v_exp_f32 v39, v39\n"
    "v_cvt_pk_bf16_f32 v242, v36, v37\n"
    "v_mfma_f32_32x32x16_bf16 v[128:143], v[104:107], v[216:219], v[128:143]\n"
    "buffer_load_dwordx4 v254, s[28:31], s43 offen offset:2048 lds\n"
    "v_exp_f32 v40, v40\n"
    "v_exp_f32 v41, v41\n"
    "v_cvt_pk_bf16_f32 v243, v38, v39\n"
    "v_exp_f32 v42, v42\n"
    "v_exp_f32 v43, v43\n"
    "v_cvt_pk_bf16_f32 v244, v40, v41\n"
    "v_mfma_f32_16x16x32_bf16 v[208:211], v[212:215], v[220:223], v[208:211]\n"
    "buffer_load_dwordx4 v255, s[28:31], s43 offen offset:3072 lds\n"
    "v_exp_f32 v44, v44\n"
    "v_exp_f32 v45, v45\n"
    "v_cvt_pk_bf16_f32 v245, v42, v43\n"
    "v_mfma_f32_32x32x16_bf16 v[144:159], v[104:107], v[220:223], v[144:159]\n"
    "s_add_u32 s43, s43, s44\n"
    "v_exp_f32 v46, v46\n"
    "v_exp_f32 v47, v47\n"
    "v_cvt_pk_bf16_f32 v246, v44, v45\n"
    "v_exp_f32 v16, v16\n"
    "v_cvt_pk_bf16_f32 v247, v46, v47\n"
    "v_exp_f32 v17, v17\n"
    "s_waitcnt vmcnt(19)\n"
    "v_mfma_f32_32x32x16_bf16 v[32:47], v[84:87], v[176:179], v[48:63]\n"
    "s_and_b32 s75, s42, 0x1000\n"
    "s_lshr_b32 s75, s75, 1\n"
    "s_add_u32 s75, s75, s59\n"
    "s_mov_b32 exec_hi, 0\n"
    "buffer_store_dwordx2 a[0:1], v0, s[16:19], s75 offen sc1\n"
    "s_mov_b32 exec_hi, -1\n"
    "v_exp_f32 v18, v18\n"
    "v_exp_f32 v19, v19\n"
    "v_cvt_pk_bf16_f32 v216, v16, v17\n"
    "v_exp_f32 v20, v20\n"
    "v_exp_f32 v21, v21\n"
    "v_cvt_pk_bf16_f32 v217, v18, v19\n"
    "v_mfma_f32_16x16x32_bf16 v[208:211], v[212:215], v[240:243], v[208:211]\n"
    "s_add_u32 m0, s42, 0x18000\n"
    "v_exp_f32 v22, v22\n"
    "v_exp_f32 v23, v23\n"
    "v_cvt_pk_bf16_f32 v218, v20, v21\n"
    "v_mfma_f32_32x32x16_bf16 v[128:143], v[108:111], v[240:243], v[128:143]\n"
    "buffer_load_dwordx4 v252, s[28:31], s43 offen offset:0 lds\n"
    "v_exp_f32 v24, v24\n"
    "v_exp_f32 v25, v25\n"
    "v_cvt_pk_bf16_f32 v219, v22, v23\n"
    "v_exp_f32 v26, v26\n"
    "v_exp_f32 v27, v27\n"
    "v_cvt_pk_bf16_f32 v220, v24, v25\n"
    "v_mfma_f32_16x16x32_bf16 v[208:211], v[212:215], v[244:247], v[208:211]\n"
    "buffer_load_dwordx4 v253, s[28:31], s43 offen offset:1024 lds\n"
    "v_exp_f32 v28, v28\n"
    "v_exp_f32 v29, v29\n"
    "v_cvt_pk_bf16_f32 v221, v26, v27\n"
    "v_mfma_f32_32x32x16_bf16 v[144:159], v[108:111], v[244:247], v[144:159]\n"
    "buffer_load_dwordx4 v254, s[28:31], s43 offen offset:2048 lds\n"
    "v_exp_f32 v30, v30\n"
    "v_exp_f32 v31, v31\n"
    "v_cvt_pk_bf16_f32 v222, v28, v29\n"
    "v_exp_f32 v32, v32\n"
    "v_cvt_pk_bf16_f32 v223, v30, v31\n"
    "v_exp_f32 v33, v33\n"
    "s_cmp_eq_u32 s54, 3\n"
    "s_cbranch_scc1 Lfx_11\n"
    "Lfb_12:\n"
    "s_waitcnt vmcnt(21)\n"
    "v_mfma_f32_32x32x16_bf16 v[16:31], v[88:91], v[176:179], v[48:63]\n"
    "buffer_load_dwordx4 v255, s[28:31], s43 offen offset:3072 lds\n"
    "v_exp_f32 v34, v34\n"
    "v_exp_f32 v35, v35\n"
    "v_cvt_pk_bf16_f32 v240, v32, v33\n"
    "v_exp_f32 v36, v36\n"
    "v_exp_f32 v37, v37\n"
    "v_cvt_pk_bf16_f32 v241, v34, v35\n"
    "v_mfma_f32_16x16x32_bf16 v[208:211], v[212:215], v[216:219], v[208:211]\n"
    "s_add_u32 s43, s43, s44\n"
    "v_exp_f32 v38, v38\n"
    "v_exp_f32 v39, v39\n"
    "v_cvt_pk_bf16_f32 v242, v36, v37\n"
    "v_mfma_f32_32x32x16_bf16 v[128:143], v[112:115], v[216:219], v[128:143]\n"
    "buffer_load_dwordx2 v[160:161], v206, s[16:19], s69 offen sc1\n"
    "v_exp_f32 v40, v40\n"
    "v_exp_f32 v41, v41\n"
    "v_cvt_pk_bf16_f32 v243, v38, v39\n"
    "v_exp_f32 v42, v42\n"
    "v_exp_f32 v43, v43\n"
    "v_cvt_pk_bf16_f32 v244, v40, v41\n"
    "v_mfma_f32_16x16x32_bf16 v[208:211], v[212:215], v[220:223], v[208:211]\n"
    "s_add_u32 s75, s90, 114688\n"
    "buffer_store_dwordx4 a[0:3], v10, s[16:19], s75 offen sc1\n"
    "v_exp_f32 v44, v44\n"
    "v_exp_f32 v45, v45\n"
    "v_cvt_pk_bf16_f32 v245, v42, v43\n"
    "v_mfma_f32_32x32x16_bf16 v[144:159], v[112:115], v[220:223], v[144:159]\n"
    "s_lshl4_add_u32 s71, s76, s39\n"
    "s_add_u32 m0, s42, 0xc000\n"
    "v_exp_f32 v46, v46\n"
    "buffer_load_dwordx4 v6, s[32:35], s71 offen offset:0 lds\n"
    "buffer_load_dwordx4 v7, s[32:35], s71 offen offset:1024 lds\n"
    "v_exp_f32 v47, v47\n"
    "v_cvt_pk_bf16_f32 v246, v44, v45\n"
    "v_exp_f32 v16, v16\n"
    "v_cvt_pk_bf16_f32 v247, v46, v47\n"
    "v_exp_f32 v17, v17\n"
    "v_min_f32 v32, -v224, v48\n"
    "buffer_load_dwordx4 v8, s[32:35], s71 offen offset:2048 lds\n"
    "v_min_f32 v33, -v225, v49\n"
    "v_min_f32 v34, -v226, v50\n"
    "v_min_f32 v35, -v227, v51\n"
    "v_min_f32 v36, -v228, v52\n"
    "v_min_f32 v37, -v229, v53\n"
    "v_min_f32 v38, -v230, v54\n"
    "v_min_f32 v39, -v231, v55\n"
    "v_min_f32 v40, -v232, v56\n"
    "v_min_f32 v41, -v233, v57\n"
    "v_min_f32 v42, -v234, v58\n"
    "v_min_f32 v43, -v235, v59\n"
    "v_min_f32 v44, -v236, v60\n"
    "v_min_f32 v45, -v237, v61\n"
    "v_min_f32 v46, -v238, v62\n"
    "v_min_f32 v47, -v239, v63\n"
    "s_waitcnt vmcnt(25)\n"
    "s_nop 0\n"
    "v_mfma_f32_32x32x16_bf16 v[32:47], v[92:95], v[176:179], v[32:47]\n"
    "buffer_load_dwordx4 v9, s[32:35], s71 offen offset:3072 lds\n"
    "v_exp_f32 v18, v18\n"
    "v_exp_f32 v19, v19\n"
    "v_cvt_pk_bf16_f32 v216, v16, v17\n"
    "v_exp_f32 v20, v20\n"
    "v_exp_f32 v21, v21\n"
    "v_cvt_pk_bf16_f32 v217, v18, v19\n"
    "v_mfma_f32_16x16x32_bf16 v[208:211], v[212:215], v[240:243], v[208:211]\n"
    "s_add_u32 s75, s90, 115712\n"
    "buffer_store_dwordx4 a[0:3], v10, s[16:19], s75 offen sc1\n"
    "v_exp_f32 v22, v22\n"
    "v_exp_f32 v23, v23\n"
    "v_cvt_pk_bf16_f32 v218, v20, v21\n"
    "v_mfma_f32_32x32x16_bf16 v[128:143], v[116:119], v[240:243], v[128:143]\n"
    "v_exp_f32 v24, v24\n"
    "v_exp_f32 v25, v25\n"
    "v_cvt_pk_bf16_f32 v219, v22, v23\n"
    "v_exp_f32 v26, v26\n"
    "v_exp_f32 v27, v27\n"
    "v_cvt_pk_bf16_f32 v220, v24, v25\n"
    "v_mfma_f32_16x16x32_bf16 v[208:211], v[212:215], v[244:247], v[208:211]\n"
    "v_exp_f32 v28, v28\n"
    "v_exp_f32 v29, v29\n"
    "v_cvt_pk_bf16_f32 v221, v26, v27\n"
    "v_mfma_f32_32x32x16_bf16 v[144:159], v[116:119], v[244:247], v[144:159]\n"
    "v_exp_f32 v30, v30\n"
    "v_exp_f32 v31, v31\n"
    "v_cvt_pk_bf16_f32 v222, v28, v29\n"
    "v_exp_f32 v32, v32\n"
    "v_cvt_pk_bf16_f32 v223, v30, v31\n"
    "v_exp_f32 v33, v33\n"
    "v_exp_f32 v34, v34\n"
    "v_exp_f32 v35, v35\n"
    "v_cvt_pk_bf16_f32 v240, v32, v33\n"
    "v_exp_f32 v36, v36\n"
    "v_exp_f32 v37, v37\n"
    "v_cvt_pk_bf16_f32 v241, v34, v35\n"
    "v_mfma_f32_16x16x32_bf16 v[208:211], v[212:215], v[216:219], v[208:211]\n"
    "v_exp_f32 v38, v38\n"
    "v_exp_f32 v39, v39\n"
    "v_cvt_pk_bf16_f32 v242, v36, v37\n"
    "v_mfma_f32_32x32x16_bf16 v[128:143], v[120:123], v[216:219], v[128:143]\n"
    "v_exp_f32 v40, v40\n"
    "v_exp_f32 v41, v41\n"
    "v_cvt_pk_bf16_f32 v243, v38, v39\n"
    "v_exp_f32 v42, v42\n"
    "v_exp_f32 v43, v43\n"
    "v_cvt_pk_bf16_f32 v244, v40, v41\n"
    "v_mfma_f32_16x16x32_bf16 v[208:211], v[212:215], v[220:223], v[208:211]\n"
    "v_exp_f32 v44, v44\n"
    "v_exp_f32 v45, v45\n"
    "v_cvt_pk_bf16_f32 v245, v42, v43\n"
    "v_mfma_f32_32x32x16_bf16 v[144:159], v[120:123], v[220:223], v[144:159]\n"
    "v_exp_f32 v46, v46\n"
    "v_exp_f32 v47, v47\n"
    "v_cvt_pk_bf16_f32 v246, v44, v45\n"
    "s_waitcnt vmcnt(26)\n"
    "v_mfma_f32_32x32x16_bf16 v[128:143], v[124:127], v[240:243], v[128:143]\n"
    "v_cvt_pk_bf16_f32 v247, v46, v47\n"
    "v_mfma_f32_16x16x32_bf16 v[208:211], v[212:215], v[240:243], v[208:211]\n"
    "v_sub_f32 v48, s70, v203\n"
    "v_sub_f32 v49, s70, v203\n"
    "v_sub_f32 v50, s70, v203\n"
    "v_sub_f32 v51, s70, v203\n"
    "v_mfma_f32_16x16x32_bf16 v[208:211], v[212:215], v[244:247], v[208:211]\n"
    "v_sub_f32 v52, s70, v203\n"
    "v_sub_f32 v53, s70, v203\n"
    "v_sub_f32 v54, s70, v203\n"
    "v_sub_f32 v55, s70, v203\n"
    "v_mfma_f32_32x32x16_bf16 v[144:159], v[124:127], v[244:247], v[144:159]\n"
    "v_sub_f32 v56, s70, v203\n"
    "v_sub_f32 v57, s70, v203\n"
    "v_sub_f32 v58, s70, v203\n"
    "v_sub_f32 v59, s70, v203\n"
    "v_sub_f32 v60, s70, v203\n"
    "v_sub_f32 v61, s70, v203\n"
    "v_sub_f32 v62, s70, v203\n"
    "v_sub_f32 v63, s70, v203\n"
    "s_waitcnt vmcnt(6)\n"
    "v_cmp_ne_u64 vcc, s[62:63], v[160:161]\n"
    "s_cbranch_vccnz Lclrr_23\n"
    "Lclrb_24:\n"
    "s_barrier\n"
    "v_add_f32 v204, v200, v208\n"
    "v_cmp_nge_f32 vcc, 0x4e800000, v204\n"
    "s_cbranch_vccnz Lslow_13\n"
    "Lslowb_14:\n"
    "v_rcp_f32 v204, v204\n"
    "v_mfma_f32_32x32x16_bf16 v[16:31], v[64:67], v[180:183], v[48:63]\n"
    "v_add_f32 v184, v184, v128\n"
    "v_add_f32 v185, v185, v129\n"
    "v_add_f32 v186, v186, v130\n"
    "v_add_f32 v187, v187, v131\n"
    "v_add_f32 v188, v188, v132\n"
    "v_add_f32 v189, v189, v133\n"
    "v_add_f32 v190, v190, v134\n"
    "v_add_f32 v191, v191, v135\n"
    "s_add_u32 s75, s55, 0\n"
    "v_mfma_f32_32x32x16_bf16 v[32:47], v[68:71], v[180:183], v[48:63]\n"
    "v_add_u32 v2, s42, v14\n"
    "v_add_u32 v3, 0x10000, v2\n"
    "ds_read_b64_tr_b16 a[80:81], v2 offset:16384\n"
    "ds_read_b64_tr_b16 a[82:83], v2 offset:17408\n"
    "ds_read_b64_tr_b16 a[84:85], v3 offset:0\n"
    "ds_read_b64_tr_b16 a[86:87], v3 offset:1024\n"
    "ds_read_b64_tr_b16 a[88:89], v3 offset:16384\n"
    "ds_read_b64_tr_b16 a[90:91], v3 offset:17408\n"
    "v_pk_add_f32 v[184:185], v[184:185], v[152:153]\n"
    "v_pk_add_f32 v[186:187], v[186:187], v[154:155]\n"
    "v_pk_add_f32 v[188:189], v[188:189], v[156:157]\n"
    "v_pk_add_f32 v[190:191], v[190:191], v[158:159]\n"
    "v_pk_mul_f32 v[184:185], v[184:185], v[204:205] op_sel_hi:[1,0]\n"
    "v_pk_mul_f32 v[186:187], v[186:187], v[204:205] op_sel_hi:[1,0]\n"
    "v_pk_mul_f32 v[188:189], v[188:189], v[204:205] op_sel_hi:[1,0]\n"
    "v_pk_mul_f32 v[190:191], v[190:191], v[204:205] op_sel_hi:[1,0]\n"
    "v_cvt_pk_bf16_f32 v184, v184, v185\n"
    "v_cvt_pk_bf16_f32 v185, v186, v187\n"
    "v_cvt_pk_bf16_f32 v186, v188, v189\n"
    "v_cvt_pk_bf16_f32 v187, v190, v191\n"
    "v_min_u32 v184, -2, v184\n"
    "v_min_u32 v185, -2, v185\n"
    "v_min_u32 v186, -2, v186\n"
    "v_min_u32 v187, -2, v187\n"
    "buffer_store_dwordx4 v[184:187], v10, s[16:19], s75 offen sc1\n"
    "ds_read_b64_tr_b16 a[92:93], v3 offset:32768\n"
    "ds_read_b64_tr_b16 a[94:95], v3 offset:33792\n"
    "v_add_u32 v2, 0x1000, v2\n"
    "v_and_b32 v2, 0x3fff, v2\n"
    "v_add_u32 v3, 0x10000, v2\n"
    "ds_read_b64_tr_b16 a[112:113], v2 offset:16384\n"
    "ds_read_b64_tr_b16 a[114:115], v2 offset:17408\n"
    "ds_read_b64_tr_b16 a[116:117], v3 offset:0\n"
    "ds_read_b64_tr_b16 a[118:119], v3 offset:1024\n"
    "ds_read_b64_tr_b16 a[120:121], v3 offset:16384\n"
    "ds_read_b64_tr_b16 a[122:123], v3 offset:17408\n"
    "ds_read_b64_tr_b16 a[124:125], v3 offset:32768\n"
    "ds_read_b64_tr_b16 a[126:127], v3 offset:33792\n"
    "v_add_u32 v2, 0x1000, v2\n"
    "v_and_b32 v2, 0x3fff, v2\n"
    "v_add_u32 v3, 0x10000, v2\n"
    "ds_read_b64_tr_b16 a[144:145], v2 offset:16384\n"
    "ds_read_b64_tr_b16 a[146:147], v2 offset:17408\n"
    "ds_read_b64_tr_b16 a[148:149], v3 offset:0\n"
    "ds_read_b64_tr_b16 a[150:151], v3 offset:1024\n"
    "ds_read_b64_tr_b16 a[152:153], v3 offset:16384\n"
    "ds_read_b64_tr_b16 a[154:155], v3 offset:17408\n"
    "ds_read_b64_tr_b16 a[156:157], v3 offset:32768\n"
    "ds_read_b64_tr_b16 a[158:159], v3 offset:33792\n"
    "v_add_u32 v2, 0x1000, v2\n"
    "v_and_b32 v2, 0x3fff, v2\n"
    "v_add_u32 v3, 0x10000, v2\n"
    "ds_read_b64_tr_b16 a[176:177], v2 offset:16384\n"
    "ds_read_b64_tr_b16 a[178:179], v2 offset:17408\n"
    "ds_read_b64_tr_b16 a[180:181], v3 offset:0\n"
    "ds_read_b64_tr_b16 a[182:183], v3 offset:1024\n"
    "ds_read_b64_tr_b16 a[184:185], v3 offset:16384\n"
    "ds_read_b64_tr_b16 a[186:187], v3 offset:17408\n"
    "ds_read_b64_tr_b16 a[188:189], v3 offset:32768\n"
    "ds_read_b64_tr_b16 a[190:191], v3 offset:33792\n"
    "v_exp_f32 v16, v16\n"
    "v_add_u32 v206, 0x10000, v11\n"
    "v_add_u32 v207, 0x10000, v12\n"
    "s_lshr_b32 s68, s42, 2\n"
    "s_add_u32 m0, s68, 0x24000\n"
    "v_lshlrev_b32 v173, 4, v4\n"
    "v_xor_b32 v173, v173, v13\n"
    "buffer_load_dwordx4 v173, s[4:7], s58 offen lds\n"
    "s_add_u32 s71, s58, 32768\n"
    "s_add_u32 m0, s68, 0x27000\n"
    "s_nop 0\n"
    "buffer_load_dwordx4 v173, s[4:7], s71 offen lds\n"
    "v_exp_f32 v17, v17\n"
    "v_exp_f32 v18, v18\n"
    "v_exp_f32 v19, v19\n"
    "v_cvt_pk_bf16_f32 v216, v16, v17\n"
    "v_exp_f32 v20, v20\n"
    "v_exp_f32 v21, v21\n"
    "v_cvt_pk_bf16_f32 v217, v18, v19\n"
    "v_and_b32 v5, 31, v1\n"
    "v_lshlrev_b32 v5, 1, v5\n"
    "buffer_load_ushort v5, v5, s[12:15], s95 offen\n"
    "s_waitcnt lgkmcnt(0)\n"
    "s_barrier\n"
    "s_add_u32 s72, s61, 8192\n"
    "v_mov_b32 v174, s62\n"
    "v_mov_b32 v175, s63\n"
    "v_exp_f32 v22, v22\n"
    "v_exp_f32 v23, v23\n"
    "v_cvt_pk_bf16_f32 v218, v20, v21\n"
    "v_exp_f32 v24, v24\n"
    "v_exp_f32 v25, v25\n"
    "v_cvt_pk_bf16_f32 v219, v22, v23\n"
    "s_waitcnt vmcnt(4)\n"
    "s_mov_b64 exec, 1\n"
    "buffer_store_dwordx2 v[174:175], off, s[16:19], s72 sc1\n"
    "s_mov_b64 exec, -1\n"
    "v_exp_f32 v26, v26\n"
    "v_exp_f32 v27, v27\n"
    "s_lshl4_add_u32 s71, s80, s39\n"
    "s_add_u32 m0, s42, 0x0\n"
    "v_cvt_pk_bf16_f32 v220, v24, v25\n"
    "v_exp_f32 v28, v28\n"
    "v_exp_f32 v29, v29\n"
    "v_cvt_pk_bf16_f32 v221, v26, v27\n"
    "v_exp_f32 v30, v30\n"
    "v_exp_f32 v31, v31\n"
    "v_cvt_pk_bf16_f32 v222, v28, v29\n"
    "v_exp_f32 v32, v32\n"
    "v_cvt_pk_bf16_f32 v223, v30, v31\n"
    "v_exp_f32 v33, v33\n"
    "s_cmp_eq_u32 s54, 1\n"
    "s_cbranch_scc1 Lfx_15\n"
    "Lfb_16:\n"
    "v_mfma_f32_32x32x16_bf16 v[16:31], v[72:75], v[180:183], v[48:63]\n"
    "buffer_load_dwordx4 v6, s[32:35], s71 offen offset:0 lds\n"
    "v_exp_f32 v34, v34\n"
    "v_exp_f32 v35, v35\n"
    "v_cvt_pk_bf16_f32 v240, v32, v33\n"
    "v_exp_f32 v36, v36\n"
    "v_exp_f32 v37, v37\n"
    "v_cvt_pk_bf16_f32 v241, v34, v35\n"
    "v_mfma_f32_16x16x32_bf16 v[208:211], v[212:215], v[216:219], 0\n"
    "buffer_load_dwordx4 v7, s[32:35], s71 offen offset:1024 lds\n"
    "v_exp_f32 v38, v38\n"
    "v_exp_f32 v39, v39\n"
    "v_cvt_pk_bf16_f32 v242, v36, v37\n"
    "v_mfma_f32_32x32x16_bf16 v[128:143], v[96:99], v[216:219], 0\n"
    "buffer_load_dwordx4 v8, s[32:35], s71 offen offset:2048 lds\n"
    "v_exp_f32 v40, v40\n"
    "v_exp_f32 v41, v41\n"
    "v_cvt_pk_bf16_f32 v243, v38, v39\n"
    "v_exp_f32 v42, v42\n"
    "v_exp_f32 v43, v43\n"
    "v_cvt_pk_bf16_f32 v244, v40, v41\n"
    "v_mfma_f32_16x16x32_bf16 v[208:211], v[212:215], v[220:223], v[208:211]\n"
    "v_exp_f32 v44, v44\n"
    "v_exp_f32 v45, v45\n"
    "v_cvt_pk_bf16_f32 v245, v42, v43\n"
    "v_mfma_f32_32x32x16_bf16 v[144:159], v[96:99], v[220:223], 0\n"
    "buffer_load_dwordx4 v9, s[32:35], s71 offen offset:3072 lds\n"
    "v_exp_f32 v46, v46\n"
    "v_exp_f32 v47, v47\n"
    "v_cvt_pk_bf16_f32 v246, v44, v45\n"
    "v_exp_f32 v16, v16\n"
    "v_cvt_pk_bf16_f32 v247, v46, v47\n"
    "v_exp_f32 v17, v17\n"
    "v_mfma_f32_32x32x16_bf16 v[32:47], v[76:79], v[180:183], v[48:63]\n"
    "s_lshl4_add_u32 s71, s82, s39\n"
    "s_add_u32 m0, s42, 0x8000\n"
    "v_exp_f32 v18, v18\n"
    "v_exp_f32 v19, v19\n"
    "v_cvt_pk_bf16_f32 v216, v16, v17\n"
    "v_exp_f32 v20, v20\n"
    "v_exp_f32 v21, v21\n"
    "v_cvt_pk_bf16_f32 v217, v18, v19\n"
    "v_mfma_f32_16x16x32_bf16 v[208:211], v[212:215], v[240:243], v[208:211]\n"
    "buffer_load_dwordx4 v6, s[32:35], s71 offen offset:0 lds\n"
    "v_exp_f32 v22, v22\n"
    "v_exp_f32 v23, v23\n"
    "v_cvt_pk_bf16_f32 v218, v20, v21\n"
    "v_mfma_f32_32x32x16_bf16 v[128:143], v[100:103], v[240:243], v[128:143]\n"
    "buffer_load_dwordx4 v7, s[32:35], s71 offen offset:1024 lds\n"
    "v_exp_f32 v24, v24\n"
    "v_exp_f32 v25, v25\n"
    "v_cvt_pk_bf16_f32 v219, v22, v23\n"
    "v_exp_f32 v26, v26\n"
    "v_exp_f32 v27, v27\n"
    "v_cvt_pk_bf16_f32 v220, v24, v25\n"
    "v_mfma_f32_16x16x32_bf16 v[208:211], v[212:215], v[244:247], v[208:211]\n"
    "buffer_load_dwordx4 v8, s[32:35], s71 offen offset:2048 lds\n"
    "v_exp_f32 v28, v28\n"
    "v_exp_f32 v29, v29\n"
    "v_cvt_pk_bf16_f32 v221, v26, v27\n"
    "v_mfma_f32_32x32x16_bf16 v[144:159], v[100:103], v[244:247], v[144:159]\n"
    "v_exp_f32 v30, v30\n"
    "v_exp_f32 v31, v31\n"
    "v_cvt_pk_bf16_f32 v222, v28, v29\n"
    "v_exp_f32 v32, v32\n"
    "v_cvt_pk_bf16_f32 v223, v30, v31\n"
    "v_exp_f32 v33, v33\n"
    "s_cmp_eq_u32 s54, 2\n"
    "s_cbranch_scc1 Lfx_17\n"
    "Lfb_18:\n"
    "v_mfma_f32_32x32x16_bf16 v[16:31], v[80:83], v[180:183], v[48:63]\n"
    "buffer_load_dwordx4 v9, s[32:35], s71 offen offset:3072 lds\n"
    "v_exp_f32 v34, v34\n"
    "v_exp_f32 v35, v35\n"
    "v_cvt_pk_bf16_f32 v240, v32, v33\n"
    "v_exp_f32 v36, v36\n"
    "v_exp_f32 v37, v37\n"
    "v_cvt_pk_bf16_f32 v241, v34, v35\n"
    "v_mfma_f32_16x16x32_bf16 v[208:211], v[212:215], v[216:219], v[208:211]\n"
    "buffer_load_dwordx4 v[164:167], v0, s[16:19], s45 offen sc1\n"
    "v_exp_f32 v38, v38\n"
    "v_exp_f32 v39, v39\n"
    "v_cvt_pk_bf16_f32 v242, v36, v37\n"
    "v_mfma_f32_32x32x16_bf16 v[128:143], v[104:107], v[216:219], v[128:143]\n"
    "v_exp_f32 v40, v40\n"
    "v_exp_f32 v41, v41\n"
    "v_cvt_pk_bf16_f32 v243, v38, v39\n"
    "v_exp_f32 v42, v42\n"
    "v_exp_f32 v43, v43\n"
    "v_cvt_pk_bf16_f32 v244, v40, v41\n"
    "v_mfma_f32_16x16x32_bf16 v[208:211], v[212:215], v[220:223], v[208:211]\n"
    "buffer_load_dwordx4 v[168:171], v0, s[16:19], s45 offen offset:1024 sc1\n"
    "v_exp_f32 v44, v44\n"
    "v_exp_f32 v45, v45\n"
    "v_cvt_pk_bf16_f32 v245, v42, v43\n"
    "v_mfma_f32_32x32x16_bf16 v[144:159], v[104:107], v[220:223], v[144:159]\n"
    "v_exp_f32 v46, v46\n"
    "v_exp_f32 v47, v47\n"
    "v_cvt_pk_bf16_f32 v246, v44, v45\n"
    "v_exp_f32 v16, v16\n"
    "v_cvt_pk_bf16_f32 v247, v46, v47\n"
    "v_exp_f32 v17, v17\n"
    "v_mfma_f32_32x32x16_bf16 v[32:47], v[84:87], v[180:183], v[48:63]\n"
    "v_exp_f32 v18, v18\n"
    "v_exp_f32 v19, v19\n"
    "v_cvt_pk_bf16_f32 v216, v16, v17\n"
    "v_exp_f32 v20, v20\n"
    "v_exp_f32 v21, v21\n"
    "v_cvt_pk_bf16_f32 v217, v18, v19\n"
    "v_mfma_f32_16x16x32_bf16 v[208:211], v[212:215], v[240:243], v[208:211]\n"
    "s_add_u32 s71, s41, 0\n"
    "s_add_u32 m0, s42, 0x1c000\n"
    "v_exp_f32 v22, v22\n"
    "v_exp_f32 v23, v23\n"
    "v_cvt_pk_bf16_f32 v218, v20, v21\n"
    "v_mfma_f32_32x32x16_bf16 v[128:143], v[108:111], v[240:243], v[128:143]\n"
    "buffer_load_dwordx4 v10, s[16:19], s71 offen offset:0 sc1 lds\n"
    "v_exp_f32 v24, v24\n"
    "v_exp_f32 v25, v25\n"
    "v_cvt_pk_bf16_f32 v219, v22, v23\n"
    "v_exp_f32 v26, v26\n"
    "v_exp_f32 v27, v27\n"
    "v_cvt_pk_bf16_f32 v220, v24, v25\n"
    "v_mfma_f32_16x16x32_bf16 v[208:211], v[212:215], v[244:247], v[208:211]\n"
    "buffer_load_dwordx4 v10, s[16:19], s71 offen offset:1024 sc1 lds\n"
    "v_exp_f32 v28, v28\n"
    "v_exp_f32 v29, v29\n"
    "v_cvt_pk_bf16_f32 v221, v26, v27\n"
    "v_mfma_f32_32x32x16_bf16 v[144:159], v[108:111], v[244:247], v[144:159]\n"
    "buffer_load_dwordx4 v10, s[16:19], s71 offen offset:2048 sc1 lds\n"
    "v_exp_f32 v30, v30\n"
    "v_exp_f32 v31, v31\n"
    "v_cvt_pk_bf16_f32 v222, v28, v29\n"
    "v_exp_f32 v32, v32\n"
    "v_cvt_pk_bf16_f32 v223, v30, v31\n"
    "v_exp_f32 v33, v33\n"
    "buffer_load_dwordx4 v10, s[16:19], s71 offen offset:3072 sc1 lds\n"
    "s_cmp_eq_u32 s54, 3\n"
    "s_cbranch_scc1 Lfx_19\n"
    "Lfb_20:\n"
    "v_min_f32 v16, -v224, v48\n"
    "v_min_f32 v17, -v225, v49\n"
    "v_min_f32 v18, -v226, v50\n"
    "s_add_u32 s71, s41, 4096\n"
    "s_add_u32 m0, s42, 0x20000\n"
    "v_min_f32 v19, -v227, v51\n"
    "v_min_f32 v20, -v228, v52\n"
    "v_min_f32 v21, -v229, v53\n"
    "v_min_f32 v22, -v230, v54\n"
    "v_min_f32 v23, -v231, v55\n"
    "v_min_f32 v24, -v232, v56\n"
    "v_min_f32 v25, -v233, v57\n"
    "v_min_f32 v26, -v234, v58\n"
    "v_min_f32 v27, -v235, v59\n"
    "v_min_f32 v28, -v236, v60\n"
    "v_min_f32 v29, -v237, v61\n"
    "v_min_f32 v30, -v238, v62\n"
    "v_min_f32 v31, -v239, v63\n"
    "s_nop 1\n"
    "v_mfma_f32_32x32x16_bf16 v[16:31], v[88:91], v[180:183], v[16:31]\n"
    "buffer_load_dwordx4 v10, s[16:19], s71 offen offset:0 sc1 lds\n"
    "v_exp_f32 v34, v34\n"
    "v_exp_f32 v35, v35\n"
    "v_cvt_pk_bf16_f32 v240, v32, v33\n"
    "v_exp_f32 v36, v36\n"
    "v_exp_f32 v37, v37\n"
    "v_cvt_pk_bf16_f32 v241, v34, v35\n"
    "v_mfma_f32_16x16x32_bf16 v[208:211], v[212:215], v[216:219], v[208:211]\n"
    "buffer_load_dwordx4 v10, s[16:19], s71 offen offset:1024 sc1 lds\n"
    "v_exp_f32 v38, v38\n"
    "v_exp_f32 v39, v39\n"
    "v_cvt_pk_bf16_f32 v242, v36, v37\n"
    "v_mfma_f32_32x32x16_bf16 v[128:143], v[112:115], v[216:219], v[128:143]\n"
    "v_exp_f32 v40, v40\n"
    "v_exp_f32 v41, v41\n"
    "v_cvt_pk_bf16_f32 v243, v38, v39\n"
    "v_exp_f32 v42, v42\n"
    "v_exp_f32 v43, v43\n"
    "v_cvt_pk_bf16_f32 v244, v40, v41\n"
    "v_mfma_f32_16x16x32_bf16 v[208:211], v[212:215], v[220:223], v[208:211]\n"
    "buffer_load_dwordx4 v10, s[16:19], s71 offen offset:2048 sc1 lds\n"
    "v_exp_f32 v44, v44\n"
    "v_exp_f32 v45, v45\n"
    "v_cvt_pk_bf16_f32 v245, v42, v43\n"
    "v_mfma_f32_32x32x16_bf16 v[144:159], v[112:115], v[220:223], v[144:159]\n"
    "buffer_load_dwordx4 v10, s[16:19], s71 offen offset:3072 sc1 lds\n"
    "s_waitcnt vmcnt(9)\n"
    "v_xor_b32 v165, s62, v165\n"
    "v_xor_b32 v167, s62, v167\n"
    "s_waitcnt vmcnt(8)\n"
    "v_xor_b32 v169, s62, v169\n"
    "v_xor_b32 v171, s62, v171\n"
    "v_or3_b32 v172, v165, v167, v169\n"
    "v_or_b32 v172, v172, v171\n"
    "v_cmp_ne_u32 vcc, 0, v172\n"
    "s_cbranch_vccnz Lgatr_25\n"
    "Lgatb_26:\n"
    "s_mul_i32 s75, s52, 2080\n"
    "s_add_u32 s75, s75, 0x25000\n"
    "v_lshl_add_u32 v172, v1, 3, s75\n"
    "v_mov_b32 v165, v166\n"
    "v_mov_b32 v169, v170\n"
    "ds_write2_b64 v172, v[164:165], v[168:169] offset0:0 offset1:64\n"
    "v_exp_f32 v46, v46\n"
    "s_lshl4_add_u32 s71, s78, s39\n"
    "s_add_u32 m0, s42, 0x14000\n"
    "s_nop 0\n"
    "buffer_load_dwordx4 v6, s[32:35], s71 offen offset:0 lds\n"
    "v_exp_f32 v47, v47\n"
    "v_cvt_pk_bf16_f32 v246, v44, v45\n"
    "v_exp_f32 v16, v16\n"
    "v_cvt_pk_bf16_f32 v247, v46, v47\n"
    "v_exp_f32 v17, v17\n"
    "v_exp_f32 v18, v18\n"
    "v_exp_f32 v19, v19\n"
    "v_cvt_pk_bf16_f32 v216, v16, v17\n"
    "v_exp_f32 v20, v20\n"
    "v_exp_f32 v21, v21\n"
    "v_cvt_pk_bf16_f32 v217, v18, v19\n"
    "v_mfma_f32_16x16x32_bf16 v[208:211], v[212:215], v[240:243], v[208:211]\n"
    "buffer_load_dwordx4 v7, s[32:35], s71 offen offset:1024 lds\n"
    "v_exp_f32 v22, v22\n"
    "v_exp_f32 v23, v23\n"
    "v_cvt_pk_bf16_f32 v218, v20, v21\n"
    "v_mfma_f32_32x32x16_bf16 v[128:143], v[116:119], v[240:243], v[128:143]\n"
    "v_exp_f32 v24, v24\n"
    "v_exp_f32 v25, v25\n"
    "v_cvt_pk_bf16_f32 v219, v22, v23\n"
    "v_exp_f32 v26, v26\n"
    "v_exp_f32 v27, v27\n"
    "v_cvt_pk_bf16_f32 v220, v24, v25\n"
    "v_mfma_f32_16x16x32_bf16 v[208:211], v[212:215], v[244:247], v[208:211]\n"
    "buffer_load_dwordx4 v8, s[32:35], s71 offen offset:2048 lds\n"
    "v_exp_f32 v28, v28\n"
    "v_exp_f32 v29, v29\n"
    "v_cvt_pk_bf16_f32 v221, v26, v27\n"
    "v_mfma_f32_32x32x16_bf16 v[144:159], v[116:119], v[244:247], v[144:159]\n"
    "buffer_load_dwordx4 v9, s[32:35], s71 offen offset:3072 lds\n"
    "v_exp_f32 v30, v30\n"
    "v_exp_f32 v31, v31\n"
    "s_lshl4_add_u32 s71, s79, s39\n"
    "s_add_u32 m0, s42, 0x18000\n"
    "v_cvt_pk_bf16_f32 v222, v28, v29\n"
    "v_mfma_f32_32x32x16_bf16 v[128:143], v[120:123], v[216:219], v[128:143]\n"
    "v_cvt_pk_bf16_f32 v223, v30, v31\n"
    "v_mfma_f32_16x16x32_bf16 v[208:211], v[212:215], v[216:219], v[208:211]\n"
    "v_and_b32 v252, 15, v1\n"
    "v_lshrrev_b32 v253, 4, v1\n"
    "v_lshlrev_b32 v254, 4, v252\n"
    "v_mfma_f32_16x16x32_bf16 v[208:211], v[212:215], v[220:223], v[208:211]\n"
    "v_lshl_add_u32 v254, v253, 9, v254\n"
    "v_add_u32 v254, s42, v254\n"
    "v_add_u32 v248, 0x1c000, v254\n"
    "v_mfma_f32_32x32x16_bf16 v[144:159], v[120:123], v[220:223], v[144:159]\n"
    "v_add_u32 v254, 0x1000, v254\n"
    "v_and_b32 v254, 0x3fff, v254\n"
    "v_add_u32 v249, 0x1c000, v254\n"
    "v_add_u32 v254, 0x1000, v254\n"
    "v_and_b32 v254, 0x3fff, v254\n"
    "v_add_u32 v250, 0x1c000, v254\n"
    "v_add_u32 v254, 0x1000, v254\n"
    "v_and_b32 v254, 0x3fff, v254\n"
    "v_add_u32 v251, 0x1c000, v254\n"
    "v_add_f32 v204, v201, v208\n"
    "v_cmp_nge_f32 vcc, 0x4e800000, v204\n"
    "s_cbranch_vccnz Lslow_21\n"
    "Lslowb_22:\n"
    "v_rcp_f32 v204, v204\n"
    "s_waitcnt vmcnt(4)\n"
    "ds_read_b128 v[64:67], v248 offset:0\n"
    "ds_read_b128 v[68:71], v248 offset:256\n"
    "ds_read_b128 v[72:75], v248 offset:2048\n"
    "ds_read_b128 v[76:79], v248 offset:2304\n"
    "ds_read_b128 v[80:83], v248 offset:16384\n"
    "ds_read_b128 v[84:87], v248 offset:16640\n"
    "ds_read_b128 v[88:91], v248 offset:18432\n"
    "ds_read_b128 v[92:95], v248 offset:18688\n"
    "v_add_f32 v192, v192, v128\n"
    "v_add_f32 v193, v193, v129\n"
    "v_add_f32 v194, v194, v130\n"
    "v_add_f32 v195, v195, v131\n"
    "v_add_f32 v196, v196, v132\n"
    "v_add_f32 v197, v197, v133\n"
    "v_add_f32 v198, v198, v134\n"
    "v_add_f32 v199, v199, v135\n"
    "s_add_u32 s75, s55, 32768\n"
    "v_pk_add_f32 v[192:193], v[192:193], v[152:153]\n"
    "v_pk_add_f32 v[194:195], v[194:195], v[154:155]\n"
    "v_pk_add_f32 v[196:197], v[196:197], v[156:157]\n"
    "v_pk_add_f32 v[198:199], v[198:199], v[158:159]\n"
    "v_pk_mul_f32 v[192:193], v[192:193], v[204:205] op_sel_hi:[1,0]\n"
    "v_pk_mul_f32 v[194:195], v[194:195], v[204:205] op_sel_hi:[1,0]\n"
    "v_pk_mul_f32 v[196:197], v[196:197], v[204:205] op_sel_hi:[1,0]\n"
    "v_pk_mul_f32 v[198:199], v[198:199], v[204:205] op_sel_hi:[1,0]\n"
    "v_cvt_pk_bf16_f32 v192, v192, v193\n"
    "v_cvt_pk_bf16_f32 v193, v194, v195\n"
    "v_cvt_pk_bf16_f32 v194, v196, v197\n"
    "v_cvt_pk_bf16_f32 v195, v198, v199\n"
    "v_min_u32 v192, -2, v192\n"
    "v_min_u32 v193, -2, v193\n"
    "v_min_u32 v194, -2, v194\n"
    "v_min_u32 v195, -2, v195\n"
    "buffer_store_dwordx4 v[192:195], v10, s[16:19], s75 offen sc1\n"
    "s_waitcnt lgkmcnt(7)\n"
    "v_max3_u32 v255, v64, v65, v66\n"
    "s_waitcnt lgkmcnt(6)\n"
    "v_max3_u32 v255, v255, v67, v68\n"
    "v_max3_u32 v255, v255, v69, v70\n"
    "s_waitcnt lgkmcnt(5)\n"
    "v_max3_u32 v255, v255, v71, v72\n"
    "v_max3_u32 v255, v255, v73, v74\n"
    "s_waitcnt lgkmcnt(4)\n"
    "v_max3_u32 v255, v255, v75, v76\n"
    "v_max3_u32 v255, v255, v77, v78\n"
    "s_waitcnt lgkmcnt(3)\n"
    "v_max3_u32 v255, v255, v79, v80\n"
    "v_max3_u32 v255, v255, v81, v82\n"
    "s_waitcnt lgkmcnt(2)\n"
    "v_max3_u32 v255, v255, v83, v84\n"
    "v_max3_u32 v255, v255, v85, v86\n"
    "s_waitcnt lgkmcnt(1)\n"
    "v_max3_u32 v255, v255, v87, v88\n"
    "v_max3_u32 v255, v255, v89, v90\n"
    "s_waitcnt lgkmcnt(0)\n"
    "v_max3_u32 v255, v255, v91, v92\n"
    "v_max3_u32 v255, v255, v93, v94\n"
    "v_max_u32 v255, v255, v95\n"
    "v_cmp_eq_u32 vcc, -1, v255\n"
    "s_cbranch_vccnz Lo0r_27\n"
    "Lo0b_28:\n"
    "s_barrier\n"
    "v_mfma_f32_16x16x32_bf16 v[48:51], a[192:195], v[64:67], 0\n"
    "ds_read_b128 v[96:99], v249 offset:0\n"
    "buffer_load_dwordx4 v6, s[32:35], s71 offen offset:0 lds\n"
    "v_mfma_f32_16x16x32_bf16 v[52:55], a[192:195], v[68:71], 0\n"
    "ds_read_b128 v[100:103], v249 offset:256\n"
    "s_and_b32 s75, s58, 0x3ff\n"
    "v_mfma_f32_16x16x32_bf16 v[48:51], a[196:199], v[72:75], v[48:51]\n"
    "ds_read_b128 v[104:107], v249 offset:2048\n"
    "s_lshr_b32 s74, s42, 7\n"
    "v_mfma_f32_16x16x32_bf16 v[52:55], a[196:199], v[76:79], v[52:55]\n"
    "ds_read_b128 v[108:111], v249 offset:2304\n"
    "s_add_u32 s75, s75, s74\n"
    "v_mfma_f32_16x16x32_bf16 v[48:51], a[200:203], v[80:83], v[48:51]\n"
    "ds_read_b128 v[64:67], v249 offset:16384\n"
    "buffer_load_dwordx4 v7, s[32:35], s71 offen offset:1024 lds\n"
    "v_mfma_f32_16x16x32_bf16 v[52:55], a[200:203], v[84:87], v[52:55]\n"
    "ds_read_b128 v[68:71], v249 offset:16640\n"
    "s_add_u32 s75, s75, 0x25000\n"
    "v_mfma_f32_16x16x32_bf16 v[48:51], a[204:207], v[88:91], v[48:51]\n"
    "ds_read_b128 v[72:75], v249 offset:18432\n"
    "v_lshl_add_u32 v2, v253, 3, s75\n"
    "v_mfma_f32_16x16x32_bf16 v[52:55], a[204:207], v[92:95], v[52:55]\n"
    "ds_read_b128 v[76:79], v249 offset:18688\n"
    "s_waitcnt lgkmcnt(4)\n"
    "v_mfma_f32_16x16x32_bf16 v[48:51], a[208:211], v[96:99], v[48:51]\n"
    "ds_read_b128 v[80:83], v250 offset:0\n"
    "v_and_b32 v3, 1, v1\n"
    "v_mfma_f32_16x16x32_bf16 v[52:55], a[208:211], v[100:103], v[52:55]\n"
    "ds_read_b128 v[84:87], v250 offset:256\n"
    "buffer_load_dwordx4 v8, s[32:35], s71 offen offset:2048 lds\n"
    "v_mfma_f32_16x16x32_bf16 v[48:51], a[212:215], v[104:107], v[48:51]\n"
    "ds_read_b128 v[88:91], v250 offset:2048\n"
    "v_mul_u32_u24 v3, 2080, v3\n"
    "v_mfma_f32_16x16x32_bf16 v[52:55], a[212:215], v[108:111], v[52:55]\n"
    "ds_read_b128 v[92:95], v250 offset:2304\n"
    "s_waitcnt lgkmcnt(4)\n"
    "v_mfma_f32_16x16x32_bf16 v[48:51], a[216:219], v[64:67], v[48:51]\n"
    "ds_read_b128 v[96:99], v250 offset:16384\n"
    "v_lshrrev_b32 v254, 5, v1\n"
    "v_mfma_f32_16x16x32_bf16 v[52:55], a[216:219], v[68:71], v[52:55]\n"
    "ds_read_b128 v[100:103], v250 offset:16640\n"
    "buffer_load_dwordx4 v9, s[32:35], s71 offen offset:3072 lds\n"
    "v_mfma_f32_16x16x32_bf16 v[48:51], a[220:223], v[72:75], v[48:51]\n"
    "ds_read_b128 v[104:107], v250 offset:18432\n"
    "s_lshl4_add_u32 s71, s77, s39\n"
    "v_mfma_f32_16x16x32_bf16 v[52:55], a[220:223], v[76:79], v[52:55]\n"
    "ds_read_b128 v[108:111], v250 offset:18688\n"
    "s_waitcnt lgkmcnt(4)\n"
    "v_mfma_f32_16x16x32_bf16 v[48:51], a[224:227], v[80:83], v[48:51]\n"
    "ds_read_b128 v[64:67], v251 offset:0\n"
    "s_add_u32 m0, s42, 0x10000\n"
    "v_mfma_f32_16x16x32_bf16 v[52:55], a[224:227], v[84:87], v[52:55]\n"
    "ds_read_b128 v[68:71], v251 offset:256\n"
    "v_lshl_add_u32 v3, v254, 4, v3\n"
    "v_mfma_f32_16x16x32_bf16 v[48:51], a[228:231], v[88:91], v[48:51]\n"
    "ds_read_b128 v[72:75], v251 offset:2048\n"
    "buffer_load_dwordx4 v6, s[32:35], s71 offen offset:0 lds\n"
    "v_mfma_f32_16x16x32_bf16 v[52:55], a[228:231], v[92:95], v[52:55]\n"
    "ds_read_b128 v[76:79], v251 offset:2304\n"
    "s_waitcnt lgkmcnt(4)\n"
    "v_mfma_f32_16x16x32_bf16 v[48:51], a[232:235], v[96:99], v[48:51]\n"
    "ds_read_b128 v[80:83], v251 offset:16384\n"
    "s_lshr_b32 s73, s42, 4\n"
    "v_mfma_f32_16x16x32_bf16 v[52:55], a[232:235], v[100:103], v[52:55]\n"
    "ds_read_b128 v[84:87], v251 offset:16640\n"
    "s_add_u32 s72, s73, 0x25820\n"
    "v_mfma_f32_16x16x32_bf16 v[48:51], a[236:239], v[104:107], v[48:51]\n"
    "ds_read_b128 v[88:91], v251 offset:18432\n"
    "buffer_load_dwordx4 v7, s[32:35], s71 offen offset:1024 lds\n"
    "v_mfma_f32_16x16x32_bf16 v[52:55], a[236:239], v[108:111], v[52:55]\n"
    "ds_read_b128 v[92:95], v251 offset:18688\n"
    "s_waitcnt lgkmcnt(4)\n"
    "v_mfma_f32_16x16x32_bf16 v[48:51], a[240:243], v[64:67], v[48:51]\n"
    "ds_read_b64 v[240:241], v2\n"
    "v_add_u32 v3, s72, v3\n"
    "v_mfma_f32_16x16x32_bf16 v[52:55], a[240:243], v[68:71], v[52:55]\n"
    "ds_read_b64 v[242:243], v2 offset:4160\n"
    "v_lshl_add_u32 v2, v253, 3, s74\n"
    "v_mfma_f32_16x16x32_bf16 v[48:51], a[244:247], v[72:75], v[48:51]\n"
    "v_and_b32 v14, 0x70, v0\n"
    "v_xor_b32 v2, v2, v14\n"
    "v_mfma_f32_16x16x32_bf16 v[52:55], a[244:247], v[76:79], v[52:55]\n"
    "s_waitcnt lgkmcnt(2)\n"
    "buffer_load_dwordx4 v8, s[32:35], s71 offen offset:2048 lds\n"
    "v_mfma_f32_16x16x32_bf16 v[48:51], a[248:251], v[80:83], v[48:51]\n"
    "v_lshl_add_u32 v2, v252, 7, v2\n"
    "v_add_u32 v2, 0x24000, v2\n"
    "v_mfma_f32_16x16x32_bf16 v[52:55], a[248:251], v[84:87], v[52:55]\n"
    "ds_read_b64 v[244:245], v2\n"
    "v_mfma_f32_16x16x32_bf16 v[48:51], a[252:255], v[88:91], v[48:51]\n"
    "ds_read_b64 v[246:247], v2 offset:2048\n"
    "s_waitcnt lgkmcnt(3)\n"
    "v_lshlrev_b32 v208, 16, v240\n"
    "v_mfma_f32_16x16x32_bf16 v[52:55], a[252:255], v[92:95], v[52:55]\n"
    "buffer_load_dwordx4 v9, s[32:35], s71 offen offset:3072 lds\n"
    "v_and_b32 v209, s37, v240\n"
    "v_lshlrev_b32 v210, 16, v241\n"
    "v_and_b32 v211, s37, v241\n"
    "s_waitcnt lgkmcnt(2)\n"
    "v_lshlrev_b32 v212, 16, v242\n"
    "v_and_b32 v213, s37, v242\n"
    "v_lshlrev_b32 v214, 16, v243\n"
    "v_and_b32 v215, s37, v243\n"
    "s_waitcnt lgkmcnt(1)\n"
    "v_lshlrev_b32 v224, 16, v244\n"
    "v_and_b32 v225, s37, v244\n"
    "v_lshlrev_b32 v226, 16, v245\n"
    "v_and_b32 v227, s37, v245\n"
    "s_waitcnt lgkmcnt(0)\n"
    "v_lshlrev_b32 v228, 16, v246\n"
    "v_and_b32 v229, s37, v246\n"
    "v_lshlrev_b32 v230, 16, v247\n"
    "v_and_b32 v231, s37, v247\n"
    "ds_read_b128 v[32:35], v3 offset:0\n"
    "ds_read_b128 v[36:39], v3 offset:32\n"
    "ds_read_b128 v[40:43], v3 offset:64\n"
    "ds_read_b128 v[44:47], v3 offset:96\n"
    "ds_read_b128 v[192:195], v3 offset:128\n"
    "ds_read_b128 v[196:199], v3 offset:160\n"
    "s_barrier\n"
    "s_waitcnt lgkmcnt(5)\n"
    "v_mfma_f32_32x32x16_bf16 v[16:31], v[32:35], a[64:67], 0\n"
    "ds_read_b128 v[120:123], v3 offset:192\n"
    "s_add_u32 s69, s69, 8192\n"
    "v_lshlrev_b32 v176, 3, v1\n"
    "buffer_load_dwordx2 v[174:175], v176, s[16:19], s69 offen sc1\n"
    "v_and_b32 v216, 31, v1\n"
    "v_lshlrev_b32 v216, 3, v216\n"
    "s_waitcnt lgkmcnt(5)\n"
    "v_mfma_f32_32x32x16_bf16 v[16:31], v[36:39], a[68:71], v[16:31]\n"
    "ds_read_b128 v[124:127], v3 offset:224\n"
    "s_add_u32 s73, s73, 0x25000\n"
    "v_add_u32 v217, s73, v216\n"
    "v_add_u32 v216, 0x25400, v216\n"
    "v_fmac_f32 v224, v48, v208\n"
    "v_fmac_f32 v225, v49, v209\n"
    "s_waitcnt lgkmcnt(5)\n"
    "v_mfma_f32_32x32x16_bf16 v[16:31], v[40:43], a[72:75], v[16:31]\n"
    "v_add_u32 v3, 0x100, v3\n"
    "v_and_b32 v3, 0xfffffbff, v3\n"
    "ds_read_b128 v[32:35], v3 offset:0\n"
    "v_fmac_f32 v226, v50, v210\n"
    "v_fmac_f32 v227, v51, v211\n"
    "v_fmac_f32 v228, v52, v208\n"
    "s_waitcnt lgkmcnt(5)\n"
    "v_mfma_f32_32x32x16_bf16 v[16:31], v[44:47], a[76:79], v[16:31]\n"
    "ds_read_b128 v[36:39], v3 offset:32\n"
    "s_waitcnt lgkmcnt(2)\n"
    "v_fmac_f32 v229, v53, v209\n"
    "v_fmac_f32 v230, v54, v210\n"
    "v_fmac_f32 v231, v55, v211\n"
    "v_mul_f32 v48, v224, v212\n"
    "v_mfma_f32_32x32x16_bf16 v[16:31], v[192:195], a[80:83], v[16:31]\n"
    "ds_read_b128 v[40:43], v3 offset:64\n"
    "v_mul_f32 v49, v225, v213\n"
    "v_mul_f32 v50, v226, v214\n"
    "v_mul_f32 v51, v227, v215\n"
    "v_mul_f32 v52, v228, v212\n"
    "v_mul_f32 v53, v229, v213\n"
    "v_mfma_f32_32x32x16_bf16 v[16:31], v[196:199], a[84:87], v[16:31]\n"
    "ds_read_b128 v[44:47], v3 offset:96\n"
    "v_mul_f32 v54, v230, v214\n"
    "v_mul_f32 v55, v231, v215\n"
    "v_cvt_pk_bf16_f32 v56, v48, v49\n"
    "v_cvt_pk_bf16_f32 v57, v50, v51\n"
    "v_cvt_pk_bf16_f32 v58, v52, v53\n"
    "v_mfma_f32_32x32x16_bf16 v[16:31], v[120:123], a[88:91], v[16:31]\n"
    "ds_read_b128 v[192:195], v3 offset:128\n"
    "v_cvt_pk_bf16_f32 v59, v54, v55\n"
    "v_min_u32 v56, -2, v56\n"
    "v_min_u32 v58, -2, v58\n"
    "v_min_u32 v57, -2, v57\n"
    "v_min_u32 v59, -2, v59\n"
    "v_mfma_f32_32x32x16_bf16 v[16:31], v[124:127], a[92:95], v[16:31]\n"
    "ds_read_b128 v[196:199], v3 offset:160\n"
    "s_waitcnt lgkmcnt(2)\n"
    "s_add_u32 s75, s57, 0\n"
    "v_permlane16_swap_b32 v56, v58\n"
    "v_mfma_f32_32x32x16_bf16 v[16:31], v[32:35], a[96:99], v[16:31]\n"
    "ds_read_b128 v[120:123], v3 offset:192\n"
    "v_permlane16_swap_b32 v57, v59\n"
    "buffer_store_dwordx4 v[56:59], v10, s[16:19], s75 offen sc1\n"
    "v_add_f32 v60, v224, v225\n"
    "v_mfma_f32_32x32x16_bf16 v[16:31], v[36:39], a[100:103], v[16:31]\n"
    "ds_read_b128 v[124:127], v3 offset:224\n"
    "v_mul_f32 v61, v224, v224\n"
    "v_fmac_f32 v61, v225, v225\n"
    "v_add_f32 v60, v60, v226\n"
    "v_fmac_f32 v61, v226, v226\n"
    "v_add_f32 v60, v60, v227\n"
    "v_mfma_f32_32x32x16_bf16 v[16:31], v[40:43], a[104:107], v[16:31]\n"
    "v_add_u32 v3, 0x100, v3\n"
    "v_and_b32 v3, 0xfffffbff, v3\n"
    "ds_read_b128 v[32:35], v3 offset:0\n"
    "s_waitcnt vmcnt(10)\n"
    "s_add_u32 s71, s41, 32768\n"
    "v_fmac_f32 v61, v227, v227\n"
    "v_mfma_f32_32x32x16_bf16 v[16:31], v[44:47], a[108:111], v[16:31]\n"
    "ds_read_b128 v[36:39], v3 offset:32\n"
    "s_waitcnt lgkmcnt(2)\n"
    "s_add_u32 m0, s42, 0x1c000\n"
    "s_nop 0\n"
    "buffer_load_dwordx4 v10, s[16:19], s71 offen offset:0 sc1 lds\n"
    "v_add_f32 v62, v228, v229\n"
    "v_mul_f32 v63, v228, v228\n"
    "v_mfma_f32_32x32x16_bf16 v[16:31], v[192:195], a[112:115], v[16:31]\n"
    "ds_read_b128 v[40:43], v3 offset:64\n"
    "v_fmac_f32 v63, v229, v229\n"
    "v_add_f32 v62, v62, v230\n"
    "v_fmac_f32 v63, v230, v230\n"
    "v_add_f32 v62, v62, v231\n"
    "v_fmac_f32 v63, v231, v231\n"
    "v_mfma_f32_32x32x16_bf16 v[16:31], v[196:199], a[116:119], v[16:31]\n"
    "ds_read_b128 v[44:47], v3 offset:96\n"
    "buffer_load_dwordx4 v10, s[16:19], s71 offen offset:1024 sc1 lds\n"
    "s_nop 0\n"
    "v_permlane16_swap_b32 v60, v62\n"
    "ds_read_b64_tr_b16 a[0:1], v11 offset:49152\n"
    "v_mfma_f32_32x32x16_bf16 v[16:31], v[120:123], a[120:123], v[16:31]\n"
    "ds_read_b128 v[192:195], v3 offset:128\n"
    "v_permlane16_swap_b32 v61, v63\n"
    "v_add_f32 v60, v60, v62\n"
    "v_add_f32 v61, v61, v63\n"
    "v_mov_b32 v62, v60\n"
    "v_mfma_f32_32x32x16_bf16 v[16:31], v[124:127], a[124:127], v[16:31]\n"
    "ds_read_b128 v[196:199], v3 offset:160\n"
    "s_waitcnt lgkmcnt(3)\n"
    "v_mov_b32 v63, v61\n"
    "s_nop 0\n"
    "v_permlane32_swap_b32 v60, v62\n"
    "v_mfma_f32_32x32x16_bf16 v[16:31], v[32:35], a[128:131], v[16:31]\n"
    "ds_read_b128 v[120:123], v3 offset:192\n"
    "buffer_load_dwordx4 v10, s[16:19], s71 offen offset:2048 sc1 lds\n"
    "v_permlane32_swap_b32 v61, v63\n"
    "v_add_f32 v60, v60, v62\n"
    "v_add_f32 v61, v61, v63\n"
    "v_mfma_f32_32x32x16_bf16 v[16:31], v[36:39], a[132:135], v[16:31]\n"
    "ds_read_b128 v[124:127], v3 offset:224\n"
    "ds_write_b64 v217, v[60:61] offset:1024\n"
    "ds_read_b64 v[244:245], v2 offset:12288\n"
    "v_mfma_f32_32x32x16_bf16 v[16:31], v[40:43], a[136:139], v[16:31]\n"
    "v_add_u32 v3, 0x100, v3\n"
    "v_and_b32 v3, 0xfffffbff, v3\n"
    "ds_read_b128 v[32:35], v3 offset:0\n"
    "buffer_load_dwordx4 v10, s[16:19], s71 offen offset:3072 sc1 lds\n"
    "s_add_u32 s71, s41, 36864\n"
    "v_mfma_f32_32x32x16_bf16 v[16:31], v[44:47], a[140:143], v[16:31]\n"
    "ds_read_b128 v[36:39], v3 offset:32\n"
    "s_waitcnt lgkmcnt(4)\n"
    "s_add_u32 m0, s42, 0x20000\n"
    "ds_read_b64 v[246:247], v2 offset:14336\n"
    "ds_read_b64_tr_b16 a[2:3], v11 offset:50176\n"
    "v_mfma_f32_32x32x16_bf16 v[16:31], v[192:195], a[144:147], v[16:31]\n"
    "ds_read_b128 v[40:43], v3 offset:64\n"
    "ds_read_b64_tr_b16 a[4:5], v12 offset:49152\n"
    "ds_read_b64_tr_b16 a[6:7], v12 offset:50176\n"
    "v_mfma_f32_32x32x16_bf16 v[16:31], v[196:199], a[148:151], v[16:31]\n"
    "ds_read_b128 v[44:47], v3 offset:96\n"
    "buffer_load_dwordx4 v10, s[16:19], s71 offen offset:0 sc1 lds\n"
    "ds_read_b64_tr_b16 a[8:9], v11 offset:51200\n"
    "ds_read_b64_tr_b16 a[10:11], v11 offset:52224\n"
    "v_mfma_f32_32x32x16_bf16 v[16:31], v[120:123], a[152:155], v[16:31]\n"
    "ds_read_b128 v[192:195], v3 offset:128\n"
    "ds_read_b64_tr_b16 a[12:13], v12 offset:51200\n"
    "ds_read_b64_tr_b16 a[14:15], v12 offset:52224\n"
    "v_mfma_f32_32x32x16_bf16 v[16:31], v[124:127], a[156:159], v[16:31]\n"
    "ds_read_b128 v[196:199], v3 offset:160\n"
    "s_waitcnt lgkmcnt(6)\n"
    "buffer_load_dwordx4 v10, s[16:19], s71 offen offset:1024 sc1 lds\n"
    "ds_read_b64_tr_b16 a[32:33], v206 offset:16384\n"
    "ds_read_b64_tr_b16 a[34:35], v206 offset:17408\n"
    "v_mfma_f32_32x32x16_bf16 v[16:31], v[32:35], a[160:163], v[16:31]\n"
    "ds_read_b128 v[120:123], v3 offset:192\n"
    "ds_read_b64_tr_b16 a[36:37], v207 offset:16384\n"
    "ds_read_b64_tr_b16 a[38:39], v207 offset:17408\n"
    "v_mfma_f32_32x32x16_bf16 v[16:31], v[36:39], a[164:167], v[16:31]\n"
    "ds_read_b128 v[124:127], v3 offset:224\n"
    "ds_read_b64_tr_b16 a[40:41], v206 offset:18432\n"
    "ds_read_b64_tr_b16 a[42:43], v206 offset:19456\n"
    "v_mfma_f32_32x32x16_bf16 v[16:31], v[40:43], a[168:171], v[16:31]\n"
    "buffer_load_dwordx4 v10, s[16:19], s71 offen offset:2048 sc1 lds\n"
    "s_barrier\n"
    "ds_read2_b64 v[112:115], v216 offset0:0 offset1:32\n"
    "ds_read2_b64 v[116:119], v216 offset0:64 offset1:96\n"
    "v_mfma_f32_32x32x16_bf16 v[16:31], v[44:47], a[172:175], v[16:31]\n"
    "s_waitcnt lgkmcnt(4)\n"
    "v_lshlrev_b32 v232, 16, v244\n"
    "v_and_b32 v233, s37, v244\n"
    "v_lshlrev_b32 v234, 16, v245\n"
    "v_and_b32 v235, s37, v245\n"
    "v_lshlrev_b32 v236, 16, v246\n"
    "v_mfma_f32_32x32x16_bf16 v[16:31], v[192:195], a[176:179], v[16:31]\n"
    "buffer_load_dwordx4 v10, s[16:19], s71 offen offset:3072 sc1 lds\n"
    "s_lshl4_add_u32 s71, s81, s39\n"
    "s_add_u32 m0, s42, 0x4000\n"
    "v_and_b32 v237, s37, v246\n"
    "v_lshlrev_b32 v238, 16, v247\n"
    "v_and_b32 v239, s37, v247\n"
    "v_mfma_f32_32x32x16_bf16 v[16:31], v[196:199], a[180:183], v[16:31]\n"
    "ds_read_b64_tr_b16 a[44:45], v207 offset:18432\n"
    "ds_read_b64_tr_b16 a[46:47], v207 offset:19456\n"
    "s_waitcnt vmcnt(14)\n"
    "ds_read_b64_tr_b16 a[48:49], v206 offset:32768\n"
    "v_mfma_f32_32x32x16_bf16 v[16:31], v[120:123], a[184:187], v[16:31]\n"
    "s_waitcnt lgkmcnt(4)\n"
    "v_add_f32 v112, v112, v114\n"
    "v_add_f32 v113, v113, v115\n"
    "s_waitcnt lgkmcnt(3)\n"
    "v_add_f32 v116, v116, v118\n"
    "v_add_f32 v117, v117, v119\n"
    "v_add_f32 v112, v112, v116\n"
    "v_add_f32 v113, v113, v117\n"
    "v_mfma_f32_32x32x16_bf16 v[16:31], v[124:127], a[188:191], v[16:31]\n"
    "buffer_load_dwordx4 v6, s[32:35], s71 offen offset:0 lds\n"
    "v_min_u32 v112, -2, v112\n"
    "v_min_u32 v113, -2, v113\n"
    "ds_read_b64_tr_b16 a[50:51], v206 offset:33792\n"
    "ds_read_b64_tr_b16 a[52:53], v207 offset:32768\n"
    "ds_read_b64_tr_b16 a[54:55], v207 offset:33792\n"
    "s_cmp_lg_u32 s52, 0\n"
    "s_cbranch_scc1 Lsst_31\n"
    "s_mov_b32 exec_hi, 0\n"
    "buffer_store_dwordx2 v[112:113], v0, s[16:19], s59 offen offset:0 sc1\n"
    "s_mov_b32 exec_hi, -1\n"
    "Lsst_31:\n"
    "ds_read_b64_tr_b16 a[56:57], v206 offset:34816\n"
    "ds_read_b64_tr_b16 a[58:59], v206 offset:35840\n"
    "ds_read_b64_tr_b16 a[60:61], v207 offset:34816\n"
    "ds_read_b64_tr_b16 a[62:63], v207 offset:35840\n"
    "buffer_load_dwordx4 v7, s[32:35], s71 offen offset:1024 lds\n"
    "buffer_load_dwordx4 v8, s[32:35], s71 offen offset:2048 lds\n"
    "buffer_load_dwordx4 v9, s[32:35], s71 offen offset:3072 lds\n"
    "s_waitcnt lgkmcnt(0)\n"
    "s_lshl4_add_u32 s71, s76, s40\n"
    "s_add_u32 m0, s42, 0x14000\n"
    "s_nop 0\n"
    "buffer_load_dwordx4 v6, s[32:35], s71 offen offset:0 lds\n"
    "buffer_load_dwordx4 v7, s[32:35], s71 offen offset:1024 lds\n"
    "buffer_load_dwordx4 v8, s[32:35], s71 offen offset:2048 lds\n"
    "buffer_load_dwordx4 v9, s[32:35], s71 offen offset:3072 lds\n"
    "s_waitcnt vmcnt(8)\n"
    "ds_read_b128 v[64:67], v248 offset:0\n"
    "ds_read_b128 v[68:71], v248 offset:256\n"
    "ds_read_b128 v[72:75], v248 offset:2048\n"
    "ds_read_b128 v[76:79], v248 offset:2304\n"
    "ds_read_b128 v[80:83], v248 offset:16384\n"
    "ds_read_b128 v[84:87], v248 offset:16640\n"
    "ds_read_b128 v[88:91], v248 offset:18432\n"
    "ds_read_b128 v[92:95], v248 offset:18688\n"
    "ds_read_b64_tr_b16 a[16:17], v206 offset:0\n"
    "ds_read_b64_tr_b16 a[18:19], v206 offset:1024\n"
    "ds_read_b64_tr_b16 a[20:21], v207 offset:0\n"
    "ds_read_b64_tr_b16 a[22:23], v207 offset:1024\n"
    "ds_read_b64_tr_b16 a[24:25], v206 offset:2048\n"
    "ds_read_b64_tr_b16 a[26:27], v206 offset:3072\n"
    "ds_read_b64_tr_b16 a[28:29], v207 offset:2048\n"
    "ds_read_b64_tr_b16 a[30:31], v207 offset:3072\n"
    "v_lshlrev_b32 v5, 16, v5\n"
    "v_add_f32 v254, v5, v16\n"
    "v_mov_b32 v5, v17\n"
    "s_waitcnt lgkmcnt(14)\n"
    "v_max3_u32 v255, v64, v65, v66\n"
    "v_max3_u32 v255, v255, v67, v68\n"
    "v_max3_u32 v255, v255, v69, v70\n"
    "s_waitcnt lgkmcnt(13)\n"
    "v_max3_u32 v255, v255, v71, v72\n"
    "v_max3_u32 v255, v255, v73, v74\n"
    "s_waitcnt lgkmcnt(12)\n"
    "v_max3_u32 v255, v255, v75, v76\n"
    "v_max3_u32 v255, v255, v77, v78\n"
    "s_waitcnt lgkmcnt(11)\n"
    "v_max3_u32 v255, v255, v79, v80\n"
    "v_max3_u32 v255, v255, v81, v82\n"
    "s_waitcnt lgkmcnt(10)\n"
    "v_max3_u32 v255, v255, v83, v84\n"
    "v_max3_u32 v255, v255, v85, v86\n"
    "s_waitcnt lgkmcnt(9)\n"
    "v_max3_u32 v255, v255, v87, v88\n"
    "v_max3_u32 v255, v255, v89, v90\n"
    "s_waitcnt lgkmcnt(8)\n"
    "v_max3_u32 v255, v255, v91, v92\n"
    "v_max3_u32 v255, v255, v93, v94\n"
    "v_max_u32 v255, v255, v95\n"
    "v_cmp_eq_u32 vcc, -1, v255\n"
    "s_cbranch_vccnz Lo1r_29\n"
    "Lo1b_30:\n"
    "v_cmp_ne_u64 vcc, s[62:63], v[174:175]\n"
    "s_cbranch_vccnz Lcl2r_32\n"
    "Lcl2b_33:\n"
    "s_barrier\n"
    "v_mfma_f32_16x16x32_bf16 v[48:51], a[192:195], v[64:67], 0\n"
    "ds_read_b128 v[96:99], v249 offset:0\n"
    "s_lshl4_add_u32 s71, s77, s40\n"
    "v_mfma_f32_16x16x32_bf16 v[52:55], a[192:195], v[68:71], 0\n"
    "ds_read_b128 v[100:103], v249 offset:256\n"
    "s_add_u32 m0, s42, 0x18000\n"
    "v_mfma_f32_16x16x32_bf16 v[48:51], a[196:199], v[72:75], v[48:51]\n"
    "ds_read_b128 v[104:107], v249 offset:2048\n"
    "buffer_load_dwordx4 v6, s[32:35], s71 offen offset:0 lds\n"
    "v_mfma_f32_16x16x32_bf16 v[52:55], a[196:199], v[76:79], v[52:55]\n"
    "ds_read_b128 v[108:111], v249 offset:2304\n"
    "v_and_b32 v247, 31, v1\n"
    "v_mfma_f32_16x16x32_bf16 v[48:51], a[200:203], v[80:83], v[48:51]\n"
    "ds_read_b128 v[64:67], v249 offset:16384\n"
    "v_lshrrev_b32 v246, 5, v1\n"
    "v_mfma_f32_16x16x32_bf16 v[52:55], a[200:203], v[84:87], v[52:55]\n"
    "ds_read_b128 v[68:71], v249 offset:16640\n"
    "v_lshlrev_b32 v247, 4, v247\n"
    "v_mfma_f32_16x16x32_bf16 v[48:51], a[204:207], v[88:91], v[48:51]\n"
    "ds_read_b128 v[72:75], v249 offset:18432\n"
    "buffer_load_dwordx4 v7, s[32:35], s71 offen offset:1024 lds\n"
    "v_mfma_f32_16x16x32_bf16 v[52:55], a[204:207], v[92:95], v[52:55]\n"
    "ds_read_b128 v[76:79], v249 offset:18688\n"
    "s_waitcnt lgkmcnt(4)\n"
    "v_mfma_f32_16x16x32_bf16 v[48:51], a[208:211], v[96:99], v[48:51]\n"
    "ds_read_b128 v[80:83], v250 offset:0\n"
    "v_lshl_add_u32 v244, v246, 12, v247\n"
    "v_mfma_f32_16x16x32_bf16 v[52:55], a[208:211], v[100:103], v[52:55]\n"
    "ds_read_b128 v[84:87], v250 offset:256\n"
    "s_lshl_b32 s72, s47, 8\n"
    "v_mfma_f32_16x16x32_bf16 v[48:51], a[212:215], v[104:107], v[48:51]\n"
    "ds_read_b128 v[88:91], v250 offset:2048\n"
    "buffer_load_dwordx4 v8, s[32:35], s71 offen offset:2048 lds\n"
    "v_mfma_f32_16x16x32_bf16 v[52:55], a[212:215], v[108:111], v[52:55]\n"
    "ds_read_b128 v[92:95], v250 offset:2304\n"
    "s_waitcnt lgkmcnt(4)\n"
    "v_mfma_f32_16x16x32_bf16 v[48:51], a[216:219], v[64:67], v[48:51]\n"
    "ds_read_b128 v[96:99], v250 offset:16384\n"
    "s_add_u32 s72, s72, 0x24000\n"
    "v_mfma_f32_16x16x32_bf16 v[52:55], a[216:219], v[68:71], v[52:55]\n"
    "ds_read_b128 v[100:103], v250 offset:16640\n"
    "v_lshl_add_u32 v246, v246, 11, v247\n"
    "v_mfma_f32_16x16x32_bf16 v[48:51], a[220:223], v[72:75], v[48:51]\n"
    "ds_read_b128 v[104:107], v250 offset:18432\n"
    "buffer_load_dwordx4 v9, s[32:35], s71 offen offset:3072 lds\n"
    "v_mfma_f32_16x16x32_bf16 v[52:55], a[220:223], v[76:79], v[52:55]\n"
    "ds_read_b128 v[108:111], v250 offset:18688\n"
    "s_waitcnt lgkmcnt(4)\n"
    "v_mfma_f32_16x16x32_bf16 v[48:51], a[224:227], v[80:83], v[48:51]\n"
    "ds_read_b128 v[64:67], v251 offset:0\n"
    "v_add_u32 v246, s72, v246\n"
    "v_mfma_f32_16x16x32_bf16 v[52:55], a[224:227], v[84:87], v[52:55]\n"
    "ds_read_b128 v[68:71], v251 offset:256\n"
    "v_add_u32 v247, s42, v0\n"
    "v_mfma_f32_16x16x32_bf16 v[48:51], a[228:231], v[88:91], v[48:51]\n"
    "ds_read_b128 v[72:75], v251 offset:2048\n"
    "v_add_u32 v240, 0xc000, v247\n"
    "v_mfma_f32_16x16x32_bf16 v[52:55], a[228:231], v[92:95], v[52:55]\n"
    "ds_read_b128 v[76:79], v251 offset:2304\n"
    "s_waitcnt lgkmcnt(4)\n"
    "v_mfma_f32_16x16x32_bf16 v[48:51], a[232:235], v[96:99], v[48:51]\n"
    "ds_read_b128 v[80:83], v251 offset:16384\n"
    "v_mfma_f32_16x16x32_bf16 v[52:55], a[232:235], v[100:103], v[52:55]\n"
    "ds_read_b128 v[84:87], v251 offset:16640\n"
    "s_xor_b32 s71, s87, 0x0\n"
    "v_mfma_f32_16x16x32_bf16 v[48:51], a[236:239], v[104:107], v[48:51]\n"
    "ds_read_b128 v[88:91], v251 offset:18432\n"
    "s_add_u32 m0, s42, 0xc000\n"
    "v_mfma_f32_16x16x32_bf16 v[52:55], a[236:239], v[108:111], v[52:55]\n"
    "ds_read_b128 v[92:95], v251 offset:18688\n"
    "s_waitcnt lgkmcnt(4)\n"
    "v_mfma_f32_16x16x32_bf16 v[48:51], a[240:243], v[64:67], v[48:51]\n"
    "buffer_load_dwordx4 v10, s[16:19], s71 offen offset:0 sc1 lds\n"
    "v_add_u32 v247, 0x1000, v247\n"
    "v_mfma_f32_16x16x32_bf16 v[52:55], a[240:243], v[68:71], v[52:55]\n"
    "v_and_b32 v247, 0x3fff, v247\n"
    "v_add_u32 v241, 0xc000, v247\n"
    "v_mfma_f32_16x16x32_bf16 v[48:51], a[244:247], v[72:75], v[48:51]\n"
    "v_add_u32 v247, 0x1000, v247\n"
    "v_and_b32 v247, 0x3fff, v247\n"
    "v_mfma_f32_16x16x32_bf16 v[52:55], a[244:247], v[76:79], v[52:55]\n"
    "s_waitcnt lgkmcnt(0)\n"
    "v_add_u32 v242, 0xc000, v247\n"
    "v_mfma_f32_16x16x32_bf16 v[48:51], a[248:251], v[80:83], v[48:51]\n"
    "buffer_load_dwordx4 v10, s[16:19], s71 offen offset:1024 sc1 lds\n"
    "v_add_u32 v247, 0x1000, v247\n"
    "v_mfma_f32_16x16x32_bf16 v[52:55], a[248:251], v[84:87], v[52:55]\n"
    "v_and_b32 v247, 0x3fff, v247\n"
    "v_add_u32 v243, 0xc000, v247\n"
    "v_mfma_f32_16x16x32_bf16 v[48:51], a[252:255], v[88:91], v[48:51]\n"
    "v_mfma_f32_16x16x32_bf16 v[52:55], a[252:255], v[92:95], v[52:55]\n"
    "buffer_load_dwordx4 v10, s[16:19], s71 offen offset:2048 sc1 lds\n"
    "buffer_load_dwordx4 v10, s[16:19], s71 offen offset:3072 sc1 lds\n"
    "s_xor_b32 s71, s87, 0x1000\n"
    "s_add_u32 m0, s42, 0x10000\n"
    "s_nop 0\n"
    "buffer_load_dwordx4 v10, s[16:19], s71 offen offset:0 sc1 lds\n"
    "buffer_load_dwordx4 v10, s[16:19], s71 offen offset:1024 sc1 lds\n"
    "buffer_load_dwordx4 v10, s[16:19], s71 offen offset:2048 sc1 lds\n"
    "buffer_load_dwordx4 v10, s[16:19], s71 offen offset:3072 sc1 lds\n"
    "v_pk_fma_f32 v[232:233], v[48:49], v[208:209], v[232:233]\n"
    "v_pk_fma_f32 v[234:235], v[50:51], v[210:211], v[234:235]\n"
    "v_pk_fma_f32 v[236:237], v[52:53], v[208:209], v[236:237]\n"
    "v_pk_fma_f32 v[238:239], v[54:55], v[210:211], v[238:239]\n"
    "v_pk_mul_f32 v[48:49], v[232:233], v[212:213]\n"
    "v_pk_mul_f32 v[50:51], v[234:235], v[214:215]\n"
    "v_pk_mul_f32 v[52:53], v[236:237], v[212:213]\n"
    "v_pk_mul_f32 v[54:55], v[238:239], v[214:215]\n"
    "v_cvt_pk_bf16_f32 v56, v48, v49\n"
    "v_cvt_pk_bf16_f32 v57, v50, v51\n"
    "v_cvt_pk_bf16_f32 v58, v52, v53\n"
    "v_cvt_pk_bf16_f32 v59, v54, v55\n"
    "v_min_u32 v56, -2, v56\n"
    "v_min_u32 v58, -2, v58\n"
    "v_min_u32 v57, -2, v57\n"
    "v_min_u32 v59, -2, v59\n"
    "s_add_u32 s75, s57, 32768\n"
    "v_permlane16_swap_b32 v56, v58\n"
    "v_permlane16_swap_b32 v57, v59\n"
    "buffer_store_dwordx4 v[56:59], v10, s[16:19], s75 offen sc1\n"
    "v_add_f32 v200, v232, v233\n"
    "v_mul_f32 v201, v232, v232\n"
    "v_fmac_f32 v201, v233, v233\n"
    "v_add_f32 v200, v200, v234\n"
    "v_fmac_f32 v201, v234, v234\n"
    "v_add_f32 v200, v200, v235\n"
    "v_fmac_f32 v201, v235, v235\n"
    "v_add_f32 v202, v236, v237\n"
    "v_mul_f32 v203, v236, v236\n"
    "v_fmac_f32 v203, v237, v237\n"
    "v_add_f32 v202, v202, v238\n"
    "v_fmac_f32 v203, v238, v238\n"
    "v_add_f32 v202, v202, v239\n"
    "v_fmac_f32 v203, v239, v239\n"
    "s_nop 0\n"
    "v_permlane16_swap_b32 v200, v202\n"
    "v_permlane16_swap_b32 v201, v203\n"
    "v_add_f32 v200, v200, v202\n"
    "v_add_f32 v201, v201, v203\n"
    "v_mov_b32 v202, v200\n"
    "v_mov_b32 v203, v201\n"
    "s_nop 0\n"
    "v_permlane32_swap_b32 v200, v202\n"
    "v_permlane32_swap_b32 v201, v203\n"
    "v_add_f32 v200, v200, v202\n"
    "v_add_f32 v201, v201, v203\n"
    "ds_write_b64 v217, v[200:201] offset:1024\n"
    "s_mov_b32 s69, 0xbb000000\n"
    "s_mov_b32 s70, 0x3727c5ac\n"
    "s_lshr_b32 s71, s42, 2\n"
    "s_add_u32 m0, s71, 0x24000\n"
    "s_nop 0\n"
    "buffer_load_dwordx4 v244, s[16:19], s88 offen offset:0 sc1 lds\n"
    "s_waitcnt vmcnt(14)\n"
    "ds_read_b64_tr_b16 a[192:193], v206 offset:16384\n"
    "ds_read_b64_tr_b16 a[194:195], v206 offset:17408\n"
    "ds_read_b64_tr_b16 a[196:197], v207 offset:16384\n"
    "ds_read_b64_tr_b16 a[198:199], v207 offset:17408\n"
    "ds_read_b64_tr_b16 a[200:201], v206 offset:18432\n"
    "ds_read_b64_tr_b16 a[202:203], v206 offset:19456\n"
    "ds_read_b64_tr_b16 a[204:205], v207 offset:18432\n"
    "ds_read_b64_tr_b16 a[206:207], v207 offset:19456\n"
    "s_waitcnt vmcnt(10)\n"
    "ds_read_b64_tr_b16 a[208:209], v206 offset:32768\n"
    "ds_read_b64_tr_b16 a[210:211], v206 offset:33792\n"
    "ds_read_b64_tr_b16 a[212:213], v207 offset:32768\n"
    "ds_read_b64_tr_b16 a[214:215], v207 offset:33792\n"
    "ds_read_b64_tr_b16 a[216:217], v206 offset:34816\n"
    "ds_read_b64_tr_b16 a[218:219], v206 offset:35840\n"
    "ds_read_b64_tr_b16 a[220:221], v207 offset:34816\n"
    "ds_read_b64_tr_b16 a[222:223], v207 offset:35840\n"
    "v_cvt_pk_bf16_f32 v252, v5, v254\n"
    "v_lshlrev_b32 v248, 16, v252\n"
    "v_and_b32 v249, s37, v252\n"
    "v_sub_f32 v255, v5, v248\n"
    "v_sub_f32 v254, v254, v249\n"
    "v_cvt_pk_bf16_f32 v253, v255, v254\n"
    "v_subrev_u32 v248, 32, v1\n"
    "v_ashrrev_i32 v248, 31, v248\n"
    "v_and_b32 v252, v248, v252\n"
    "v_and_b32 v253, v248, v253\n"
    "v_mov_b32 v254, v252\n"
    "v_mov_b32 v255, v253\n"
    "s_waitcnt lgkmcnt(8)\n"
    "s_xor_b32 s71, s87, 0x10000\n"
    "s_add_u32 m0, s42, 0x14000\n"
    "s_nop 0\n"
    "buffer_load_dwordx4 v10, s[16:19], s71 offen offset:0 sc1 lds\n"
    "buffer_load_dwordx4 v10, s[16:19], s71 offen offset:1024 sc1 lds\n"
    "buffer_load_dwordx4 v10, s[16:19], s71 offen offset:2048 sc1 lds\n"
    "buffer_load_dwordx4 v10, s[16:19], s71 offen offset:3072 sc1 lds\n"
    "s_cmp_ge_u32 s53, 28\n"
    "s_cbranch_scc1 Lidle_34\n"
    "s_waitcnt vmcnt(6)\n"
    "ds_read_b128 v[48:51], v240 offset:0\n"
    "ds_read_b128 v[52:55], v240 offset:1024\n"
    "ds_read_b128 v[56:59], v240 offset:2048\n"
    "ds_read_b128 v[60:63], v240 offset:3072\n"
    "ds_read_b128 v[64:67], v240 offset:16384\n"
    "ds_read_b128 v[68:71], v240 offset:17408\n"
    "ds_read_b128 v[72:75], v240 offset:18432\n"
    "ds_read_b128 v[76:79], v240 offset:19456\n"
    "s_waitcnt lgkmcnt(7)\n"
    "v_mfma_f32_32x32x16_bf16 v[16:31], a[64:67], v[48:51], 0\n"
    "v_max3_u32 v208, v48, v49, v50\n"
    "s_waitcnt lgkmcnt(6)\n"
    "v_max3_u32 v208, v208, v51, v52\n"
    "v_max3_u32 v208, v208, v53, v54\n"
    "s_waitcnt lgkmcnt(5)\n"
    "v_max3_u32 v208, v208, v55, v56\n"
    "v_max3_u32 v208, v208, v57, v58\n"
    "v_mfma_f32_32x32x16_bf16 v[16:31], a[68:71], v[52:55], v[16:31]\n"
    "s_waitcnt lgkmcnt(4)\n"
    "v_max3_u32 v208, v208, v59, v60\n"
    "v_max3_u32 v208, v208, v61, v62\n"
    "s_waitcnt lgkmcnt(3)\n"
    "v_max3_u32 v208, v208, v63, v64\n"
    "v_max3_u32 v208, v208, v65, v66\n"
    "s_waitcnt lgkmcnt(2)\n"
    "v_max3_u32 v208, v208, v67, v68\n"
    "v_mfma_f32_32x32x16_bf16 v[16:31], a[72:75], v[56:59], v[16:31]\n"
    "v_max3_u32 v208, v208, v69, v70\n"
    "s_waitcnt lgkmcnt(1)\n"
    "v_max3_u32 v208, v208, v71, v72\n"
    "v_max3_u32 v208, v208, v73, v74\n"
    "s_waitcnt lgkmcnt(0)\n"
    "v_max3_u32 v208, v208, v75, v76\n"
    "v_max3_u32 v208, v208, v77, v78\n"
    "v_mfma_f32_32x32x16_bf16 v[16:31], a[76:79], v[60:63], v[16:31]\n"
    "v_max_u32 v208, v208, v79\n"
    "v_cmp_eq_u32 vcc, -1, v208\n"
    "s_cbranch_vccnz Ly0r_39\n"
    "Ly0b_40:\n"
    "s_barrier\n"
    "v_mfma_f32_32x32x16_bf16 v[16:31], a[80:83], v[64:67], v[16:31]\n"
    "ds_read_b128 v[80:83], v241 offset:0\n"
    "ds_read_b128 v[84:87], v241 offset:1024\n"
    "s_xor_b32 s71, s87, 0x11000\n"
    "s_add_u32 m0, s42, 0x18000\n"
    "s_nop 0\n"
    "buffer_load_dwordx4 v10, s[16:19], s71 offen offset:0 sc1 lds\n"
    "v_mfma_f32_32x32x16_bf16 v[16:31], a[84:87], v[68:71], v[16:31]\n"
    "ds_read_b128 v[88:91], v241 offset:2048\n"
    "ds_read_b128 v[92:95], v241 offset:3072\n"
    "ds_read_b64_tr_b16 a[224:225], v11 offset:0\n"
    "v_mfma_f32_32x32x16_bf16 v[16:31], a[88:91], v[72:75], v[16:31]\n"
    "ds_read_b128 v[48:51], v241 offset:16384\n"
    "ds_read_b128 v[52:55], v241 offset:17408\n"
    "v_mfma_f32_32x32x16_bf16 v[16:31], a[92:95], v[76:79], v[16:31]\n"
    "ds_read_b128 v[56:59], v241 offset:18432\n"
    "ds_read_b128 v[60:63], v241 offset:19456\n"
    "s_waitcnt lgkmcnt(7)\n"
    "buffer_load_dwordx4 v10, s[16:19], s71 offen offset:1024 sc1 lds\n"
    "ds_read2_b64 v[112:115], v216 offset0:0 offset1:32\n"
    "v_mfma_f32_32x32x16_bf16 v[16:31], a[96:99], v[80:83], v[16:31]\n"
    "ds_read_b128 v[64:67], v242 offset:0\n"
    "ds_read2_b64 v[116:119], v216 offset0:64 offset1:96\n"
    "s_waitcnt lgkmcnt(2)\n"
    "v_add_f32 v112, v112, v114\n"
    "v_add_f32 v113, v113, v115\n"
    "s_waitcnt lgkmcnt(0)\n"
    "v_add_f32 v116, v116, v118\n"
    "v_add_f32 v117, v117, v119\n"
    "v_mfma_f32_32x32x16_bf16 v[16:31], a[100:103], v[84:87], v[16:31]\n"
    "ds_read_b128 v[68:71], v242 offset:1024\n"
    "v_add_f32 v112, v112, v116\n"
    "v_add_f32 v113, v113, v117\n"
    "v_min_u32 v112, -2, v112\n"
    "v_min_u32 v113, -2, v113\n"
    "v_mfma_f32_32x32x16_bf16 v[16:31], a[104:107], v[88:91], v[16:31]\n"
    "ds_read_b128 v[72:75], v242 offset:2048\n"
    "buffer_load_dwordx4 v10, s[16:19], s71 offen offset:2048 sc1 lds\n"
    "ds_read_b64_tr_b16 a[226:227], v11 offset:1024\n"
    "ds_read_b64_tr_b16 a[228:229], v12 offset:0\n"
    "v_mfma_f32_32x32x16_bf16 v[16:31], a[108:111], v[92:95], v[16:31]\n"
    "ds_read_b128 v[76:79], v242 offset:3072\n"
    "s_cmp_lg_u32 s52, 0\n"
    "s_cbranch_scc1 Lsst_36\n"
    "s_mov_b32 exec_hi, 0\n"
    "buffer_store_dwordx2 v[112:113], v0, s[16:19], s59 offen offset:2048 sc1\n"
    "s_mov_b32 exec_hi, -1\n"
    "Lsst_36:\n"
    "v_mfma_f32_32x32x16_bf16 v[16:31], a[112:115], v[48:51], v[16:31]\n"
    "ds_read_b128 v[80:83], v242 offset:16384\n"
    "ds_read_b64_tr_b16 a[230:231], v12 offset:1024\n"
    "ds_read_b64_tr_b16 a[232:233], v11 offset:2048\n"
    "v_mfma_f32_32x32x16_bf16 v[16:31], a[116:119], v[52:55], v[16:31]\n"
    "ds_read_b128 v[84:87], v242 offset:17408\n"
    "ds_read_b64_tr_b16 a[234:235], v11 offset:3072\n"
    "ds_read_b64_tr_b16 a[236:237], v12 offset:2048\n"
    "v_mfma_f32_32x32x16_bf16 v[16:31], a[120:123], v[56:59], v[16:31]\n"
    "ds_read_b128 v[88:91], v242 offset:18432\n"
    "ds_read_b64_tr_b16 a[238:239], v12 offset:3072\n"
    "ds_read_b64_tr_b16 a[240:241], v11 offset:16384\n"
    "v_mfma_f32_32x32x16_bf16 v[16:31], a[124:127], v[60:63], v[16:31]\n"
    "ds_read_b128 v[92:95], v242 offset:19456\n"
    "s_waitcnt lgkmcnt(14)\n"
    "buffer_load_dwordx4 v10, s[16:19], s71 offen offset:3072 sc1 lds\n"
    "ds_read_b64_tr_b16 a[242:243], v11 offset:17408\n"
    "ds_read_b64_tr_b16 a[244:245], v12 offset:16384\n"
    "v_mfma_f32_32x32x16_bf16 v[16:31], a[128:131], v[64:67], v[16:31]\n"
    "ds_read_b128 v[48:51], v243 offset:0\n"
    "ds_read_b64_tr_b16 a[246:247], v12 offset:17408\n"
    "ds_read_b64_tr_b16 a[248:249], v11 offset:18432\n"
    "v_mfma_f32_32x32x16_bf16 v[16:31], a[132:135], v[68:71], v[16:31]\n"
    "ds_read_b128 v[52:55], v243 offset:1024\n"
    "s_waitcnt lgkmcnt(14)\n"
    "ds_read_b64_tr_b16 a[250:251], v11 offset:19456\n"
    "ds_read_b64_tr_b16 a[252:253], v12 offset:18432\n"
    "v_mfma_f32_32x32x16_bf16 v[16:31], a[136:139], v[72:75], v[16:31]\n"
    "ds_read_b128 v[56:59], v243 offset:2048\n"
    "s_waitcnt vmcnt(8)\n"
    "s_barrier\n"
    "ds_read_b128 v[192:195], v246 offset:0\n"
    "ds_read_b128 v[196:199], v246 offset:1024\n"
    "v_mfma_f32_32x32x16_bf16 v[16:31], a[140:143], v[76:79], v[16:31]\n"
    "ds_read_b128 v[60:63], v243 offset:3072\n"
    "s_waitcnt lgkmcnt(14)\n"
    "ds_read_b64_tr_b16 a[254:255], v12 offset:19456\n"
    "v_mfma_f32_32x32x16_bf16 v[16:31], a[144:147], v[80:83], v[16:31]\n"
    "ds_read_b128 v[64:67], v243 offset:16384\n"
    "s_waitcnt vmcnt(0)\n"
    "ds_read_b128 v[80:83], v240 offset:32768\n"
    "v_mfma_f32_32x32x16_bf16 v[16:31], a[148:151], v[84:87], v[16:31]\n"
    "ds_read_b128 v[68:71], v243 offset:17408\n"
    "ds_read_b128 v[84:87], v240 offset:33792\n"
    "s_waitcnt lgkmcnt(14)\n"
    "s_waitcnt lgkmcnt(7)\n"
    "v_max3_u32 v209, v192, v193, v194\n"
    "s_waitcnt lgkmcnt(6)\n"
    "v_max3_u32 v209, v209, v195, v196\n"
    "v_mfma_f32_32x32x16_bf16 v[16:31], a[152:155], v[88:91], v[16:31]\n"
    "ds_read_b128 v[72:75], v243 offset:18432\n"
    "ds_read_b128 v[88:91], v240 offset:34816\n"
    "v_max3_u32 v209, v209, v197, v198\n"
    "v_max_u32 v209, v209, v199\n"
    "v_cmp_eq_u32 vcc, -1, v209\n"
    "s_cbranch_vccnz Ls0r_37\n"
    "Ls0b_38:\n"
    "v_mfma_f32_32x32x16_bf16 v[16:31], a[156:159], v[92:95], v[16:31]\n"
    "ds_read_b128 v[76:79], v243 offset:19456\n"
    "ds_read_b128 v[92:95], v240 offset:35840\n"
    "v_xor_b32 v246, 0x200, v246\n"
    "v_add_f32 v192, v192, v194\n"
    "v_add_f32 v193, v193, v195\n"
    "v_mfma_f32_32x32x16_bf16 v[16:31], a[160:163], v[48:51], v[16:31]\n"
    "ds_read_b128 v[48:51], v240 offset:49152\n"
    "v_add_f32 v196, v196, v198\n"
    "v_add_f32 v197, v197, v199\n"
    "v_add_f32 v192, v192, v196\n"
    "v_add_f32 v193, v193, v197\n"
    "v_mov_b32 v194, v192\n"
    "v_mfma_f32_32x32x16_bf16 v[16:31], a[164:167], v[52:55], v[16:31]\n"
    "ds_read_b128 v[52:55], v240 offset:50176\n"
    "s_waitcnt lgkmcnt(11)\n"
    "v_mov_b32 v195, v193\n"
    "s_nop 0\n"
    "v_permlane32_swap_b32 v192, v194\n"
    "v_mfma_f32_32x32x16_bf16 v[16:31], a[168:171], v[56:59], v[16:31]\n"
    "ds_read_b128 v[56:59], v240 offset:51200\n"
    "v_permlane32_swap_b32 v193, v195\n"
    "v_add_f32 v192, v192, v194\n"
    "v_add_f32 v193, v193, v195\n"
    "v_mul_f32 v192, s69, v192\n"
    "v_mfma_f32_32x32x16_bf16 v[16:31], a[172:175], v[60:63], v[16:31]\n"
    "ds_read_b128 v[60:63], v240 offset:52224\n"
    "s_waitcnt lgkmcnt(9)\n"
    "v_mul_f32 v193, s69, v193\n"
    "v_fmac_f32 v193, v192, v192\n"
    "v_min_f32 v193, 0, v193\n"
    "v_sub_f32 v193, s70, v193\n"
    "v_mfma_f32_32x32x16_bf16 v[16:31], a[176:179], v[64:67], v[16:31]\n"
    "v_sqrt_f32 v193, v193\n"
    "s_nop 0\n"
    "v_cvt_pk_bf16_f32 v248, v192, v193\n"
    "v_lshlrev_b32 v194, 16, v248\n"
    "v_and_b32 v195, s37, v248\n"
    "v_mfma_f32_32x32x16_bf16 v[16:31], a[180:183], v[68:71], v[16:31]\n"
    "s_waitcnt lgkmcnt(5)\n"
    "s_xor_b32 s71, s87, 0x8000\n"
    "s_add_u32 m0, s42, 0x0\n"
    "s_nop 0\n"
    "buffer_load_dwordx4 v10, s[16:19], s71 offen offset:0 sc1 lds\n"
    "v_sub_f32 v192, v192, v194\n"
    "v_mfma_f32_32x32x16_bf16 v[16:31], a[184:187], v[72:75], v[16:31]\n"
    "v_sub_f32 v193, v193, v195\n"
    "v_cvt_pk_bf16_f32 v250, v192, v193\n"
    "v_mov_b32 v249, v248\n"
    "v_mov_b32 v251, v250\n"
    "v_max3_u32 v208, v80, v81, v82\n"
    "v_max3_u32 v208, v208, v83, v84\n"
    "v_mfma_f32_32x32x16_bf16 v[16:31], a[188:191], v[76:79], v[16:31]\n"
    "v_max3_u32 v208, v208, v85, v86\n"
    "v_max3_u32 v208, v208, v87, v88\n"
    "v_max3_u32 v208, v208, v89, v90\n"
    "s_waitcnt lgkmcnt(4)\n"
    "v_max3_u32 v208, v208, v91, v92\n"
    "v_max3_u32 v208, v208, v93, v94\n"
    "s_waitcnt lgkmcnt(3)\n"
    "v_max3_u32 v208, v208, v95, v48\n"
    "v_max3_u32 v208, v208, v49, v50\n"
    "s_waitcnt lgkmcnt(2)\n"
    "v_max3_u32 v208, v208, v51, v52\n"
    "v_max3_u32 v208, v208, v53, v54\n"
    "s_waitcnt lgkmcnt(1)\n"
    "v_max3_u32 v208, v208, v55, v56\n"
    "v_max3_u32 v208, v208, v57, v58\n"
    "s_waitcnt lgkmcnt(0)\n"
    "v_max3_u32 v208, v208, v59, v60\n"
    "v_max3_u32 v208, v208, v61, v62\n"
    "v_max_u32 v208, v208, v63\n"
    "buffer_load_dwordx4 v10, s[16:19], s71 offen offset:1024 sc1 lds\n"
    "buffer_load_dwordx4 v10, s[16:19], s71 offen offset:2048 sc1 lds\n"
    "buffer_load_dwordx4 v10, s[16:19], s71 offen offset:3072 sc1 lds\n"
    "v_cmp_eq_u32 vcc, -1, v208\n"
    "s_cbranch_vccnz Ly1r_41\n"
    "Ly1b_42:\n"
    "s_barrier\n"
    "v_mfma_f32_32x32x16_bf16 v[16:31], v[252:255], v[248:251], v[16:31]\n"
    "v_mfma_f32_32x32x16_bf16 v[32:47], a[64:67], v[80:83], 0\n"
    "ds_read_b128 v[64:67], v241 offset:32768\n"
    "s_xor_b32 s71, s87, 0x9000\n"
    "s_add_u32 m0, s42, 0x4000\n"
    "s_nop 0\n"
    "buffer_load_dwordx4 v10, s[16:19], s71 offen offset:0 sc1 lds\n"
    "ds_read_b128 v[192:195], v246 offset:0\n"
    "v_mfma_f32_32x32x16_bf16 v[32:47], a[68:71], v[84:87], v[32:47]\n"
    "ds_read_b128 v[68:71], v241 offset:33792\n"
    "ds_read_b128 v[196:199], v246 offset:1024\n"
    "v_mfma_f32_32x32x16_bf16 v[32:47], a[72:75], v[88:91], v[32:47]\n"
    "ds_read_b128 v[72:75], v241 offset:34816\n"
    "v_max_f32 v16, 0, v16\n"
    "v_max_f32 v17, 0, v17\n"
    "v_max_f32 v18, 0, v18\n"
    "v_max_f32 v19, 0, v19\n"
    "v_max_f32 v20, 0, v20\n"
    "v_mfma_f32_32x32x16_bf16 v[32:47], a[76:79], v[92:95], v[32:47]\n"
    "ds_read_b128 v[76:79], v241 offset:35840\n"
    "buffer_load_dwordx4 v10, s[16:19], s71 offen offset:1024 sc1 lds\n"
    "v_max_f32 v21, 0, v21\n"
    "v_max_f32 v22, 0, v22\n"
    "v_max_f32 v23, 0, v23\n"
    "v_mul_f32 v16, v16, v16\n"
    "v_mfma_f32_32x32x16_bf16 v[32:47], a[80:83], v[48:51], v[32:47]\n"
    "ds_read_b128 v[80:83], v241 offset:49152\n"
    "v_mul_f32 v17, v17, v17\n"
    "v_mul_f32 v18, v18, v18\n"
    "v_mul_f32 v19, v19, v19\n"
    "v_mul_f32 v20, v20, v20\n"
    "v_mul_f32 v21, v21, v21\n"
    "v_mfma_f32_32x32x16_bf16 v[32:47], a[84:87], v[52:55], v[32:47]\n"
    "ds_read_b128 v[84:87], v241 offset:50176\n"
    "v_mul_f32 v22, v22, v22\n"
    "v_mul_f32 v23, v23, v23\n"
    "v_cvt_pk_bf16_f32 v16, v16, v17\n"
    "v_cvt_pk_bf16_f32 v17, v18, v19\n"
    "v_cvt_pk_bf16_f32 v18, v20, v21\n"
    "v_mfma_f32_32x32x16_bf16 v[32:47], a[88:91], v[56:59], v[32:47]\n"
    "ds_read_b128 v[88:91], v241 offset:51200\n"
    "buffer_load_dwordx4 v10, s[16:19], s71 offen offset:2048 sc1 lds\n"
    "v_cvt_pk_bf16_f32 v19, v22, v23\n"
    "buffer_store_dwordx4 v[16:19], v10, s[16:19], s89 offen offset:0 sc1\n"
    "v_max_f32 v24, 0, v24\n"
    "v_mfma_f32_32x32x16_bf16 v[32:47], a[92:95], v[60:63], v[32:47]\n"
    "ds_read_b128 v[92:95], v241 offset:52224\n"
    "v_add_u32 v241, 0xffff4000, v241\n"
    "s_waitcnt lgkmcnt(7)\n"
    "v_max_f32 v25, 0, v25\n"
    "v_max_f32 v26, 0, v26\n"
    "v_max_f32 v27, 0, v27\n"
    "v_mfma_f32_32x32x16_bf16 v[32:47], a[96:99], v[64:67], v[32:47]\n"
    "ds_read_b128 v[48:51], v242 offset:32768\n"
    "v_max_f32 v28, 0, v28\n"
    "v_max_f32 v29, 0, v29\n"
    "v_max_f32 v30, 0, v30\n"
    "v_max_f32 v31, 0, v31\n"
    "v_mul_f32 v24, v24, v24\n"
    "v_mfma_f32_32x32x16_bf16 v[32:47], a[100:103], v[68:71], v[32:47]\n"
    "ds_read_b128 v[52:55], v242 offset:33792\n"
    "s_waitcnt lgkmcnt(6)\n"
    "v_mul_f32 v25, v25, v25\n"
    "v_mul_f32 v26, v26, v26\n"
    "v_mul_f32 v27, v27, v27\n"
    "v_mul_f32 v28, v28, v28\n"
    "v_mfma_f32_32x32x16_bf16 v[32:47], a[104:107], v[72:75], v[32:47]\n"
    "ds_read_b128 v[56:59], v242 offset:34816\n"
    "v_mul_f32 v29, v29, v29\n"
    "v_mul_f32 v30, v30, v30\n"
    "v_mul_f32 v31, v31, v31\n"
    "v_cvt_pk_bf16_f32 v24, v24, v25\n"
    "v_cvt_pk_bf16_f32 v25, v26, v27\n"
    "v_mfma_f32_32x32x16_bf16 v[32:47], a[108:111], v[76:79], v[32:47]\n"
    "ds_read_b128 v[60:63], v242 offset:35840\n"
    "s_waitcnt lgkmcnt(6)\n"
    "buffer_load_dwordx4 v10, s[16:19], s71 offen offset:3072 sc1 lds\n"
    "s_lshr_b32 s71, s42, 2\n"
    "s_add_u32 m0, s71, 0x26800\n"
    "v_cvt_pk_bf16_f32 v26, v28, v29\n"
    "v_mfma_f32_32x32x16_bf16 v[32:47], a[112:115], v[80:83], v[32:47]\n"
    "ds_read_b128 v[64:67], v242 offset:49152\n"
    "v_add_u32 v240, 0xffff4000, v240\n"
    "v_cvt_pk_bf16_f32 v27, v30, v31\n"
    "buffer_store_dwordx4 v[24:27], v10, s[16:19], s89 offen offset:1024 sc1\n"
    "s_add_u32 s89, s89, 114688\n"
    "v_mfma_f32_32x32x16_bf16 v[32:47], a[116:119], v[84:87], v[32:47]\n"
    "ds_read_b128 v[68:71], v242 offset:50176\n"
    "s_waitcnt lgkmcnt(6)\n"
    "v_min_u32 v212, 6, v1\n"
    "v_lshlrev_b32 v212, 6, v212\n"
    "s_bfe_u32 s74, s41, 0x10010\n"
    "s_mul_i32 s74, s74, 448\n"
    "v_mfma_f32_32x32x16_bf16 v[32:47], a[120:123], v[88:91], v[32:47]\n"
    "ds_read_b128 v[72:75], v242 offset:51200\n"
    "s_sub_u32 s73, 448, s74\n"
    "v_add_u32 v213, s73, v212\n"
    "v_add_u32 v212, s74, v212\n"
    "v_mov_b32 v214, s62\n"
    "v_mov_b32 v215, s63\n"
    "v_mfma_f32_32x32x16_bf16 v[32:47], a[124:127], v[92:95], v[32:47]\n"
    "ds_read_b128 v[76:79], v242 offset:52224\n"
    "v_add_u32 v242, 0xffff4000, v242\n"
    "s_waitcnt lgkmcnt(6)\n"
    "s_add_u32 s66, s92, 4096\n"
    "v_max3_u32 v209, v192, v193, v194\n"
    "v_mfma_f32_32x32x16_bf16 v[32:47], a[128:131], v[48:51], v[32:47]\n"
    "ds_read_b128 v[80:83], v243 offset:32768\n"
    "buffer_load_dwordx4 v244, s[16:19], s88 offen offset:2048 sc1 lds\n"
    "s_xor_b32 s71, s87, 0x18000\n"
    "s_add_u32 m0, s42, 0x1c000\n"
    "v_max3_u32 v209, v209, v195, v196\n"
    "v_mfma_f32_32x32x16_bf16 v[32:47], a[132:135], v[52:55], v[32:47]\n"
    "ds_read_b128 v[84:87], v243 offset:33792\n"
    "s_waitcnt lgkmcnt(6)\n"
    "v_max3_u32 v209, v209, v197, v198\n"
    "v_max_u32 v209, v209, v199\n"
    "v_cmp_eq_u32 vcc, -1, v209\n"
    "s_cbranch_vccnz Ls1r_43\n"
    "Ls1b_44:\n"
    "v_mfma_f32_32x32x16_bf16 v[32:47], a[136:139], v[56:59], v[32:47]\n"
    "ds_read_b128 v[88:91], v243 offset:34816\n"
    "v_xor_b32 v246, 0x200, v246\n"
    "v_add_f32 v192, v192, v194\n"
    "v_add_f32 v193, v193, v195\n"
    "v_add_f32 v196, v196, v198\n"
    "v_add_f32 v197, v197, v199\n"
    "v_mfma_f32_32x32x16_bf16 v[32:47], a[140:143], v[60:63], v[32:47]\n"
    "ds_read_b128 v[92:95], v243 offset:35840\n"
    "s_waitcnt lgkmcnt(6)\n"
    "buffer_load_dwordx4 v10, s[16:19], s71 offen offset:0 sc1 lds\n"
    "v_add_f32 v192, v192, v196\n"
    "v_add_f32 v193, v193, v197\n"
    "v_mov_b32 v194, v192\n"
    "v_mfma_f32_32x32x16_bf16 v[32:47], a[144:147], v[64:67], v[32:47]\n"
    "ds_read_b128 v[48:51], v243 offset:49152\n"
    "s_waitcnt vmcnt(3)\n"
    "ds_read_b128 v[64:67], v240 offset:0\n"
    "v_mov_b32 v195, v193\n"
    "s_nop 0\n"
    "v_mfma_f32_32x32x16_bf16 v[32:47], a[148:151], v[68:71], v[32:47]\n"
    "ds_read_b128 v[52:55], v243 offset:50176\n"
    "ds_read_b128 v[68:71], v240 offset:1024\n"
    "s_waitcnt lgkmcnt(8)\n"
    "v_permlane32_swap_b32 v192, v194\n"
    "v_mfma_f32_32x32x16_bf16 v[32:47], a[152:155], v[72:75], v[32:47]\n"
    "ds_read_b128 v[56:59], v243 offset:51200\n"
    "ds_read_b128 v[72:75], v240 offset:2048\n"
    "buffer_load_dwordx4 v10, s[16:19], s71 offen offset:1024 sc1 lds\n"
    "v_permlane32_swap_b32 v193, v195\n"
    "v_add_f32 v192, v192, v194\n"
    "v_mfma_f32_32x32x16_bf16 v[32:47], a[156:159], v[76:79], v[32:47]\n"
    "ds_read_b128 v[60:63], v243 offset:52224\n"
    "v_add_u32 v243, 0xffff4000, v243\n"
    "ds_read_b128 v[76:79], v240 offset:3072\n"
    "s_waitcnt lgkmcnt(10)\n"
    "v_add_f32 v193, v193, v195\n"
    "v_mul_f32 v192, s69, v192\n"
    "v_mfma_f32_32x32x16_bf16 v[32:47], a[160:163], v[80:83], v[32:47]\n"
    "ds_read_b128 v[80:83], v240 offset:16384\n"
    "v_mul_f32 v193, s69, v193\n"
    "v_fmac_f32 v193, v192, v192\n"
    "v_min_f32 v193, 0, v193\n"
    "v_sub_f32 v193, s70, v193\n"
    "v_mfma_f32_32x32x16_bf16 v[32:47], a[164:167], v[84:87], v[32:47]\n"
    "ds_read_b128 v[84:87], v240 offset:17408\n"
    "s_waitcnt lgkmcnt(10)\n"
    "buffer_load_dwordx4 v10, s[16:19], s71 offen offset:2048 sc1 lds\n"
    "v_sqrt_f32 v193, v193\n"
    "s_nop 0\n"
    "v_mfma_f32_32x32x16_bf16 v[32:47], a[168:171], v[88:91], v[32:47]\n"
    "ds_read_b128 v[88:91], v240 offset:18432\n"
    "v_cvt_pk_bf16_f32 v248, v192, v193\n"
    "v_lshlrev_b32 v194, 16, v248\n"
    "v_and_b32 v195, s37, v248\n"
    "v_sub_f32 v192, v192, v194\n"
    "v_sub_f32 v193, v193, v195\n"
    "v_mfma_f32_32x32x16_bf16 v[32:47], a[172:175], v[92:95], v[32:47]\n"
    "ds_read_b128 v[92:95], v240 offset:19456\n"
    "s_waitcnt lgkmcnt(9)\n"
    "v_cvt_pk_bf16_f32 v250, v192, v193\n"
    "v_mov_b32 v249, v248\n"
    "v_mov_b32 v251, v250\n"
    "v_max3_u32 v208, v64, v65, v66\n"
    "v_mfma_f32_32x32x16_bf16 v[32:47], a[176:179], v[48:51], v[32:47]\n"
    "buffer_load_dwordx4 v10, s[16:19], s71 offen offset:3072 sc1 lds\n"
    "s_xor_b32 s71, s87, 0x19000\n"
    "s_add_u32 m0, s42, 0x20000\n"
    "s_waitcnt lgkmcnt(8)\n"
    "v_max3_u32 v208, v208, v67, v68\n"
    "v_max3_u32 v208, v208, v69, v70\n"
    "v_mfma_f32_32x32x16_bf16 v[32:47], a[180:183], v[52:55], v[32:47]\n"
    "s_waitcnt lgkmcnt(5)\n"
    "v_max3_u32 v208, v208, v71, v72\n"
    "v_max3_u32 v208, v208, v73, v74\n"
    "s_waitcnt lgkmcnt(4)\n"
    "v_max3_u32 v208, v208, v75, v76\n"
    "v_max3_u32 v208, v208, v77, v78\n"
    "v_mfma_f32_32x32x16_bf16 v[32:47], a[184:187], v[56:59], v[32:47]\n"
    "s_waitcnt lgkmcnt(3)\n"
    "v_max3_u32 v208, v208, v79, v80\n"
    "v_max3_u32 v208, v208, v81, v82\n"
    "s_waitcnt lgkmcnt(2)\n"
    "v_max3_u32 v208, v208, v83, v84\n"
    "v_max3_u32 v208, v208, v85, v86\n"
    "s_waitcnt lgkmcnt(1)\n"
    "v_max3_u32 v208, v208, v87, v88\n"
    "v_mfma_f32_32x32x16_bf16 v[32:47], a[188:191], v[60:63], v[32:47]\n"
    "buffer_load_dwordx4 v10, s[16:19], s71 offen offset:0 sc1 lds\n"
    "v_max3_u32 v208, v208, v89, v90\n"
    "s_waitcnt lgkmcnt(0)\n"
    "v_max3_u32 v208, v208, v91, v92\n"
    "v_max3_u32 v208, v208, v93, v94\n"
    "v_max_u32 v208, v208, v95\n"
    "buffer_load_dwordx4 v10, s[16:19], s71 offen offset:1024 sc1 lds\n"
    "buffer_load_dwordx4 v10, s[16:19], s71 offen offset:2048 sc1 lds\n"
    "buffer_load_dwordx4 v10, s[16:19], s71 offen offset:3072 sc1 lds\n"
    "v_cmp_eq_u32 vcc, -1, v208\n"
    "s_cbranch_vccnz Ly2r_45\n"
    "Ly2b_46:\n"
    "s_waitcnt vmcnt(8)\n"
    "s_barrier\n"
    "v_mfma_f32_32x32x16_bf16 v[32:47], v[252:255], v[248:251], v[32:47]\n"
    "v_mfma_f32_32x32x16_bf16 v[16:31], a[64:67], v[64:67], 0\n"
    "ds_read_b128 v[48:51], v241 offset:0\n"
    "s_xor_b32 s67, s91, 0x0\n"
    "ds_read_b128 v[192:195], v246 offset:12288\n"
    "ds_read_b128 v[196:199], v246 offset:13312\n"
    "v_mfma_f32_32x32x16_bf16 v[16:31], a[68:71], v[68:71], v[16:31]\n"
    "ds_read_b128 v[52:55], v241 offset:1024\n"
    "s_mov_b64 exec, 1\n"
    "buffer_store_dwordx2 v[214:215], off, s[16:19], s67 sc1\n"
    "s_mov_b64 exec, -1\n"
    "v_mfma_f32_32x32x16_bf16 v[16:31], a[72:75], v[72:75], v[16:31]\n"
    "ds_read_b128 v[56:59], v241 offset:2048\n"
    "s_lshl4_add_u32 s71, s78, s40\n"
    "s_add_u32 m0, s42, 0xc000\n"
    "v_max_f32 v32, 0, v32\n"
    "v_max_f32 v33, 0, v33\n"
    "v_max_f32 v34, 0, v34\n"
    "v_mfma_f32_32x32x16_bf16 v[16:31], a[76:79], v[76:79], v[16:31]\n"
    "ds_read_b128 v[60:63], v241 offset:3072\n"
    "v_max_f32 v35, 0, v35\n"
    "v_max_f32 v36, 0, v36\n"
    "v_max_f32 v37, 0, v37\n"
    "v_max_f32 v38, 0, v38\n"
    "v_max_f32 v39, 0, v39\n"
    "v_mfma_f32_32x32x16_bf16 v[16:31], a[80:83], v[80:83], v[16:31]\n"
    "ds_read_b128 v[64:67], v241 offset:16384\n"
    "buffer_load_dwordx4 v6, s[32:35], s71 offen offset:0 lds\n"
    "v_mul_f32 v32, v32, v32\n"
    "v_mul_f32 v33, v33, v33\n"
    "v_mul_f32 v34, v34, v34\n"
    "v_mul_f32 v35, v35, v35\n"
    "v_mfma_f32_32x32x16_bf16 v[16:31], a[84:87], v[84:87], v[16:31]\n"
    "ds_read_b128 v[68:71], v241 offset:17408\n"
    "v_mul_f32 v36, v36, v36\n"
    "v_mul_f32 v37, v37, v37\n"
    "v_mul_f32 v38, v38, v38\n"
    "v_mul_f32 v39, v39, v39\n"
    "v_cvt_pk_bf16_f32 v32, v32, v33\n"
    "v_mfma_f32_32x32x16_bf16 v[16:31], a[88:91], v[88:91], v[16:31]\n"
    "ds_read_b128 v[72:75], v241 offset:18432\n"
    "v_cvt_pk_bf16_f32 v33, v34, v35\n"
    "v_cvt_pk_bf16_f32 v34, v36, v37\n"
    "v_cvt_pk_bf16_f32 v35, v38, v39\n"
    "buffer_store_dwordx4 v[32:35], v10, s[16:19], s90 offen offset:0 sc1\n"
    "v_mfma_f32_32x32x16_bf16 v[16:31], a[92:95], v[92:95], v[16:31]\n"
    "ds_read_b128 v[76:79], v241 offset:19456\n"
    "v_add_u32 v241, 0x1c000, v241\n"
    "s_waitcnt lgkmcnt(6)\n"
    "v_max_f32 v40, 0, v40\n"
    "v_max_f32 v41, 0, v41\n"
    "v_max_f32 v42, 0, v42\n"
    "v_mfma_f32_32x32x16_bf16 v[16:31], a[96:99], v[48:51], v[16:31]\n"
    "ds_read_b128 v[80:83], v242 offset:0\n"
    "v_max_f32 v43, 0, v43\n"
    "v_max_f32 v44, 0, v44\n"
    "v_max_f32 v45, 0, v45\n"
    "v_max_f32 v46, 0, v46\n"
    "v_max_f32 v47, 0, v47\n"
    "v_mfma_f32_32x32x16_bf16 v[16:31], a[100:103], v[52:55], v[16:31]\n"
    "ds_read_b128 v[84:87], v242 offset:1024\n"
    "s_waitcnt lgkmcnt(6)\n"
    "buffer_load_dwordx4 v7, s[32:35], s71 offen offset:1024 lds\n"
    "v_mul_f32 v40, v40, v40\n"
    "v_mul_f32 v41, v41, v41\n"
    "v_mul_f32 v42, v42, v42\n"
    "v_mfma_f32_32x32x16_bf16 v[16:31], a[104:107], v[56:59], v[16:31]\n"
    "ds_read_b128 v[88:91], v242 offset:2048\n"
    "v_mul_f32 v43, v43, v43\n"
    "v_mul_f32 v44, v44, v44\n"
    "v_mul_f32 v45, v45, v45\n"
    "v_mul_f32 v46, v46, v46\n"
    "v_mul_f32 v47, v47, v47\n"
    "v_mfma_f32_32x32x16_bf16 v[16:31], a[108:111], v[60:63], v[16:31]\n"
    "ds_read_b128 v[92:95], v242 offset:3072\n"
    "s_waitcnt lgkmcnt(6)\n"
    "v_cvt_pk_bf16_f32 v40, v40, v41\n"
    "v_cvt_pk_bf16_f32 v41, v42, v43\n"
    "v_cvt_pk_bf16_f32 v42, v44, v45\n"
    "v_mfma_f32_32x32x16_bf16 v[16:31], a[112:115], v[64:67], v[16:31]\n"
    "ds_read_b128 v[48:51], v242 offset:16384\n"
    "v_add_u32 v240, 0x1c000, v240\n"
    "buffer_load_dwordx4 v8, s[32:35], s71 offen offset:2048 lds\n"
    "v_cvt_pk_bf16_f32 v43, v46, v47\n"
    "buffer_store_dwordx4 v[40:43], v10, s[16:19], s90 offen offset:1024 sc1\n"
    "v_mfma_f32_32x32x16_bf16 v[16:31], a[116:119], v[68:71], v[16:31]\n"
    "ds_read_b128 v[52:55], v242 offset:17408\n"
    "s_waitcnt lgkmcnt(6)\n"
    "s_add_u32 s90, s90, 114688\n"
    "v_max3_u32 v209, v192, v193, v194\n"
    "v_max3_u32 v209, v209, v195, v196\n"
    "v_mfma_f32_32x32x16_bf16 v[16:31], a[120:123], v[72:75], v[16:31]\n"
    "ds_read_b128 v[56:59], v242 offset:18432\n"
    "v_max3_u32 v209, v209, v197, v198\n"
    "v_max_u32 v209, v209, v199\n"
    "v_cmp_eq_u32 vcc, -1, v209\n"
    "s_cbranch_vccnz Ls2r_47\n"
    "Ls2b_48:\n"
    "v_xor_b32 v246, 0x200, v246\n"
    "v_mfma_f32_32x32x16_bf16 v[16:31], a[124:127], v[76:79], v[16:31]\n"
    "ds_read_b128 v[60:63], v242 offset:19456\n"
    "v_add_u32 v242, 0x1c000, v242\n"
    "s_waitcnt lgkmcnt(6)\n"
    "v_add_f32 v192, v192, v194\n"
    "v_add_f32 v193, v193, v195\n"
    "v_add_f32 v196, v196, v198\n"
    "v_mfma_f32_32x32x16_bf16 v[16:31], a[128:131], v[80:83], v[16:31]\n"
    "ds_read_b128 v[64:67], v243 offset:0\n"
    "v_add_f32 v197, v197, v199\n"
    "v_add_f32 v192, v192, v196\n"
    "v_add_f32 v193, v193, v197\n"
    "v_mov_b32 v194, v192\n"
    "v_mov_b32 v195, v193\n"
    "v_mfma_f32_32x32x16_bf16 v[16:31], a[132:135], v[84:87], v[16:31]\n"
    "ds_read_b128 v[68:71], v243 offset:1024\n"
    "s_waitcnt lgkmcnt(6)\n"
    "buffer_load_dwordx4 v9, s[32:35], s71 offen offset:3072 lds\n"
    "s_lshl4_add_u32 s71, s79, s40\n"
    "s_add_u32 m0, s42, 0x10000\n"
    "s_nop 0\n"
    "v_mfma_f32_32x32x16_bf16 v[16:31], a[136:139], v[88:91], v[16:31]\n"
    "ds_read_b128 v[72:75], v243 offset:2048\n"
    "v_permlane32_swap_b32 v192, v194\n"
    "v_permlane32_swap_b32 v193, v195\n"
    "v_add_f32 v192, v192, v194\n"
    "v_mfma_f32_32x32x16_bf16 v[16:31], a[140:143], v[92:95], v[16:31]\n"
    "ds_read_b128 v[76:79], v243 offset:3072\n"
    "s_waitcnt lgkmcnt(6)\n"
    "v_add_f32 v193, v193, v195\n"
    "v_mul_f32 v192, s69, v192\n"
    "v_mul_f32 v193, s69, v193\n"
    "v_fmac_f32 v193, v192, v192\n"
    "v_mfma_f32_32x32x16_bf16 v[16:31], a[144:147], v[48:51], v[16:31]\n"
    "ds_read_b128 v[80:83], v243 offset:16384\n"
    "s_waitcnt vmcnt(7)\n"
    "ds_read_b128 v[48:51], v240 offset:0\n"
    "buffer_load_dwordx4 v6, s[32:35], s71 offen offset:0 lds\n"
    "v_min_f32 v193, 0, v193\n"
    "v_sub_f32 v193, s70, v193\n"
    "v_mfma_f32_32x32x16_bf16 v[16:31], a[148:151], v[52:55], v[16:31]\n"
    "ds_read_b128 v[84:87], v243 offset:17408\n"
    "ds_read_b128 v[52:55], v240 offset:1024\n"
    "s_waitcnt lgkmcnt(8)\n"
    "v_sqrt_f32 v193, v193\n"
    "s_nop 0\n"
    "v_mfma_f32_32x32x16_bf16 v[16:31], a[152:155], v[56:59], v[16:31]\n"
    "ds_read_b128 v[88:91], v243 offset:18432\n"
    "ds_read_b128 v[56:59], v240 offset:2048\n"
    "v_cvt_pk_bf16_f32 v248, v192, v193\n"
    "v_lshlrev_b32 v194, 16, v248\n"
    "v_and_b32 v195, s37, v248\n"
    "v_sub_f32 v192, v192, v194\n"
    "v_mfma_f32_32x32x16_bf16 v[16:31], a[156:159], v[60:63], v[16:31]\n"
    "ds_read_b128 v[92:95], v243 offset:19456\n"
    "v_add_u32 v243, 0x1c000, v243\n"
    "ds_read_b128 v[60:63], v240 offset:3072\n"
    "s_waitcnt lgkmcnt(10)\n"
    "buffer_load_dwordx4 v7, s[32:35], s71 offen offset:1024 lds\n"
    "v_sub_f32 v193, v193, v195\n"
    "v_mfma_f32_32x32x16_bf16 v[16:31], a[160:163], v[64:67], v[16:31]\n"
    "ds_read_b128 v[64:67], v240 offset:16384\n"
    "v_cvt_pk_bf16_f32 v250, v192, v193\n"
    "v_mov_b32 v249, v248\n"
    "v_mov_b32 v251, v250\n"
    "s_waitcnt lgkmcnt(7)\n"
    "v_max3_u32 v208, v48, v49, v50\n"
    "s_waitcnt lgkmcnt(5)\n"
    "v_max3_u32 v208, v208, v51, v52\n"
    "v_mfma_f32_32x32x16_bf16 v[16:31], a[164:167], v[68:71], v[16:31]\n"
    "ds_read_b128 v[68:71], v240 offset:17408\n"
    "v_max3_u32 v208, v208, v53, v54\n"
    "s_waitcnt lgkmcnt(4)\n"
    "v_max3_u32 v208, v208, v55, v56\n"
    "v_max3_u32 v208, v208, v57, v58\n"
    "v_mfma_f32_32x32x16_bf16 v[16:31], a[168:171], v[72:75], v[16:31]\n"
    "ds_read_b128 v[72:75], v240 offset:18432\n"
    "buffer_load_dwordx4 v8, s[32:35], s71 offen offset:2048 lds\n"
    "s_waitcnt lgkmcnt(3)\n"
    "v_max3_u32 v208, v208, v59, v60\n"
    "v_max3_u32 v208, v208, v61, v62\n"
    "s_waitcnt lgkmcnt(2)\n"
    "v_max3_u32 v208, v208, v63, v64\n"
    "v_mfma_f32_32x32x16_bf16 v[16:31], a[172:175], v[76:79], v[16:31]\n"
    "ds_read_b128 v[76:79], v240 offset:19456\n"
    "v_max3_u32 v208, v208, v65, v66\n"
    "s_waitcnt lgkmcnt(2)\n"
    "v_max3_u32 v208, v208, v67, v68\n"
    "v_mfma_f32_32x32x16_bf16 v[16:31], a[176:179], v[80:83], v[16:31]\n"
    "v_max3_u32 v208, v208, v69, v70\n"
    "s_waitcnt lgkmcnt(1)\n"
    "v_max3_u32 v208, v208, v71, v72\n"
    "v_mfma_f32_32x32x16_bf16 v[16:31], a[180:183], v[84:87], v[16:31]\n"
    "buffer_load_dwordx4 v9, s[32:35], s71 offen offset:3072 lds\n"
    "s_lshl4_add_u32 s71, s80, s40\n"
    "s_add_u32 m0, s42, 0x14000\n"
    "v_max3_u32 v208, v208, v73, v74\n"
    "s_waitcnt lgkmcnt(0)\n"
    "v_max3_u32 v208, v208, v75, v76\n"
    "v_mfma_f32_32x32x16_bf16 v[16:31], a[184:187], v[88:91], v[16:31]\n"
    "v_max3_u32 v208, v208, v77, v78\n"
    "v_max_u32 v208, v208, v79\n"
    "v_mfma_f32_32x32x16_bf16 v[16:31], a[188:191], v[92:95], v[16:31]\n"
    "buffer_load_dwordx4 v6, s[32:35], s71 offen offset:0 lds\n"
    "buffer_load_dwordx4 v7, s[32:35], s71 offen offset:1024 lds\n"
    "buffer_load_dwordx4 v8, s[32:35], s71 offen offset:2048 lds\n"
    "buffer_load_dwordx4 v9, s[32:35], s71 offen offset:3072 lds\n"
    "v_cmp_eq_u32 vcc, -1, v208\n"
    "s_cbranch_vccnz Ly3r_49\n"
    "Ly3b_50:\n"
    "s_barrier\n"
    "v_mfma_f32_32x32x16_bf16 v[16:31], v[252:255], v[248:251], v[16:31]\n"
    "v_mfma_f32_32x32x16_bf16 v[32:47], a[64:67], v[48:51], 0\n"
    "ds_read_b128 v[80:83], v241 offset:0\n"
    "s_xor_b32 s67, s91, 0x2000\n"
    "ds_read_b128 v[192:195], v246 offset:12288\n"
    "ds_read_b128 v[196:199], v246 offset:13312\n"
    "v_mfma_f32_32x32x16_bf16 v[32:47], a[68:71], v[52:55], v[32:47]\n"
    "ds_read_b128 v[84:87], v241 offset:1024\n"
    "s_waitcnt vmcnt(9)\n"
    "s_mov_b64 exec, 1\n"
    "buffer_store_dwordx2 v[214:215], off, s[16:19], s67 sc1\n"
    "s_mov_b64 exec, -1\n"
    "v_mfma_f32_32x32x16_bf16 v[32:47], a[72:75], v[56:59], v[32:47]\n"
    "ds_read_b128 v[88:91], v241 offset:2048\n"
    "v_max_f32 v16, 0, v16\n"
    "v_max_f32 v17, 0, v17\n"
    "v_max_f32 v18, 0, v18\n"
    "v_max_f32 v19, 0, v19\n"
    "v_max_f32 v20, 0, v20\n"
    "v_mfma_f32_32x32x16_bf16 v[32:47], a[76:79], v[60:63], v[32:47]\n"
    "ds_read_b128 v[92:95], v241 offset:3072\n"
    "v_max_f32 v21, 0, v21\n"
    "v_max_f32 v22, 0, v22\n"
    "v_max_f32 v23, 0, v23\n"
    "v_mul_f32 v16, v16, v16\n"
    "v_mul_f32 v17, v17, v17\n"
    "v_mfma_f32_32x32x16_bf16 v[32:47], a[80:83], v[64:67], v[32:47]\n"
    "ds_read_b128 v[48:51], v241 offset:16384\n"
    "buffer_load_dwordx2 v[210:211], v212, s[16:19], s92 offen sc1\n"
    "s_lshl4_add_u32 s71, s81, s40\n"
    "s_add_u32 m0, s42, 0x18000\n"
    "v_mul_f32 v18, v18, v18\n"
    "v_mul_f32 v19, v19, v19\n"
    "v_mfma_f32_32x32x16_bf16 v[32:47], a[84:87], v[68:71], v[32:47]\n"
    "ds_read_b128 v[52:55], v241 offset:17408\n"
    "v_mul_f32 v20, v20, v20\n"
    "v_mul_f32 v21, v21, v21\n"
    "v_mul_f32 v22, v22, v22\n"
    "v_mul_f32 v23, v23, v23\n"
    "v_cvt_pk_bf16_f32 v16, v16, v17\n"
    "v_mfma_f32_32x32x16_bf16 v[32:47], a[88:91], v[72:75], v[32:47]\n"
    "ds_read_b128 v[56:59], v241 offset:18432\n"
    "v_cvt_pk_bf16_f32 v17, v18, v19\n"
    "v_cvt_pk_bf16_f32 v18, v20, v21\n"
    "v_cvt_pk_bf16_f32 v19, v22, v23\n"
    "buffer_store_dwordx4 v[16:19], v10, s[16:19], s89 offen offset:0 sc1\n"
    "v_mfma_f32_32x32x16_bf16 v[32:47], a[92:95], v[76:79], v[32:47]\n"
    "ds_read_b128 v[60:63], v241 offset:19456\n"
    "s_waitcnt lgkmcnt(6)\n"
    "v_max_f32 v24, 0, v24\n"
    "v_max_f32 v25, 0, v25\n"
    "v_max_f32 v26, 0, v26\n"
    "v_max_f32 v27, 0, v27\n"
    "v_mfma_f32_32x32x16_bf16 v[32:47], a[96:99], v[80:83], v[32:47]\n"
    "ds_read_b128 v[64:67], v242 offset:0\n"
    "v_max_f32 v28, 0, v28\n"
    "v_max_f32 v29, 0, v29\n"
    "v_max_f32 v30, 0, v30\n"
    "v_max_f32 v31, 0, v31\n"
    "v_mul_f32 v24, v24, v24\n"
    "v_mfma_f32_32x32x16_bf16 v[32:47], a[100:103], v[84:87], v[32:47]\n"
    "ds_read_b128 v[68:71], v242 offset:1024\n"
    "s_waitcnt lgkmcnt(6)\n"
    "buffer_load_dwordx4 v6, s[32:35], s71 offen offset:0 lds\n"
    "v_mul_f32 v25, v25, v25\n"
    "v_mul_f32 v26, v26, v26\n"
    "v_mul_f32 v27, v27, v27\n"
    "v_mfma_f32_32x32x16_bf16 v[32:47], a[104:107], v[88:91], v[32:47]\n"
    "ds_read_b128 v[72:75], v242 offset:2048\n"
    "v_mul_f32 v28, v28, v28\n"
    "v_mul_f32 v29, v29, v29\n"
    "v_mul_f32 v30, v30, v30\n"
    "v_mul_f32 v31, v31, v31\n"
    "v_cvt_pk_bf16_f32 v24, v24, v25\n"
    "v_mfma_f32_32x32x16_bf16 v[32:47], a[108:111], v[92:95], v[32:47]\n"
    "ds_read_b128 v[76:79], v242 offset:3072\n"
    "s_waitcnt lgkmcnt(6)\n"
    "v_cvt_pk_bf16_f32 v25, v26, v27\n"
    "v_cvt_pk_bf16_f32 v26, v28, v29\n"
    "v_cvt_pk_bf16_f32 v27, v30, v31\n"
    "v_mfma_f32_32x32x16_bf16 v[32:47], a[112:115], v[48:51], v[32:47]\n"
    "ds_read_b128 v[80:83], v242 offset:16384\n"
    "buffer_load_dwordx4 v7, s[32:35], s71 offen offset:1024 lds\n"
    "buffer_store_dwordx4 v[24:27], v10, s[16:19], s89 offen offset:1024 sc1\n"
    "v_max3_u32 v209, v192, v193, v194\n"
    "v_max3_u32 v209, v209, v195, v196\n"
    "v_mfma_f32_32x32x16_bf16 v[32:47], a[116:119], v[52:55], v[32:47]\n"
    "ds_read_b128 v[84:87], v242 offset:17408\n"
    "s_waitcnt lgkmcnt(6)\n"
    "v_max3_u32 v209, v209, v197, v198\n"
    "v_max_u32 v209, v209, v199\n"
    "v_cmp_eq_u32 vcc, -1, v209\n"
    "s_cbranch_vccnz Ls3r_51\n"
    "Ls3b_52:\n"
    "v_mfma_f32_32x32x16_bf16 v[32:47], a[120:123], v[56:59], v[32:47]\n"
    "ds_read_b128 v[88:91], v242 offset:18432\n"
    "v_xor_b32 v246, 0x200, v246\n"
    "v_add_f32 v192, v192, v194\n"
    "v_add_f32 v193, v193, v195\n"
    "v_add_f32 v196, v196, v198\n"
    "v_add_f32 v197, v197, v199\n"
    "v_mfma_f32_32x32x16_bf16 v[32:47], a[124:127], v[60:63], v[32:47]\n"
    "ds_read_b128 v[92:95], v242 offset:19456\n"
    "s_waitcnt lgkmcnt(6)\n"
    "v_add_f32 v192, v192, v196\n"
    "v_add_f32 v193, v193, v197\n"
    "v_mov_b32 v194, v192\n"
    "v_mov_b32 v195, v193\n"
    "v_mfma_f32_32x32x16_bf16 v[32:47], a[128:131], v[64:67], v[32:47]\n"
    "ds_read_b128 v[48:51], v243 offset:0\n"
    "s_nop 0\n"
    "v_permlane32_swap_b32 v192, v194\n"
    "v_permlane32_swap_b32 v193, v195\n"
    "v_mfma_f32_32x32x16_bf16 v[32:47], a[132:135], v[68:71], v[32:47]\n"
    "ds_read_b128 v[52:55], v243 offset:1024\n"
    "s_waitcnt lgkmcnt(6)\n"
    "buffer_load_dwordx4 v8, s[32:35], s71 offen offset:2048 lds\n"
    "v_add_f32 v192, v192, v194\n"
    "v_add_f32 v193, v193, v195\n"
    "v_mul_f32 v192, s69, v192\n"
    "v_mfma_f32_32x32x16_bf16 v[32:47], a[136:139], v[72:75], v[32:47]\n"
    "ds_read_b128 v[56:59], v243 offset:2048\n"
    "v_mul_f32 v193, s69, v193\n"
    "v_fmac_f32 v193, v192, v192\n"
    "v_min_f32 v193, 0, v193\n"
    "v_sub_f32 v193, s70, v193\n"
    "v_mfma_f32_32x32x16_bf16 v[32:47], a[140:143], v[76:79], v[32:47]\n"
    "ds_read_b128 v[60:63], v243 offset:3072\n"
    "s_waitcnt lgkmcnt(6)\n"
    "v_sqrt_f32 v193, v193\n"
    "s_nop 0\n"
    "v_cvt_pk_bf16_f32 v248, v192, v193\n"
    "v_mfma_f32_32x32x16_bf16 v[32:47], a[144:147], v[80:83], v[32:47]\n"
    "ds_read_b128 v[64:67], v243 offset:16384\n"
    "buffer_load_dwordx4 v9, s[32:35], s71 offen offset:3072 lds\n"
    "s_waitcnt vmcnt(6)\n"
    "v_cmp_ne_u64 vcc, s[62:63], v[210:211]\n"
    "s_cbranch_vccnz Lhp00r_53\n"
    "Lhp00b_54:\n"
    "s_add_u32 s65, s76, s93\n"
    "v_lshlrev_b32 v194, 16, v248\n"
    "v_mfma_f32_32x32x16_bf16 v[32:47], a[148:151], v[84:87], v[32:47]\n"
    "ds_read_b128 v[68:71], v243 offset:17408\n"
    "s_waitcnt lgkmcnt(6)\n"
    "v_and_b32 v195, s37, v248\n"
    "v_sub_f32 v192, v192, v194\n"
    "v_sub_f32 v193, v193, v195\n"
    "v_cvt_pk_bf16_f32 v250, v192, v193\n"
    "v_mfma_f32_32x32x16_bf16 v[32:47], a[152:155], v[88:91], v[32:47]\n"
    "ds_read_b128 v[72:75], v243 offset:18432\n"
    "v_mov_b32 v249, v248\n"
    "v_mov_b32 v251, v250\n"
    "v_mfma_f32_32x32x16_bf16 v[32:47], a[156:159], v[92:95], v[32:47]\n"
    "ds_read_b128 v[76:79], v243 offset:19456\n"
    "s_waitcnt lgkmcnt(6)\n"
    "buffer_load_dwordx4 v[96:99], v10, s[16:19], s65 offen offset:0 sc1\n"
    "v_mfma_f32_32x32x16_bf16 v[32:47], a[160:163], v[48:51], v[32:47]\n"
    "v_mfma_f32_32x32x16_bf16 v[32:47], a[164:167], v[52:55], v[32:47]\n"
    "s_waitcnt lgkmcnt(4)\n"
    "v_mfma_f32_32x32x16_bf16 v[32:47], a[168:171], v[56:59], v[32:47]\n"
    "buffer_load_dwordx4 v[100:103], v10, s[16:19], s65 offen offset:1024 sc1\n"
    "s_add_u32 s65, s77, s93\n"
    "v_mfma_f32_32x32x16_bf16 v[32:47], a[172:175], v[60:63], v[32:47]\n"
    "s_waitcnt lgkmcnt(2)\n"
    "v_mfma_f32_32x32x16_bf16 v[32:47], a[176:179], v[64:67], v[32:47]\n"
    "v_mfma_f32_32x32x16_bf16 v[32:47], a[180:183], v[68:71], v[32:47]\n"
    "s_waitcnt lgkmcnt(0)\n"
    "buffer_load_dwordx4 v[104:107], v10, s[16:19], s65 offen offset:0 sc1\n"
    "v_mfma_f32_32x32x16_bf16 v[32:47], a[184:187], v[72:75], v[32:47]\n"
    "v_mfma_f32_32x32x16_bf16 v[32:47], a[188:191], v[76:79], v[32:47]\n"
    "v_mfma_f32_32x32x16_bf16 v[32:47], v[252:255], v[248:251], v[32:47]\n"
    "buffer_load_dwordx4 v[108:111], v10, s[16:19], s65 offen offset:1024 sc1\n"
    "buffer_load_dwordx2 v[210:211], v213, s[16:19], s92 offen sc1\n"
    "s_xor_b32 s67, s91, 0x1000\n"
    "s_waitcnt vmcnt(7)\n"
    "s_mov_b64 exec, 1\n"
    "buffer_store_dwordx2 v[214:215], off, s[16:19], s67 sc1\n"
    "s_mov_b64 exec, -1\n"
    "s_branch Lf1end_35\n"
    "Lidle_34:\n"
    "s_waitcnt vmcnt(0) lgkmcnt(0)\n"
    "s_barrier\n"
    "ds_read2_b64 v[112:115], v216 offset0:0 offset1:32\n"
    "ds_read2_b64 v[116:119], v216 offset0:64 offset1:96\n"
    "s_waitcnt lgkmcnt(1)\n"
    "v_add_f32 v112, v112, v114\n"
    "v_add_f32 v113, v113, v115\n"
    "s_waitcnt lgkmcnt(0)\n"
    "v_add_f32 v116, v116, v118\n"
    "v_add_f32 v117, v117, v119\n"
    "v_add_f32 v112, v112, v116\n"
    "v_add_f32 v113, v113, v117\n"
    "v_min_u32 v112, -2, v112\n"
    "v_min_u32 v113, -2, v113\n"
    "s_cmp_lg_u32 s52, 0\n"
    "s_cbranch_scc1 Lsst_55\n"
    "s_mov_b32 exec_hi, 0\n"
    "buffer_store_dwordx2 v[112:113], v0, s[16:19], s59 offen offset:2048 sc1\n"
    "s_mov_b32 exec_hi, -1\n"
    "Lsst_55:\n"
    "v_and_b32 v16, 31, v1\n"
    "v_lshlrev_b32 v16, 10, v16\n"
    "v_lshrrev_b32 v17, 5, v1\n"
    "v_lshl_add_u32 v16, v17, 9, v16\n"
    "s_lshl4_add_u32 s71, s78, s40\n"
    "s_and_b32 s71, s71, 0xfffffdff\n"
    "buffer_load_dword v17, v16, s[32:35], s71 offen\n"
    "s_lshl4_add_u32 s71, s79, s40\n"
    "s_and_b32 s71, s71, 0xfffffdff\n"
    "buffer_load_dword v17, v16, s[32:35], s71 offen\n"
    "s_lshl4_add_u32 s71, s80, s40\n"
    "s_and_b32 s71, s71, 0xfffffdff\n"
    "buffer_load_dword v17, v16, s[32:35], s71 offen\n"
    "s_lshl4_add_u32 s71, s81, s40\n"
    "s_and_b32 s71, s71, 0xfffffdff\n"
    "buffer_load_dword v17, v16, s[32:35], s71 offen\n"
    "s_lshl4_add_u32 s71, s82, s40\n"
    "s_and_b32 s71, s71, 0xfffffdff\n"
    "buffer_load_dword v17, v16, s[32:35], s71 offen\n"
    "ds_read_b64_tr_b16 a[224:225], v11 offset:0\n"
    "ds_read_b64_tr_b16 a[226:227], v11 offset:1024\n"
    "ds_read_b64_tr_b16 a[228:229], v12 offset:0\n"
    "ds_read_b64_tr_b16 a[230:231], v12 offset:1024\n"
    "ds_read_b64_tr_b16 a[232:233], v11 offset:2048\n"
    "ds_read_b64_tr_b16 a[234:235], v11 offset:3072\n"
    "ds_read_b64_tr_b16 a[236:237], v12 offset:2048\n"
    "ds_read_b64_tr_b16 a[238:239], v12 offset:3072\n"
    "ds_read_b64_tr_b16 a[240:241], v11 offset:16384\n"
    "ds_read_b64_tr_b16 a[242:243], v11 offset:17408\n"
    "ds_read_b64_tr_b16 a[244:245], v12 offset:16384\n"
    "ds_read_b64_tr_b16 a[246:247], v12 offset:17408\n"
    "ds_read_b64_tr_b16 a[248:249], v11 offset:18432\n"
    "ds_read_b64_tr_b16 a[250:251], v11 offset:19456\n"
    "ds_read_b64_tr_b16 a[252:253], v12 offset:18432\n"
    "ds_read_b64_tr_b16 a[254:255], v12 offset:19456\n"
    "v_min_u32 v212, 6, v1\n"
    "v_lshlrev_b32 v212, 6, v212\n"
    "s_bfe_u32 s74, s41, 0x10010\n"
    "s_mul_i32 s74, s74, 448\n"
    "s_sub_u32 s73, 448, s74\n"
    "v_add_u32 v213, s73, v212\n"
    "v_add_u32 v212, s74, v212\n"
    "v_mov_b32 v214, s62\n"
    "v_mov_b32 v215, s63\n"
    "s_add_u32 s66, s92, 4096\n"
    "s_waitcnt lgkmcnt(0)\n"
    "s_lshl4_add_u32 s71, s78, s40\n"
    "s_add_u32 m0, s42, 0xc000\n"
    "s_nop 0\n"
    "buffer_load_dwordx4 v6, s[32:35], s71 offen offset:0 lds\n"
    "buffer_load_dwordx4 v7, s[32:35], s71 offen offset:1024 lds\n"
    "buffer_load_dwordx4 v8, s[32:35], s71 offen offset:2048 lds\n"
    "buffer_load_dwordx4 v9, s[32:35], s71 offen offset:3072 lds\n"
    "s_lshl4_add_u32 s71, s79, s40\n"
    "s_add_u32 m0, s42, 0x10000\n"
    "s_nop 0\n"
    "buffer_load_dwordx4 v6, s[32:35], s71 offen offset:0 lds\n"
    "buffer_load_dwordx4 v7, s[32:35], s71 offen offset:1024 lds\n"
    "buffer_load_dwordx4 v8, s[32:35], s71 offen offset:2048 lds\n"
    "buffer_load_dwordx4 v9, s[32:35], s71 offen offset:3072 lds\n"
    "s_lshl4_add_u32 s71, s80, s40\n"
    "s_add_u32 m0, s42, 0x14000\n"
    "s_nop 0\n"
    "buffer_load_dwordx4 v6, s[32:35], s71 offen offset:0 lds\n"
    "buffer_load_dwordx4 v7, s[32:35], s71 offen offset:1024 lds\n"
    "buffer_load_dwordx4 v8, s[32:35], s71 offen offset:2048 lds\n"
    "buffer_load_dwordx4 v9, s[32:35], s71 offen offset:3072 lds\n"
    "s_lshl4_add_u32 s71, s81, s40\n"
    "s_add_u32 m0, s42, 0x18000\n"
    "s_nop 0\n"
    "buffer_load_dwordx4 v6, s[32:35], s71 offen offset:0 lds\n"
    "buffer_load_dwordx4 v7, s[32:35], s71 offen offset:1024 lds\n"
    "buffer_load_dwordx4 v8, s[32:35], s71 offen offset:2048 lds\n"
    "buffer_load_dwordx4 v9, s[32:35], s71 offen offset:3072 lds\n"
    "s_cmp_lg_u32 s2, 255\n"
    "s_cbranch_scc1 Lepx_56\n"
    "s_cmp_lg_u32 s52, 0\n"
    "s_cbranch_scc1 Lepx_56\n"
    "s_mov_b32 s75, 0x8000\n"
    "buffer_load_dwordx4 v[48:51], v0, s[16:19], s75 offen offset:0 sc1\n"
    "buffer_load_dwordx4 v[52:55], v0, s[16:19], s75 offen offset:1024 sc1\n"
    "buffer_load_dwordx4 v[56:59], v0, s[16:19], s75 offen offset:2048 sc1\n"
    "buffer_load_dwordx4 v[60:63], v0, s[16:19], s75 offen offset:3072 sc1\n"
    "s_mov_b32 s75, 0x9000\n"
    "buffer_load_dwordx4 v[64:67], v0, s[16:19], s75 offen offset:0 sc1\n"
    "buffer_load_dwordx4 v[68:71], v0, s[16:19], s75 offen offset:1024 sc1\n"
    "buffer_load_dwordx4 v[72:75], v0, s[16:19], s75 offen offset:2048 sc1\n"
    "buffer_load_dwordx4 v[76:79], v0, s[16:19], s75 offen offset:3072 sc1\n"
    "s_waitcnt vmcnt(7)\n"
    "v_xor_b32 v48, s62, v48\n"
    "v_xor_b32 v49, s63, v49\n"
    "v_xor_b32 v50, s62, v50\n"
    "v_xor_b32 v51, s63, v51\n"
    "s_waitcnt vmcnt(6)\n"
    "v_xor_b32 v52, s62, v52\n"
    "v_xor_b32 v53, s63, v53\n"
    "v_xor_b32 v54, s62, v54\n"
    "v_xor_b32 v55, s63, v55\n"
    "s_waitcnt vmcnt(5)\n"
    "v_xor_b32 v56, s62, v56\n"
    "v_xor_b32 v57, s63, v57\n"
    "v_xor_b32 v58, s62, v58\n"
    "v_xor_b32 v59, s63, v59\n"
    "s_waitcnt vmcnt(4)\n"
    "v_xor_b32 v60, s62, v60\n"
    "v_xor_b32 v61, s63, v61\n"
    "v_xor_b32 v62, s62, v62\n"
    "v_xor_b32 v63, s63, v63\n"
    "s_waitcnt vmcnt(3)\n"
    "v_xor_b32 v64, s62, v64\n"
    "v_xor_b32 v65, s63, v65\n"
    "v_xor_b32 v66, s62, v66\n"
    "v_xor_b32 v67, s63, v67\n"
    "s_waitcnt vmcnt(2)\n"
    "v_xor_b32 v68, s62, v68\n"
    "v_xor_b32 v69, s63, v69\n"
    "v_xor_b32 v70, s62, v70\n"
    "v_xor_b32 v71, s63, v71\n"
    "s_waitcnt vmcnt(1)\n"
    "v_xor_b32 v72, s62, v72\n"
    "v_xor_b32 v73, s63, v73\n"
    "v_xor_b32 v74, s62, v74\n"
    "v_xor_b32 v75, s63, v75\n"
    "s_waitcnt vmcnt(0)\n"
    "v_xor_b32 v76, s62, v76\n"
    "v_xor_b32 v77, s63, v77\n"
    "v_xor_b32 v78, s62, v78\n"
    "v_xor_b32 v79, s63, v79\n"
    "v_or3_b32 v208, v48, v49, v50\n"
    "v_or3_b32 v208, v208, v51, v52\n"
    "v_or3_b32 v208, v208, v53, v54\n"
    "v_or3_b32 v208, v208, v55, v56\n"
    "v_or3_b32 v208, v208, v57, v58\n"
    "v_or3_b32 v208, v208, v59, v60\n"
    "v_or3_b32 v208, v208, v61, v62\n"
    "v_or3_b32 v208, v208, v63, v64\n"
    "v_or3_b32 v208, v208, v65, v66\n"
    "v_or3_b32 v208, v208, v67, v68\n"
    "v_or3_b32 v208, v208, v69, v70\n"
    "v_or3_b32 v208, v208, v71, v72\n"
    "v_or3_b32 v208, v208, v73, v74\n"
    "v_or3_b32 v208, v208, v75, v76\n"
    "v_or3_b32 v208, v208, v77, v78\n"
    "v_or_b32 v208, v208, v79\n"
    "v_cmp_ne_u32 vcc, 0, v208\n"
    "s_cbranch_vccnz Lepr_57\n"
    "Lepb_58:\n"
    "s_mov_b64 exec, 1\n"
    "buffer_store_dwordx2 v[214:215], off, s[16:19], 0 sc1\n"
    "s_mov_b64 exec, -1\n"
    "Lepx_56:\n"
    "s_sleep 12\n"
    "buffer_load_dwordx2 v[210:211], v212, s[16:19], s92 offen sc1\n"
    "s_waitcnt vmcnt(0)\n"
    "v_cmp_ne_u64 vcc, s[62:63], v[210:211]\n"
    "s_cbranch_vccnz Lhp00r_59\n"
    "Lhp00b_60:\n"
    "s_add_u32 s65, s76, s93\n"
    "buffer_load_dwordx4 v[96:99], v10, s[16:19], s65 offen offset:0 sc1\n"
    "buffer_load_dwordx4 v[100:103], v10, s[16:19], s65 offen offset:1024 sc1\n"
    "s_add_u32 s65, s77, s93\n"
    "buffer_load_dwordx4 v[104:107], v10, s[16:19], s65 offen offset:0 sc1\n"
    "buffer_load_dwordx4 v[108:111], v10, s[16:19], s65 offen offset:1024 sc1\n"
    "s_sleep 8\n"
    "buffer_load_dwordx2 v[210:211], v213, s[16:19], s92 offen sc1\n"
    "s_waitcnt vmcnt(0)\n"
    "Lf1end_35:\n"
    "s_waitcnt vmcnt(5)\n"
    "v_mfma_f32_32x32x16_bf16 a[160:175], a[0:3], v[96:99], 0\n"
    "s_add_u32 s65, s78, s93\n"
    "buffer_load_dwordx4 v[112:115], v10, s[16:19], s65 offen offset:0 sc1\n"
    "ds_read_b64_tr_b16 a[64:65], v11 offset:32768\n"
    "ds_read_b64_tr_b16 a[66:67], v11 offset:33792\n"
    "ds_read_b64_tr_b16 a[68:69], v12 offset:32768\n"
    "v_mfma_f32_32x32x16_bf16 a[176:191], a[4:7], v[96:99], 0\n"
    "buffer_load_dwordx4 v[116:119], v10, s[16:19], s65 offen offset:1024 sc1\n"
    "s_add_u32 s65, s79, s93\n"
    "ds_read_b64_tr_b16 a[70:71], v12 offset:33792\n"
    "ds_read_b64_tr_b16 a[72:73], v11 offset:34816\n"
    "ds_read_b64_tr_b16 a[74:75], v11 offset:35840\n"
    "s_waitcnt vmcnt(6)\n"
    "v_mfma_f32_32x32x16_bf16 a[160:175], a[8:11], v[100:103], a[160:175]\n"
    "buffer_load_dwordx4 v[120:123], v10, s[16:19], s65 offen offset:0 sc1\n"
    "ds_read_b64_tr_b16 a[76:77], v12 offset:34816\n"
    "ds_read_b64_tr_b16 a[78:79], v12 offset:35840\n"
    "s_mul_i32 s74, s52, 9216\n"
    "s_add_u32 s74, s74, 0x1c000\n"
    "v_mfma_f32_32x32x16_bf16 a[176:191], a[12:15], v[100:103], a[176:191]\n"
    "buffer_load_dwordx4 v[124:127], v10, s[16:19], s65 offen offset:1024 sc1\n"
    "s_add_u32 s65, s80, s93\n"
    "v_add_u32 v240, s74, v0\n"
    "v_and_b32 v244, 7, v1\n"
    "v_mul_u32_u24 v241, 1088, v244\n"
    "v_lshl_add_u32 v241, v4, 4, v241\n"
    "s_waitcnt vmcnt(7)\n"
    "v_mfma_f32_32x32x16_bf16 a[160:175], a[16:19], v[104:107], a[160:175]\n"
    "buffer_load_dwordx4 v[128:131], v10, s[16:19], s65 offen offset:0 sc1\n"
    "s_lshr_b32 s74, s42, 5\n"
    "s_add_u32 s74, s74, 0x1c000\n"
    "v_add_u32 v241, s74, v241\n"
    "s_lshr_b32 s74, s42, 6\n"
    "s_add_u32 s74, s74, 0x4000\n"
    "v_mfma_f32_32x32x16_bf16 a[176:191], a[20:23], v[104:107], a[176:191]\n"
    "buffer_load_dwordx4 v[132:135], v10, s[16:19], s65 offen offset:1024 sc1\n"
    "s_add_u32 s65, s81, s93\n"
    "v_and_b32 v242, 15, v1\n"
    "v_mul_u32_u24 v242, 272, v242\n"
    "v_and_b32 v243, 48, v1\n"
    "v_add3_u32 v242, v242, v243, s74\n"
    "s_waitcnt vmcnt(8)\n"
    "v_mfma_f32_32x32x16_bf16 a[160:175], a[24:27], v[108:111], a[160:175]\n"
    "buffer_load_dwordx4 v[136:139], v10, s[16:19], s65 offen offset:0 sc1\n"
    "s_mul_i32 s74, s52, 2176\n"
    "s_add_u32 s74, s74, 0x4000\n"
    "v_lshl_add_u32 v243, v1, 5, s74\n"
    "v_lshl_add_u32 v243, v4, 4, v243\n"
    "v_mfma_f32_32x32x16_bf16 a[176:191], a[28:31], v[108:111], a[176:191]\n"
    "buffer_load_dwordx4 v[140:143], v10, s[16:19], s65 offen offset:1024 sc1\n"
    "s_add_u32 s65, s82, s93\n"
    "s_and_b32 s73, s58, 0x3ff\n"
    "v_lshlrev_b32 v244, 4, v244\n"
    "s_add_u32 s74, s73, 0x26860\n"
    "v_add_u32 v245, s74, v244\n"
    "s_waitcnt vmcnt(7)\n"
    "v_mfma_f32_32x32x16_bf16 a[160:175], a[32:35], v[112:115], a[160:175]\n"
    "buffer_load_dwordx4 v[144:147], v10, s[16:19], s65 offen offset:0 sc1\n"
    "v_lshlrev_b32 v246, 2, v4\n"
    "v_lshl_add_u32 v247, v4, 9, v244\n"
    "ds_read_b64_tr_b16 a[80:81], v11 offset:49152\n"
    "ds_read_b64_tr_b16 a[82:83], v11 offset:50176\n"
    "v_mfma_f32_32x32x16_bf16 a[176:191], a[36:39], v[112:115], a[176:191]\n"
    "buffer_load_dwordx4 v[148:151], v10, s[16:19], s65 offen offset:1024 sc1\n"
    "v_cmp_ne_u64 vcc, s[62:63], v[210:211]\n"
    "s_cbranch_vccnz Lhp01r_61\n"
    "Lhp01b_62:\n"
    "s_add_u32 s65, s76, s94\n"
    "ds_read_b64_tr_b16 a[84:85], v12 offset:49152\n"
    "ds_read_b64_tr_b16 a[86:87], v12 offset:50176\n"
    "s_waitcnt vmcnt(8)\n"
    "v_mfma_f32_32x32x16_bf16 a[160:175], a[40:43], v[116:119], a[160:175]\n"
    "buffer_load_dwordx4 v[152:155], v10, s[16:19], s65 offen offset:0 sc1\n"
    "ds_read_b64_tr_b16 a[88:89], v11 offset:51200\n"
    "ds_read_b64_tr_b16 a[90:91], v11 offset:52224\n"
    "ds_read_b64_tr_b16 a[92:93], v12 offset:51200\n"
    "v_max_f32 v32, 0, v32\n"
    "v_max_f32 v33, 0, v33\n"
    "v_mfma_f32_32x32x16_bf16 a[176:191], a[44:47], v[116:119], a[176:191]\n"
    "buffer_load_dwordx4 v[156:159], v10, s[16:19], s65 offen offset:1024 sc1\n"
    "s_add_u32 s65, s77, s94\n"
    "ds_read_b64_tr_b16 a[94:95], v12 offset:52224\n"
    "ds_read_b64_tr_b16 a[96:97], v206 offset:0\n"
    "ds_read_b64_tr_b16 a[98:99], v206 offset:1024\n"
    "s_waitcnt vmcnt(9)\n"
    "v_mfma_f32_32x32x16_bf16 a[160:175], a[48:51], v[120:123], a[160:175]\n"
    "buffer_load_dwordx4 v[160:163], v10, s[16:19], s65 offen offset:0 sc1\n"
    "s_lshl4_add_u32 s71, s82, s40\n"
    "s_add_u32 m0, s42, 0x0\n"
    "ds_read_b64_tr_b16 a[100:101], v207 offset:0\n"
    "ds_read_b64_tr_b16 a[102:103], v207 offset:1024\n"
    "ds_read_b64_tr_b16 a[104:105], v206 offset:2048\n"
    "v_mfma_f32_32x32x16_bf16 a[176:191], a[52:55], v[120:123], a[176:191]\n"
    "buffer_load_dwordx4 v6, s[32:35], s71 offen offset:0 lds\n"
    "ds_read_b64_tr_b16 a[106:107], v206 offset:3072\n"
    "ds_read_b64_tr_b16 a[108:109], v207 offset:2048\n"
    "ds_read_b64_tr_b16 a[110:111], v207 offset:3072\n"
    "v_max_f32 v34, 0, v34\n"
    "s_waitcnt vmcnt(10)\n"
    "v_mfma_f32_32x32x16_bf16 a[160:175], a[56:59], v[124:127], a[160:175]\n"
    "buffer_load_dwordx4 v7, s[32:35], s71 offen offset:1024 lds\n"
    "ds_read_b64_tr_b16 a[112:113], v206 offset:16384\n"
    "ds_read_b64_tr_b16 a[114:115], v206 offset:17408\n"
    "ds_read_b64_tr_b16 a[116:117], v207 offset:16384\n"
    "v_max_f32 v35, 0, v35\n"
    "v_max_f32 v36, 0, v36\n"
    "v_mfma_f32_32x32x16_bf16 a[176:191], a[60:63], v[124:127], a[176:191]\n"
    "buffer_load_dwordx4 v8, s[32:35], s71 offen offset:2048 lds\n"
    "ds_read_b64_tr_b16 a[118:119], v207 offset:17408\n"
    "ds_read_b64_tr_b16 a[120:121], v206 offset:18432\n"
    "ds_read_b64_tr_b16 a[122:123], v206 offset:19456\n"
    "v_max_f32 v37, 0, v37\n"
    "v_max_f32 v38, 0, v38\n"
    "s_waitcnt vmcnt(11)\n"
    "v_mfma_f32_32x32x16_bf16 a[160:175], a[224:227], v[128:131], a[160:175]\n"
    "buffer_load_dwordx4 v9, s[32:35], s71 offen offset:3072 lds\n"
    "ds_read_b64_tr_b16 a[124:125], v207 offset:18432\n"
    "ds_read_b64_tr_b16 a[126:127], v207 offset:19456\n"
    "ds_read_b64_tr_b16 a[128:129], v206 offset:32768\n"
    "v_max_f32 v39, 0, v39\n"
    "v_mfma_f32_32x32x16_bf16 a[176:191], a[228:231], v[128:131], a[176:191]\n"
    "buffer_load_dwordx4 v[20:23], v244, s[20:23], s73 offen\n"
    "ds_read_b64_tr_b16 a[130:131], v206 offset:33792\n"
    "ds_read_b64_tr_b16 a[132:133], v207 offset:32768\n"
    "ds_read_b64_tr_b16 a[134:135], v207 offset:33792\n"
    "v_mul_f32 v32, v32, v32\n"
    "v_mul_f32 v33, v33, v33\n"
    "s_waitcnt vmcnt(12)\n"
    "v_mfma_f32_32x32x16_bf16 a[160:175], a[232:235], v[132:135], a[160:175]\n"
    "buffer_load_dwordx4 v[164:167], v10, s[16:19], s65 offen offset:1024 sc1\n"
    "s_add_u32 s65, s78, s94\n"
    "ds_read_b64_tr_b16 a[136:137], v206 offset:34816\n"
    "ds_read_b64_tr_b16 a[138:139], v206 offset:35840\n"
    "ds_read_b64_tr_b16 a[140:141], v207 offset:34816\n"
    "v_mul_f32 v34, v34, v34\n"
    "v_mfma_f32_32x32x16_bf16 a[176:191], a[236:239], v[132:135], a[176:191]\n"
    "buffer_load_dwordx4 v[168:171], v10, s[16:19], s65 offen offset:0 sc1\n"
    "ds_read_b64_tr_b16 a[142:143], v207 offset:35840\n"
    "v_mul_f32 v35, v35, v35\n"
    "v_mul_f32 v36, v36, v36\n"
    "v_mul_f32 v37, v37, v37\n"
    "v_mul_f32 v38, v38, v38\n"
    "s_waitcnt vmcnt(13)\n"
    "v_mfma_f32_32x32x16_bf16 a[160:175], a[240:243], v[136:139], a[160:175]\n"
    "buffer_load_dwordx4 v[172:175], v10, s[16:19], s65 offen offset:1024 sc1\n"
    "s_add_u32 s65, s79, s94\n"
    "v_mul_f32 v39, v39, v39\n"
    "v_cvt_pk_bf16_f32 v32, v32, v33\n"
    "v_cvt_pk_bf16_f32 v33, v34, v35\n"
    "v_cvt_pk_bf16_f32 v34, v36, v37\n"
    "v_mfma_f32_32x32x16_bf16 a[176:191], a[244:247], v[136:139], a[176:191]\n"
    "buffer_load_dwordx4 v[176:179], v10, s[16:19], s65 offen offset:0 sc1\n"
    "v_cvt_pk_bf16_f32 v35, v38, v39\n"
    "v_max_f32 v40, 0, v40\n"
    "v_max_f32 v41, 0, v41\n"
    "v_max_f32 v42, 0, v42\n"
    "v_max_f32 v43, 0, v43\n"
    "s_waitcnt vmcnt(14)\n"
    "v_mfma_f32_32x32x16_bf16 a[160:175], a[248:251], v[140:143], a[160:175]\n"
    "buffer_load_dwordx4 v[180:183], v10, s[16:19], s65 offen offset:1024 sc1\n"
    "v_max_f32 v44, 0, v44\n"
    "v_max_f32 v45, 0, v45\n"
    "v_max_f32 v46, 0, v46\n"
    "v_max_f32 v47, 0, v47\n"
    "v_mul_f32 v40, v40, v40\n"
    "v_mfma_f32_32x32x16_bf16 a[176:191], a[252:255], v[140:143], a[176:191]\n"
    "s_add_u32 s65, s80, s94\n"
    "buffer_load_dwordx4 v[184:187], v10, s[16:19], s65 offen offset:0 sc1\n"
    "v_mul_f32 v41, v41, v41\n"
    "v_mul_f32 v42, v42, v42\n"
    "v_mul_f32 v43, v43, v43\n"
    "v_mul_f32 v44, v44, v44\n"
    "s_waitcnt vmcnt(15) lgkmcnt(14)\n"
    "v_mfma_f32_32x32x16_bf16 a[160:175], a[64:67], v[144:147], a[160:175]\n"
    "v_mul_f32 v45, v45, v45\n"
    "v_mul_f32 v46, v46, v46\n"
    "v_mul_f32 v47, v47, v47\n"
    "v_cvt_pk_bf16_f32 v40, v40, v41\n"
    "v_cvt_pk_bf16_f32 v41, v42, v43\n"
    "v_cvt_pk_bf16_f32 v42, v44, v45\n"
    "v_mfma_f32_32x32x16_bf16 a[176:191], a[68:71], v[144:147], a[176:191]\n"
    "buffer_load_dwordx4 v[188:191], v10, s[16:19], s65 offen offset:1024 sc1\n"
    "v_cvt_pk_bf16_f32 v43, v46, v47\n"
    "buffer_store_dwordx4 v[32:35], v10, s[16:19], s90 offen offset:0 sc1\n"
    "buffer_store_dwordx4 v[40:43], v10, s[16:19], s90 offen offset:1024 sc1\n"
    "s_waitcnt vmcnt(17)\n"
    "v_mfma_f32_32x32x16_bf16 a[160:175], a[72:75], v[148:151], a[160:175]\n"
    "s_barrier\n"
    "v_mfma_f32_32x32x16_bf16 a[176:191], a[76:79], v[148:151], a[176:191]\n"
    "s_waitcnt vmcnt(16)\n"
    "v_mfma_f32_32x32x16_bf16 a[160:175], a[192:195], v[152:155], a[160:175]\n"
    "buffer_load_dwordx2 v[210:211], v212, s[16:19], s66 offen sc1\n"
    "s_add_u32 s65, s81, s94\n"
    "v_mfma_f32_32x32x16_bf16 a[176:191], a[196:199], v[152:155], a[176:191]\n"
    "buffer_load_dwordx4 v[96:99], v10, s[16:19], s65 offen offset:0 sc1\n"
    "s_waitcnt vmcnt(17)\n"
    "v_mfma_f32_32x32x16_bf16 a[160:175], a[200:203], v[156:159], a[160:175]\n"
    "buffer_load_dwordx4 v[100:103], v10, s[16:19], s65 offen offset:1024 sc1\n"
    "ds_write_b128 v242, v[224:227] offset:0\n"
    "v_mfma_f32_32x32x16_bf16 a[176:191], a[204:207], v[156:159], a[176:191]\n"
    "s_add_u32 s65, s82, s94\n"
    "buffer_load_dwordx4 v[104:107], v10, s[16:19], s65 offen offset:0 sc1\n"
    "s_waitcnt vmcnt(18)\n"
    "v_mfma_f32_32x32x16_bf16 a[160:175], a[208:211], v[160:163], a[160:175]\n"
    "ds_write_b128 v242, v[228:231] offset:4352\n"
    "v_mfma_f32_32x32x16_bf16 a[176:191], a[212:215], v[160:163], a[176:191]\n"
    "buffer_load_dwordx4 v[108:111], v10, s[16:19], s65 offen offset:1024 sc1\n"
    "s_add_u32 s93, s93, 114688\n"
    "s_waitcnt vmcnt(4)\n"
    "v_cmp_ne_u64 vcc, s[62:63], v[210:211]\n"
    "s_cbranch_vccnz Lhp10r_63\n"
    "Lhp10b_64:\n"
    "v_mfma_f32_32x32x16_bf16 a[160:175], a[216:219], v[164:167], a[160:175]\n"
    "ds_write_b128 v242, v[232:235] offset:8704\n"
    "v_mfma_f32_32x32x16_bf16 a[176:191], a[220:223], v[164:167], a[176:191]\n"
    "s_add_u32 s65, s76, s93\n"
    "buffer_load_dwordx4 v[112:115], v10, s[16:19], s65 offen offset:0 sc1\n"
    "v_mfma_f32_32x32x16_bf16 a[160:175], a[80:83], v[168:171], a[160:175]\n"
    "ds_write_b128 v242, v[236:239] offset:13056\n"
    "ds_read_b128 v[16:19], v245\n"
    "v_mfma_f32_32x32x16_bf16 a[176:191], a[84:87], v[168:171], a[176:191]\n"
    "buffer_load_dwordx4 v[116:119], v10, s[16:19], s65 offen offset:1024 sc1\n"
    "v_mfma_f32_32x32x16_bf16 a[160:175], a[88:91], v[172:175], a[160:175]\n"
    "v_mfma_f32_32x32x16_bf16 a[176:191], a[92:95], v[172:175], a[176:191]\n"
    "s_add_u32 s65, s77, s93\n"
    "buffer_load_dwordx4 v[120:123], v10, s[16:19], s65 offen offset:0 sc1\n"
    "v_mfma_f32_32x32x16_bf16 a[160:175], a[96:99], v[176:179], a[160:175]\n"
    "s_waitcnt lgkmcnt(0)\n"
    "v_lshlrev_b32 v192, 16, v16\n"
    "v_and_b32 v193, s37, v16\n"
    "v_lshlrev_b32 v194, 16, v17\n"
    "v_and_b32 v195, s37, v17\n"
    "v_lshlrev_b32 v196, 16, v18\n"
    "v_and_b32 v197, s37, v18\n"
    "v_mfma_f32_32x32x16_bf16 a[176:191], a[100:103], v[176:179], a[176:191]\n"
    "buffer_load_dwordx4 v[124:127], v10, s[16:19], s65 offen offset:1024 sc1\n"
    "v_lshlrev_b32 v198, 16, v19\n"
    "v_and_b32 v199, s37, v19\n"
    "v_lshlrev_b32 v248, 16, v20\n"
    "v_and_b32 v249, s37, v20\n"
    "v_lshlrev_b32 v250, 16, v21\n"
    "v_mfma_f32_32x32x16_bf16 a[160:175], a[104:107], v[180:183], a[160:175]\n"
    "s_and_b32 s72, s59, 0xfffff000\n"
    "s_lshr_b32 s67, s42, 5\n"
    "s_add_u32 s72, s72, s67\n"
    "buffer_load_dwordx4 v[216:219], v247, s[16:19], s72 offen sc1\n"
    "v_and_b32 v251, s37, v21\n"
    "v_lshlrev_b32 v252, 16, v22\n"
    "v_mfma_f32_32x32x16_bf16 a[176:191], a[108:111], v[180:183], a[176:191]\n"
    "s_add_u32 s65, s78, s93\n"
    "buffer_load_dwordx4 v[128:131], v10, s[16:19], s65 offen offset:0 sc1\n"
    "v_and_b32 v253, s37, v22\n"
    "v_lshlrev_b32 v254, 16, v23\n"
    "v_and_b32 v255, s37, v23\n"
    "v_mfma_f32_32x32x16_bf16 a[160:175], a[112:115], v[184:187], a[160:175]\n"
    "v_mfma_f32_32x32x16_bf16 a[176:191], a[116:119], v[184:187], a[176:191]\n"
    "buffer_load_dwordx4 v[132:135], v10, s[16:19], s65 offen offset:1024 sc1\n"
    "v_mfma_f32_32x32x16_bf16 a[160:175], a[120:123], v[188:191], a[160:175]\n"
    "ds_read_b64_tr_b16 a[144:145], v11 offset:0\n"
    "ds_read_b64_tr_b16 a[146:147], v11 offset:1024\n"
    "ds_read_b64_tr_b16 a[148:149], v12 offset:0\n"
    "v_mfma_f32_32x32x16_bf16 a[176:191], a[124:127], v[188:191], a[176:191]\n"
    "s_add_u32 s65, s79, s93\n"
    "buffer_load_dwordx4 v[136:139], v10, s[16:19], s65 offen offset:0 sc1\n"
    "ds_read_b64_tr_b16 a[150:151], v12 offset:1024\n"
    "ds_read_b64_tr_b16 a[152:153], v11 offset:2048\n"
    "ds_read_b64_tr_b16 a[154:155], v11 offset:3072\n"
    "s_waitcnt vmcnt(11)\n"
    "v_mfma_f32_32x32x16_bf16 a[160:175], a[128:131], v[96:99], a[160:175]\n"
    "ds_read_b64_tr_b16 a[156:157], v12 offset:2048\n"
    "ds_read_b64_tr_b16 a[158:159], v12 offset:3072\n"
    "s_barrier\n"
    "ds_read_b128 v[224:227], v243 offset:0\n"
    "v_mfma_f32_32x32x16_bf16 a[176:191], a[132:135], v[96:99], a[176:191]\n"
    "buffer_load_dwordx4 v[140:143], v10, s[16:19], s65 offen offset:1024 sc1\n"
    "ds_read_b128 v[228:231], v243 offset:16\n"
    "ds_read_b128 v[232:235], v243 offset:8704\n"
    "ds_read_b128 v[236:239], v243 offset:8720\n"
    "s_waitcnt vmcnt(11)\n"
    "v_mfma_f32_32x32x16_bf16 a[160:175], a[136:139], v[100:103], a[160:175]\n"
    "v_mfma_f32_32x32x16_bf16 a[176:191], a[140:143], v[100:103], a[176:191]\n"
    "s_add_u32 s65, s80, s93\n"
    "buffer_load_dwordx4 v[144:147], v10, s[16:19], s65 offen offset:0 sc1\n"
    "s_waitcnt vmcnt(11) lgkmcnt(10)\n"
    "v_mfma_f32_32x32x16_bf16 a[160:175], a[144:147], v[104:107], a[160:175]\n"
    "s_waitcnt lgkmcnt(8)\n"
    "v_mfma_f32_32x32x16_bf16 a[176:191], a[148:151], v[104:107], a[176:191]\n"
    "buffer_load_dwordx4 v[148:151], v10, s[16:19], s65 offen offset:1024 sc1\n"
    "s_waitcnt vmcnt(11) lgkmcnt(6)\n"
    "v_mfma_f32_32x32x16_bf16 a[160:175], a[152:155], v[108:111], a[160:175]\n"
    "s_waitcnt lgkmcnt(4)\n"
    "v_mfma_f32_32x32x16_bf16 a[176:191], a[156:159], v[108:111], a[176:191]\n"
    "s_add_u32 s65, s81, s93\n"
    "buffer_load_dwordx4 v[152:155], v10, s[16:19], s65 offen offset:0 sc1\n"
    "s_waitcnt vmcnt(11)\n"
    "v_mfma_f32_32x32x16_bf16 v[48:63], a[0:3], v[112:115], 0\n"
    "v_mfma_f32_32x32x16_bf16 v[64:79], a[4:7], v[112:115], 0\n"
    "buffer_load_dwordx4 v[156:159], v10, s[16:19], s65 offen offset:1024 sc1\n"
    "s_waitcnt vmcnt(11)\n"
    "v_mfma_f32_32x32x16_bf16 v[48:63], a[8:11], v[116:119], v[48:63]\n"
    "s_nop 1\n"
    "ds_write_b128 v240, a[160:163] offset:0\n"
    "v_mfma_f32_32x32x16_bf16 v[64:79], a[12:15], v[116:119], v[64:79]\n"
    "s_add_u32 s65, s82, s93\n"
    "buffer_load_dwordx4 v[160:163], v10, s[16:19], s65 offen offset:0 sc1\n"
    "s_waitcnt vmcnt(11)\n"
    "v_mfma_f32_32x32x16_bf16 v[48:63], a[16:19], v[120:123], v[48:63]\n"
    "ds_write_b128 v240, a[164:167] offset:1088\n"
    "v_mfma_f32_32x32x16_bf16 v[64:79], a[20:23], v[120:123], v[64:79]\n"
    "buffer_load_dwordx4 v[164:167], v10, s[16:19], s65 offen offset:1024 sc1\n"
    "s_add_u32 s94, s94, 114688\n"
    "s_waitcnt vmcnt(11)\n"
    "v_mfma_f32_32x32x16_bf16 v[48:63], a[24:27], v[124:127], v[48:63]\n"
    "ds_write_b128 v240, a[168:171] offset:2176\n"
    "v_mfma_f32_32x32x16_bf16 v[64:79], a[28:31], v[124:127], v[64:79]\n"
    "s_add_u32 s65, s76, s94\n"
    "buffer_load_dwordx4 v[168:171], v10, s[16:19], s65 offen offset:0 sc1\n"
    "s_waitcnt vmcnt(10)\n"
    "v_mfma_f32_32x32x16_bf16 v[48:63], a[32:35], v[128:131], v[48:63]\n"
    "ds_write_b128 v240, a[172:175] offset:3264\n"
    "v_mfma_f32_32x32x16_bf16 v[64:79], a[36:39], v[128:131], v[64:79]\n"
    "buffer_load_dwordx4 v[172:175], v10, s[16:19], s65 offen offset:1024 sc1\n"
    "ds_write_b128 v240, a[176:179] offset:4352\n"
    "s_waitcnt vmcnt(10)\n"
    "v_mfma_f32_32x32x16_bf16 v[48:63], a[40:43], v[132:135], v[48:63]\n"
    "ds_write_b128 v240, a[180:183] offset:5440\n"
    "v_mfma_f32_32x32x16_bf16 v[64:79], a[44:47], v[132:135], v[64:79]\n"
    "s_add_u32 s65, s77, s94\n"
    "buffer_load_dwordx4 v[176:179], v10, s[16:19], s65 offen offset:0 sc1\n"
    "s_waitcnt vmcnt(10)\n"
    "v_mfma_f32_32x32x16_bf16 v[48:63], a[48:51], v[136:139], v[48:63]\n"
    "ds_write_b128 v240, a[184:187] offset:6528\n"
    "v_mfma_f32_32x32x16_bf16 v[64:79], a[52:55], v[136:139], v[64:79]\n"
    "buffer_load_dwordx4 v[180:183], v10, s[16:19], s65 offen offset:1024 sc1\n"
    "ds_write_b128 v240, a[188:191] offset:7616\n"
    "s_waitcnt vmcnt(10)\n"
    "v_mfma_f32_32x32x16_bf16 v[48:63], a[56:59], v[140:143], v[48:63]\n"
    "s_waitcnt lgkmcnt(0)\n"
    "s_barrier\n"
    "v_mfma_f32_32x32x16_bf16 v[64:79], a[60:63], v[140:143], v[64:79]\n"
    "s_add_u32 s65, s78, s94\n"
    "buffer_load_dwordx4 v[184:187], v10, s[16:19], s65 offen offset:0 sc1\n"
    "ds_read_b128 v[16:19], v241 offset:0\n"
    "ds_read_b128 v[20:23], v241 offset:512\n"
    "s_waitcnt vmcnt(10)\n"
    "v_mfma_f32_32x32x16_bf16 v[48:63], a[224:227], v[144:147], v[48:63]\n"
    "ds_read_b128 v[24:27], v241 offset:9216\n"
    "ds_read_b128 v[28:31], v241 offset:9728\n"
    "v_mfma_f32_32x32x16_bf16 v[64:79], a[228:231], v[144:147], v[64:79]\n"
    "buffer_load_dwordx4 v[188:191], v10, s[16:19], s65 offen offset:1024 sc1\n"
    "ds_read_b128 v[32:35], v241 offset:18432\n"
    "ds_read_b128 v[36:39], v241 offset:18944\n"
    "s_waitcnt vmcnt(10)\n"
    "v_mfma_f32_32x32x16_bf16 v[48:63], a[232:235], v[148:151], v[48:63]\n"
    "ds_read_b128 v[40:43], v241 offset:27648\n"
    "ds_read_b128 v[44:47], v241 offset:28160\n"
    "v_add_f32 v216, v216, v218\n"
    "v_add_f32 v217, v217, v219\n"
    "s_nop 0\n"
    "v_add_f32_dpp v216, v216, v216 row_ror:8 row_mask:0xf bank_mask:0xf\n"
    "v_mfma_f32_32x32x16_bf16 v[64:79], a[236:239], v[148:151], v[64:79]\n"
    "s_add_u32 s65, s79, s94\n"
    "buffer_load_dwordx4 v[96:99], v10, s[16:19], s65 offen offset:0 sc1\n"
    "v_add_f32_dpp v217, v217, v217 row_ror:8 row_mask:0xf bank_mask:0xf\n"
    "v_mov_b32 v218, v216\n"
    "v_mov_b32 v219, v217\n"
    "s_nop 0\n"
    "s_waitcnt vmcnt(10)\n"
    "v_mfma_f32_32x32x16_bf16 v[48:63], a[240:243], v[152:155], v[48:63]\n"
    "v_permlane16_swap_b32 v216, v218\n"
    "v_permlane16_swap_b32 v217, v219\n"
    "v_add_f32 v216, v216, v218\n"
    "v_add_f32 v217, v217, v219\n"
    "v_mfma_f32_32x32x16_bf16 v[64:79], a[244:247], v[152:155], v[64:79]\n"
    "buffer_load_dwordx4 v[100:103], v10, s[16:19], s65 offen offset:1024 sc1\n"
    "v_mul_f32 v216, s69, v216\n"
    "v_mul_f32 v217, s69, v217\n"
    "v_fmac_f32 v217, v216, v216\n"
    "v_min_f32 v217, 0, v217\n"
    "v_sub_f32 v217, s70, v217\n"
    "s_waitcnt vmcnt(10)\n"
    "v_mfma_f32_32x32x16_bf16 v[48:63], a[248:251], v[156:159], v[48:63]\n"
    "v_rcp_f32 v217, v217\n"
    "s_nop 0\n"
    "ds_bpermute_b32 v220, v246, v217\n"
    "ds_bpermute_b32 v221, v246, v217 offset:128\n"
    "s_waitcnt lgkmcnt(7)\n"
    "v_add_f32 v16, v16, v24\n"
    "v_mfma_f32_32x32x16_bf16 v[64:79], a[252:255], v[156:159], v[64:79]\n"
    "s_add_u32 s65, s80, s94\n"
    "buffer_load_dwordx4 v[104:107], v10, s[16:19], s65 offen offset:0 sc1\n"
    "v_add_f32 v17, v17, v25\n"
    "v_add_f32 v18, v18, v26\n"
    "v_add_f32 v19, v19, v27\n"
    "s_waitcnt lgkmcnt(6)\n"
    "v_add_f32 v20, v20, v28\n"
    "s_waitcnt vmcnt(10)\n"
    "v_mfma_f32_32x32x16_bf16 v[48:63], a[64:67], v[160:163], v[48:63]\n"
    "v_add_f32 v21, v21, v29\n"
    "v_add_f32 v22, v22, v30\n"
    "v_add_f32 v23, v23, v31\n"
    "s_waitcnt lgkmcnt(3)\n"
    "v_add_f32 v32, v32, v40\n"
    "v_add_f32 v33, v33, v41\n"
    "v_add_f32 v34, v34, v42\n"
    "v_mfma_f32_32x32x16_bf16 v[64:79], a[68:71], v[160:163], v[64:79]\n"
    "s_waitcnt vmcnt(8)\n"
    "v_max3_u32 v208, v168, v169, v170\n"
    "s_waitcnt vmcnt(7)\n"
    "v_max3_u32 v208, v208, v171, v172\n"
    "v_max3_u32 v208, v208, v173, v174\n"
    "buffer_load_dwordx4 v[108:111], v10, s[16:19], s65 offen offset:1024 sc1\n"
    "v_add_f32 v35, v35, v43\n"
    "s_waitcnt lgkmcnt(2)\n"
    "v_add_f32 v36, v36, v44\n"
    "v_mfma_f32_32x32x16_bf16 v[48:63], a[72:75], v[164:167], v[48:63]\n"
    "s_waitcnt vmcnt(7)\n"
    "v_max3_u32 v208, v208, v175, v176\n"
    "v_max3_u32 v208, v208, v177, v178\n"
    "s_waitcnt vmcnt(6)\n"
    "v_max3_u32 v208, v208, v179, v180\n"
    "v_add_f32 v37, v37, v45\n"
    "v_add_f32 v38, v38, v46\n"
    "v_add_f32 v39, v39, v47\n"
    "v_mfma_f32_32x32x16_bf16 v[64:79], a[76:79], v[164:167], v[64:79]\n"
    "v_max3_u32 v208, v208, v181, v182\n"
    "v_max_u32 v208, v208, v183\n"
    "v_cmp_eq_u32 vcc, -1, v208\n"
    "s_cbranch_vccnz Lhsr_65\n"
    "Lhsb_66:\n"
    "s_add_u32 s65, s81, s94\n"
    "buffer_load_dwordx4 v[112:115], v10, s[16:19], s65 offen offset:0 sc1\n"
    "v_mfma_f32_32x32x16_bf16 v[48:63], a[192:195], v[168:171], v[48:63]\n"
    "v_add_f32 v16, v16, v32\n"
    "v_add_f32 v17, v17, v33\n"
    "v_add_f32 v18, v18, v34\n"
    "v_add_f32 v19, v19, v35\n"
    "v_add_f32 v20, v20, v36\n"
    "v_add_f32 v21, v21, v37\n"
    "v_mfma_f32_32x32x16_bf16 v[64:79], a[196:199], v[168:171], v[64:79]\n"
    "buffer_load_dwordx4 v[116:119], v10, s[16:19], s65 offen offset:1024 sc1\n"
    "v_add_f32 v22, v22, v38\n"
    "v_add_f32 v23, v23, v39\n"
    "s_waitcnt lgkmcnt(1)\n"
    "v_fma_f32 v16, v16, v220, v248\n"
    "v_fma_f32 v17, v17, v220, v249\n"
    "v_fma_f32 v18, v18, v220, v250\n"
    "v_mfma_f32_32x32x16_bf16 v[48:63], a[200:203], v[172:175], v[48:63]\n"
    "v_fma_f32 v19, v19, v220, v251\n"
    "v_fma_f32 v20, v20, v220, v252\n"
    "v_fma_f32 v21, v21, v220, v253\n"
    "v_fma_f32 v22, v22, v220, v254\n"
    "v_fma_f32 v23, v23, v220, v255\n"
    "v_mfma_f32_32x32x16_bf16 v[64:79], a[204:207], v[172:175], v[64:79]\n"
    "s_add_u32 s65, s82, s94\n"
    "buffer_load_dwordx4 v[120:123], v10, s[16:19], s65 offen offset:0 sc1\n"
    "v_fmac_f32 v224, v16, v192\n"
    "v_fmac_f32 v225, v17, v193\n"
    "v_fmac_f32 v226, v18, v194\n"
    "v_fmac_f32 v227, v19, v195\n"
    "v_mfma_f32_32x32x16_bf16 v[48:63], a[208:211], v[176:179], v[48:63]\n"
    "v_fmac_f32 v228, v20, v196\n"
    "v_fmac_f32 v229, v21, v197\n"
    "v_fmac_f32 v230, v22, v198\n"
    "v_fmac_f32 v231, v23, v199\n"
    "v_cvt_pk_bf16_f32 v16, v224, v225\n"
    "v_cvt_pk_bf16_f32 v17, v226, v227\n"
    "v_mfma_f32_32x32x16_bf16 v[64:79], a[212:215], v[176:179], v[64:79]\n"
    "s_waitcnt vmcnt(8)\n"
    "v_max3_u32 v208, v184, v185, v186\n"
    "s_waitcnt vmcnt(7)\n"
    "v_max3_u32 v208, v208, v187, v188\n"
    "v_max3_u32 v208, v208, v189, v190\n"
    "buffer_load_dwordx4 v[124:127], v10, s[16:19], s65 offen offset:1024 sc1\n"
    "v_cvt_pk_bf16_f32 v18, v228, v229\n"
    "v_mfma_f32_32x32x16_bf16 v[48:63], a[216:219], v[180:183], v[48:63]\n"
    "s_waitcnt vmcnt(7)\n"
    "v_max3_u32 v208, v208, v191, v96\n"
    "v_max3_u32 v208, v208, v97, v98\n"
    "s_waitcnt vmcnt(6)\n"
    "v_max3_u32 v208, v208, v99, v100\n"
    "v_cvt_pk_bf16_f32 v19, v230, v231\n"
    "v_mfma_f32_32x32x16_bf16 v[64:79], a[220:223], v[180:183], v[64:79]\n"
    "v_max3_u32 v208, v208, v101, v102\n"
    "v_max_u32 v208, v208, v103\n"
    "v_cmp_eq_u32 vcc, -1, v208\n"
    "s_cbranch_vccnz Lhsr_67\n"
    "Lhsb_68:\n"
    "s_add_u32 s74, s58, 0\n"
    "buffer_store_dwordx4 v[16:19], v13, s[8:11], s74 offen sc1\n"
    "v_mfma_f32_32x32x16_bf16 v[48:63], a[80:83], v[184:187], v[48:63]\n"
    "s_barrier\n"
    "v_mfma_f32_32x32x16_bf16 v[64:79], a[84:87], v[184:187], v[64:79]\n"
    "v_mfma_f32_32x32x16_bf16 v[48:63], a[88:91], v[188:191], v[48:63]\n"
    "v_mfma_f32_32x32x16_bf16 v[64:79], a[92:95], v[188:191], v[64:79]\n"
    "v_mfma_f32_32x32x16_bf16 v[48:63], a[96:99], v[96:99], v[48:63]\n"
    "v_mfma_f32_32x32x16_bf16 v[64:79], a[100:103], v[96:99], v[64:79]\n"
    "s_waitcnt vmcnt(6)\n"
    "v_max3_u32 v208, v104, v105, v106\n"
    "s_waitcnt vmcnt(5)\n"
    "v_max3_u32 v208, v208, v107, v108\n"
    "v_max3_u32 v208, v208, v109, v110\n"
    "s_waitcnt vmcnt(4)\n"
    "v_max3_u32 v208, v208, v111, v112\n"
    "v_mfma_f32_32x32x16_bf16 v[48:63], a[104:107], v[100:103], v[48:63]\n"
    "v_max3_u32 v208, v208, v113, v114\n"
    "s_waitcnt vmcnt(3)\n"
    "v_max3_u32 v208, v208, v115, v116\n"
    "v_max3_u32 v208, v208, v117, v118\n"
    "s_waitcnt vmcnt(2)\n"
    "v_max3_u32 v208, v208, v119, v120\n"
    "v_mfma_f32_32x32x16_bf16 v[64:79], a[108:111], v[100:103], v[64:79]\n"
    "v_max3_u32 v208, v208, v121, v122\n"
    "s_waitcnt vmcnt(1)\n"
    "v_max3_u32 v208, v208, v123, v124\n"
    "v_max3_u32 v208, v208, v125, v126\n"
    "v_max_u32 v208, v208, v127\n"
    "v_cmp_eq_u32 vcc, -1, v208\n"
    "s_cbranch_vccnz Lhsr_69\n"
    "Lhsb_70:\n"
    "v_mfma_f32_32x32x16_bf16 v[48:63], a[112:115], v[104:107], v[48:63]\n"
    "v_mfma_f32_32x32x16_bf16 v[48:63], a[120:123], v[108:111], v[48:63]\n"
    "v_mfma_f32_32x32x16_bf16 v[48:63], a[128:131], v[112:115], v[48:63]\n"
    "v_mfma_f32_32x32x16_bf16 v[48:63], a[136:139], v[116:119], v[48:63]\n"
    "v_mfma_f32_32x32x16_bf16 v[48:63], a[144:147], v[120:123], v[48:63]\n"
    "v_mfma_f32_32x32x16_bf16 v[48:63], a[152:155], v[124:127], v[48:63]\n"
    "v_mfma_f32_32x32x16_bf16 v[64:79], a[116:119], v[104:107], v[64:79]\n"
    "v_mfma_f32_32x32x16_bf16 v[64:79], a[124:127], v[108:111], v[64:79]\n"
    "s_nop 9\n"
    "ds_write_b128 v240, v[48:51] offset:0\n"
    "v_mfma_f32_32x32x16_bf16 v[64:79], a[132:135], v[112:115], v[64:79]\n"
    "ds_write_b128 v240, v[52:55] offset:1088\n"
    "v_mfma_f32_32x32x16_bf16 v[64:79], a[140:143], v[116:119], v[64:79]\n"
    "ds_write_b128 v240, v[56:59] offset:2176\n"
    "v_mfma_f32_32x32x16_bf16 v[64:79], a[148:151], v[120:123], v[64:79]\n"
    "v_mfma_f32_32x32x16_bf16 v[64:79], a[156:159], v[124:127], v[64:79]\n"
    "ds_write_b128 v240, v[60:63] offset:3264\n"
    "s_nop 10\n"
    "ds_write_b128 v240, v[64:67] offset:4352\n"
    "ds_write_b128 v240, v[68:71] offset:5440\n"
    "ds_write_b128 v240, v[72:75] offset:6528\n"
    "ds_write_b128 v240, v[76:79] offset:7616\n"
    "s_waitcnt lgkmcnt(0)\n"
    "s_barrier\n"
    "ds_read_b128 v[16:19], v241 offset:0\n"
    "ds_read_b128 v[20:23], v241 offset:512\n"
    "ds_read_b128 v[24:27], v241 offset:9216\n"
    "ds_read_b128 v[28:31], v241 offset:9728\n"
    "ds_read_b128 v[32:35], v241 offset:18432\n"
    "ds_read_b128 v[36:39], v241 offset:18944\n"
    "ds_read_b128 v[40:43], v241 offset:27648\n"
    "ds_read_b128 v[44:47], v241 offset:28160\n"
    "s_waitcnt lgkmcnt(5)\n"
    "v_pk_add_f32 v[16:17], v[16:17], v[24:25]\n"
    "v_pk_add_f32 v[18:19], v[18:19], v[26:27]\n"
    "s_waitcnt lgkmcnt(4)\n"
    "v_pk_add_f32 v[20:21], v[20:21], v[28:29]\n"
    "v_pk_add_f32 v[22:23], v[22:23], v[30:31]\n"
    "s_waitcnt lgkmcnt(1)\n"
    "v_pk_add_f32 v[32:33], v[32:33], v[40:41]\n"
    "v_pk_add_f32 v[34:35], v[34:35], v[42:43]\n"
    "s_waitcnt lgkmcnt(0)\n"
    "v_pk_add_f32 v[36:37], v[36:37], v[44:45]\n"
    "v_pk_add_f32 v[38:39], v[38:39], v[46:47]\n"
    "v_pk_add_f32 v[16:17], v[16:17], v[32:33]\n"
    "v_pk_add_f32 v[18:19], v[18:19], v[34:35]\n"
    "v_pk_add_f32 v[20:21], v[20:21], v[36:37]\n"
    "v_pk_add_f32 v[22:23], v[22:23], v[38:39]\n"
    "v_pk_fma_f32 v[16:17], v[16:17], v[220:221], v[248:249] op_sel:[0,1,0] op_sel_hi:[1,1,1]\n"
    "v_pk_fma_f32 v[18:19], v[18:19], v[220:221], v[250:251] op_sel:[0,1,0] op_sel_hi:[1,1,1]\n"
    "v_pk_fma_f32 v[20:21], v[20:21], v[220:221], v[252:253] op_sel:[0,1,0] op_sel_hi:[1,1,1]\n"
    "v_pk_fma_f32 v[22:23], v[22:23], v[220:221], v[254:255] op_sel:[0,1,0] op_sel_hi:[1,1,1]\n"
    "v_pk_fma_f32 v[232:233], v[16:17], v[192:193], v[232:233]\n"
    "v_pk_fma_f32 v[234:235], v[18:19], v[194:195], v[234:235]\n"
    "v_pk_fma_f32 v[236:237], v[20:21], v[196:197], v[236:237]\n"
    "v_pk_fma_f32 v[238:239], v[22:23], v[198:199], v[238:239]\n"
    "v_cvt_pk_bf16_f32 v16, v232, v233\n"
    "v_cvt_pk_bf16_f32 v17, v234, v235\n"
    "v_cvt_pk_bf16_f32 v18, v236, v237\n"
    "v_cvt_pk_bf16_f32 v19, v238, v239\n"
    "s_add_u32 s74, s58, 32768\n"
    "buffer_store_dwordx4 v[16:19], v13, s[8:11], s74 offen sc1\n"
    "s_endpgm\n"
    "Lmainend:\n"
    "Lmodr_2:\n"
    "s_mov_b32 s50, m0\n"
    "Lmodr_2l:\n"
    "s_nop 15\n"
    "s_sub_u32 s64, s64, 1\n"
    "s_cbranch_scc1 Lmodb_3x\n"
    "s_sleep 1\n"
    "s_waitcnt lgkmcnt(0)\n"
    "buffer_load_dwordx4 v[40:43], v0, s[16:19], s66 offen sc1\n"
    "buffer_load_dwordx4 v[44:47], v0, s[16:19], s67 offen sc1\n"
    "buffer_load_dwordx4 v[32:35], v0, s[16:19], s68 offen sc1\n"
    "buffer_load_dwordx4 v[60:63], v0, s[16:19], s69 offen sc1\n"
    "s_waitcnt vmcnt(0) lgkmcnt(0)\n"
    "v_xor_b32 v41, s62, v41\n"
    "v_xor_b32 v43, s62, v43\n"
    "v_xor_b32 v45, s62, v45\n"
    "v_xor_b32 v47, s62, v47\n"
    "v_xor_b32 v33, s62, v33\n"
    "v_xor_b32 v35, s62, v35\n"
    "v_xor_b32 v61, s62, v61\n"
    "v_xor_b32 v63, s62, v63\n"
    "v_or3_b32 v50, v41, v43, v45\n"
    "v_or3_b32 v50, v50, v47, v33\n"
    "v_or3_b32 v50, v50, v35, v61\n"
    "v_or_b32 v50, v50, v63\n"
    "v_cmp_ne_u32 vcc, 0, v50\n"
    "s_cbranch_vccnz Lmodr_2l\n"
    "s_waitcnt vmcnt(0) lgkmcnt(0)\n"
    "s_mov_b32 m0, s50\n"
    "s_nop 15\n"
    "s_branch Lmodb_3\n"
    "Lmodb_3x:\n"
    "s_mov_b32 s64, 0\n"
    "s_waitcnt vmcnt(0) lgkmcnt(0)\n"
    "s_mov_b32 m0, s50\n"
    "s_nop 4\n"
    "s_branch Lmodb_3\n"
    "Lkvfr_4:\n"
    "s_mov_b32 s50, m0\n"
    "Lkvfr_4l:\n"
    "s_nop 15\n"
    "s_sub_u32 s64, s64, 1\n"
    "s_cbranch_scc1 Lkvfb_5x\n"
    "s_sleep 1\n"
    "s_waitcnt lgkmcnt(0)\n"
    "buffer_load_dwordx2 v[204:205], v5, s[16:19], 0 offen sc1\n"
    "s_waitcnt vmcnt(0) lgkmcnt(0)\n"
    "v_cmp_ne_u64 vcc, s[62:63], v[204:205]\n"
    "s_cbranch_vccnz Lkvfr_4l\n"
    "s_waitcnt vmcnt(0) lgkmcnt(0)\n"
    "s_mov_b32 m0, s50\n"
    "s_nop 15\n"
    "s_branch Lkvfb_5\n"
    "Lkvfb_5x:\n"
    "s_mov_b32 s64, 0\n"
    "s_waitcnt vmcnt(0) lgkmcnt(0)\n"
    "s_mov_b32 m0, s50\n"
    "s_nop 4\n"
    "s_branch Lkvfb_5\n"
    "Lfx_7:\n"
    "s_nop 7\n"
    "v_mov_b32 v48, 0xf149f2ca\n"
    "v_mov_b32 v49, 0xf149f2ca\n"
    "v_mov_b32 v50, 0xf149f2ca\n"
    "v_mov_b32 v51, 0xf149f2ca\n"
    "v_mov_b32 v52, 0xf149f2ca\n"
    "v_mov_b32 v53, 0xf149f2ca\n"
    "v_mov_b32 v54, 0xf149f2ca\n"
    "v_mov_b32 v55, 0xf149f2ca\n"
    "v_mov_b32 v56, 0xf149f2ca\n"
    "v_mov_b32 v57, 0xf149f2ca\n"
    "v_mov_b32 v58, 0xf149f2ca\n"
    "v_mov_b32 v59, 0xf149f2ca\n"
    "v_mov_b32 v60, 0xf149f2ca\n"
    "v_mov_b32 v61, 0xf149f2ca\n"
    "v_mov_b32 v62, 0xf149f2ca\n"
    "v_mov_b32 v63, 0xf149f2ca\n"
    "s_nop 1\n"
    "s_branch Lfb_8\n"
    "Lfx_9:\n"
    "s_nop 7\n"
    "v_mov_b32 v48, 0xf149f2ca\n"
    "v_mov_b32 v49, 0xf149f2ca\n"
    "v_mov_b32 v50, 0xf149f2ca\n"
    "v_mov_b32 v51, 0xf149f2ca\n"
    "v_mov_b32 v52, 0xf149f2ca\n"
    "v_mov_b32 v53, 0xf149f2ca\n"
    "v_mov_b32 v54, 0xf149f2ca\n"
    "v_mov_b32 v55, 0xf149f2ca\n"
    "v_mov_b32 v56, 0xf149f2ca\n"
    "v_mov_b32 v57, 0xf149f2ca\n"
    "v_mov_b32 v58, 0xf149f2ca\n"
    "v_mov_b32 v59, 0xf149f2ca\n"
    "v_mov_b32 v60, 0xf149f2ca\n"
    "v_mov_b32 v61, 0xf149f2ca\n"
    "v_mov_b32 v62, 0xf149f2ca\n"
    "v_mov_b32 v63, 0xf149f2ca\n"
    "s_nop 1\n"
    "s_branch Lfb_10\n"
    "Lfx_11:\n"
    "s_nop 7\n"
    "v_mov_b32 v48, 0xf149f2ca\n"
    "v_mov_b32 v49, 0xf149f2ca\n"
    "v_mov_b32 v50, 0xf149f2ca\n"
    "v_mov_b32 v51, 0xf149f2ca\n"
    "v_mov_b32 v52, 0xf149f2ca\n"
    "v_mov_b32 v53, 0xf149f2ca\n"
    "v_mov_b32 v54, 0xf149f2ca\n"
    "v_mov_b32 v55, 0xf149f2ca\n"
    "v_mov_b32 v56, 0xf149f2ca\n"
    "v_mov_b32 v57, 0xf149f2ca\n"
    "v_mov_b32 v58, 0xf149f2ca\n"
    "v_mov_b32 v59, 0xf149f2ca\n"
    "v_mov_b32 v60, 0xf149f2ca\n"
    "v_mov_b32 v61, 0xf149f2ca\n"
    "v_mov_b32 v62, 0xf149f2ca\n"
    "v_mov_b32 v63, 0xf149f2ca\n"
    "s_nop 1\n"
    "s_branch Lfb_12\n"
    "Lfx_15:\n"
    "s_nop 7\n"
    "v_mov_b32 v48, 0xf149f2ca\n"
    "v_mov_b32 v49, 0xf149f2ca\n"
    "v_mov_b32 v50, 0xf149f2ca\n"
    "v_mov_b32 v51, 0xf149f2ca\n"
    "v_mov_b32 v52, 0xf149f2ca\n"
    "v_mov_b32 v53, 0xf149f2ca\n"
    "v_mov_b32 v54, 0xf149f2ca\n"
    "v_mov_b32 v55, 0xf149f2ca\n"
    "v_mov_b32 v56, 0xf149f2ca\n"
    "v_mov_b32 v57, 0xf149f2ca\n"
    "v_mov_b32 v58, 0xf149f2ca\n"
    "v_mov_b32 v59, 0xf149f2ca\n"
    "v_mov_b32 v60, 0xf149f2ca\n"
    "v_mov_b32 v61, 0xf149f2ca\n"
    "v_mov_b32 v62, 0xf149f2ca\n"
    "v_mov_b32 v63, 0xf149f2ca\n"
    "s_nop 1\n"
    "s_branch Lfb_16\n"
    "Lfx_17:\n"
    "s_nop 7\n"
    "v_mov_b32 v48, 0xf149f2ca\n"
    "v_mov_b32 v49, 0xf149f2ca\n"
    "v_mov_b32 v50, 0xf149f2ca\n"
    "v_mov_b32 v51, 0xf149f2ca\n"
    "v_mov_b32 v52, 0xf149f2ca\n"
    "v_mov_b32 v53, 0xf149f2ca\n"
    "v_mov_b32 v54, 0xf149f2ca\n"
    "v_mov_b32 v55, 0xf149f2ca\n"
    "v_mov_b32 v56, 0xf149f2ca\n"
    "v_mov_b32 v57, 0xf149f2ca\n"
    "v_mov_b32 v58, 0xf149f2ca\n"
    "v_mov_b32 v59, 0xf149f2ca\n"
    "v_mov_b32 v60, 0xf149f2ca\n"
    "v_mov_b32 v61, 0xf149f2ca\n"
    "v_mov_b32 v62, 0xf149f2ca\n"
    "v_mov_b32 v63, 0xf149f2ca\n"
    "s_nop 1\n"
    "s_branch Lfb_18\n"
    "Lfx_19:\n"
    "s_nop 7\n"
    "v_mov_b32 v48, 0xf149f2ca\n"
    "v_mov_b32 v49, 0xf149f2ca\n"
    "v_mov_b32 v50, 0xf149f2ca\n"
    "v_mov_b32 v51, 0xf149f2ca\n"
    "v_mov_b32 v52, 0xf149f2ca\n"
    "v_mov_b32 v53, 0xf149f2ca\n"
    "v_mov_b32 v54, 0xf149f2ca\n"
    "v_mov_b32 v55, 0xf149f2ca\n"
    "v_mov_b32 v56, 0xf149f2ca\n"
    "v_mov_b32 v57, 0xf149f2ca\n"
    "v_mov_b32 v58, 0xf149f2ca\n"
    "v_mov_b32 v59, 0xf149f2ca\n"
    "v_mov_b32 v60, 0xf149f2ca\n"
    "v_mov_b32 v61, 0xf149f2ca\n"
    "v_mov_b32 v62, 0xf149f2ca\n"
    "v_mov_b32 v63, 0xf149f2ca\n"
    "s_nop 1\n"
    "s_branch Lfb_20\n"
    "Lclrr_23:\n"
    "s_mov_b32 s50, m0\n"
    "Lclrr_23l:\n"
    "s_nop 15\n"
    "s_sub_u32 s64, s64, 1\n"
    "s_cbranch_scc1 Lclrb_24x\n"
    "s_sleep 1\n"
    "s_waitcnt lgkmcnt(0)\n"
    "buffer_load_dwordx2 v[160:161], v206, s[16:19], s69 offen sc1\n"
    "s_waitcnt vmcnt(0) lgkmcnt(0)\n"
    "v_cmp_ne_u64 vcc, s[62:63], v[160:161]\n"
    "s_cbranch_vccnz Lclrr_23l\n"
    "s_waitcnt vmcnt(0) lgkmcnt(0)\n"
    "s_mov_b32 m0, s50\n"
    "s_nop 15\n"
    "s_branch Lclrb_24\n"
    "Lclrb_24x:\n"
    "s_mov_b32 s64, 0\n"
    "s_waitcnt vmcnt(0) lgkmcnt(0)\n"
    "s_mov_b32 m0, s50\n"
    "s_nop 4\n"
    "s_branch Lclrb_24\n"
    "Lgatr_25:\n"
    "s_mov_b32 s50, m0\n"
    "Lgatr_25l:\n"
    "s_nop 15\n"
    "s_sub_u32 s64, s64, 1\n"
    "s_cbranch_scc1 Lgatb_26x\n"
    "s_sleep 1\n"
    "s_waitcnt lgkmcnt(0)\n"
    "buffer_load_dwordx4 v[164:167], v0, s[16:19], s45 offen sc1\n"
    "buffer_load_dwordx4 v[168:171], v0, s[16:19], s45 offen offset:1024 sc1\n"
    "s_waitcnt vmcnt(0) lgkmcnt(0)\n"
    "v_xor_b32 v165, s62, v165\n"
    "v_xor_b32 v167, s62, v167\n"
    "v_xor_b32 v169, s62, v169\n"
    "v_xor_b32 v171, s62, v171\n"
    "v_or3_b32 v172, v165, v167, v169\n"
    "v_or_b32 v172, v172, v171\n"
    "v_cmp_ne_u32 vcc, 0, v172\n"
    "s_cbranch_vccnz Lgatr_25l\n"
    "s_waitcnt vmcnt(0) lgkmcnt(0)\n"
    "s_mov_b32 m0, s50\n"
    "s_nop 15\n"
    "s_branch Lgatb_26\n"
    "Lgatb_26x:\n"
    "s_mov_b32 s64, 0\n"
    "s_waitcnt vmcnt(0) lgkmcnt(0)\n"
    "s_mov_b32 m0, s50\n"
    "s_nop 4\n"
    "s_branch Lgatb_26\n"
    "Lo0r_27:\n"
    "s_mov_b32 s50, m0\n"
    "Lo0r_27l:\n"
    "s_nop 15\n"
    "s_sub_u32 s64, s64, 1\n"
    "s_cbranch_scc1 Lo0b_28x\n"
    "s_sleep 1\n"
    "s_waitcnt lgkmcnt(0)\n"
    "s_add_u32 s51, s41, 0\n"
    "v_and_b32 v255, 7, v1\n"
    "v_cmp_eq_u32 vcc, 3, v255\n"
    "v_mov_b32 v255, 0x0\n"
    "s_nop 0\n"
    "v_cndmask_b32 v255, 0, v255, vcc\n"
    "v_lshl_add_u32 v255, v1, 7, v255\n"
    "buffer_load_dword v255, v255, s[16:19], s51 offen offset:124 sc1\n"
    "s_waitcnt vmcnt(0)\n"
    "v_cmp_eq_u32 vcc, -1, v255\n"
    "s_cbranch_vccnz Lo0r_27l\n"
    "s_add_u32 s51, s41, 0\n"
    "s_add_u32 m0, s42, 0x1c000\n"
    "s_nop 0\n"
    "buffer_load_dwordx4 v10, s[16:19], s51 offen offset:0 sc1 lds\n"
    "buffer_load_dwordx4 v10, s[16:19], s51 offen offset:1024 sc1 lds\n"
    "buffer_load_dwordx4 v10, s[16:19], s51 offen offset:2048 sc1 lds\n"
    "buffer_load_dwordx4 v10, s[16:19], s51 offen offset:3072 sc1 lds\n"
    "s_add_u32 s51, s41, 4096\n"
    "s_add_u32 m0, s42, 0x20000\n"
    "s_nop 0\n"
    "buffer_load_dwordx4 v10, s[16:19], s51 offen offset:0 sc1 lds\n"
    "buffer_load_dwordx4 v10, s[16:19], s51 offen offset:1024 sc1 lds\n"
    "buffer_load_dwordx4 v10, s[16:19], s51 offen offset:2048 sc1 lds\n"
    "buffer_load_dwordx4 v10, s[16:19], s51 offen offset:3072 sc1 lds\n"
    "s_waitcnt vmcnt(0)\n"
    "ds_read_b128 v[64:67], v248 offset:0\n"
    "ds_read_b128 v[68:71], v248 offset:256\n"
    "ds_read_b128 v[72:75], v248 offset:2048\n"
    "ds_read_b128 v[76:79], v248 offset:2304\n"
    "ds_read_b128 v[80:83], v248 offset:16384\n"
    "ds_read_b128 v[84:87], v248 offset:16640\n"
    "ds_read_b128 v[88:91], v248 offset:18432\n"
    "ds_read_b128 v[92:95], v248 offset:18688\n"
    "s_waitcnt vmcnt(0) lgkmcnt(0)\n"
    "v_max3_u32 v255, v64, v65, v66\n"
    "v_max3_u32 v255, v255, v67, v68\n"
    "v_max3_u32 v255, v255, v69, v70\n"
    "v_max3_u32 v255, v255, v71, v72\n"
    "v_max3_u32 v255, v255, v73, v74\n"
    "v_max3_u32 v255, v255, v75, v76\n"
    "v_max3_u32 v255, v255, v77, v78\n"
    "v_max3_u32 v255, v255, v79, v80\n"
    "v_max3_u32 v255, v255, v81, v82\n"
    "v_max3_u32 v255, v255, v83, v84\n"
    "v_max3_u32 v255, v255, v85, v86\n"
    "v_max3_u32 v255, v255, v87, v88\n"
    "v_max3_u32 v255, v255, v89, v90\n"
    "v_max3_u32 v255, v255, v91, v92\n"
    "v_max3_u32 v255, v255, v93, v94\n"
    "v_max_u32 v255, v255, v95\n"
    "v_cmp_eq_u32 vcc, -1, v255\n"
    "s_cbranch_vccnz Lo0r_27l\n"
    "s_waitcnt vmcnt(0) lgkmcnt(0)\n"
    "s_mov_b32 m0, s50\n"
    "s_nop 15\n"
    "s_branch Lo0b_28\n"
    "Lo0b_28x:\n"
    "s_mov_b32 s64, 0\n"
    "s_waitcnt vmcnt(0) lgkmcnt(0)\n"
    "s_mov_b32 m0, s50\n"
    "s_nop 4\n"
    "s_branch Lo0b_28\n"
    "Lo1r_29:\n"
    "s_mov_b32 s50, m0\n"
    "Lo1r_29l:\n"
    "s_nop 15\n"
    "s_sub_u32 s64, s64, 1\n"
    "s_cbranch_scc1 Lo1b_30x\n"
    "s_sleep 1\n"
    "s_waitcnt lgkmcnt(0)\n"
    "s_add_u32 s51, s41, 32768\n"
    "v_and_b32 v255, 7, v1\n"
    "v_cmp_eq_u32 vcc, 3, v255\n"
    "v_mov_b32 v255, 0x0\n"
    "s_nop 0\n"
    "v_cndmask_b32 v255, 0, v255, vcc\n"
    "v_lshl_add_u32 v255, v1, 7, v255\n"
    "buffer_load_dword v255, v255, s[16:19], s51 offen offset:124 sc1\n"
    "s_waitcnt vmcnt(0)\n"
    "v_cmp_eq_u32 vcc, -1, v255\n"
    "s_cbranch_vccnz Lo1r_29l\n"
    "s_add_u32 s51, s41, 32768\n"
    "s_add_u32 m0, s42, 0x1c000\n"
    "s_nop 0\n"
    "buffer_load_dwordx4 v10, s[16:19], s51 offen offset:0 sc1 lds\n"
    "buffer_load_dwordx4 v10, s[16:19], s51 offen offset:1024 sc1 lds\n"
    "buffer_load_dwordx4 v10, s[16:19], s51 offen offset:2048 sc1 lds\n"
    "buffer_load_dwordx4 v10, s[16:19], s51 offen offset:3072 sc1 lds\n"
    "s_add_u32 s51, s41, 36864\n"
    "s_add_u32 m0, s42, 0x20000\n"
    "s_nop 0\n"
    "buffer_load_dwordx4 v10, s[16:19], s51 offen offset:0 sc1 lds\n"
    "buffer_load_dwordx4 v10, s[16:19], s51 offen offset:1024 sc1 lds\n"
    "buffer_load_dwordx4 v10, s[16:19], s51 offen offset:2048 sc1 lds\n"
    "buffer_load_dwordx4 v10, s[16:19], s51 offen offset:3072 sc1 lds\n"
    "s_waitcnt vmcnt(0)\n"
    "ds_read_b128 v[64:67], v248 offset:0\n"
    "ds_read_b128 v[68:71], v248 offset:256\n"
    "ds_read_b128 v[72:75], v248 offset:2048\n"
    "ds_read_b128 v[76:79], v248 offset:2304\n"
    "ds_read_b128 v[80:83], v248 offset:16384\n"
    "ds_read_b128 v[84:87], v248 offset:16640\n"
    "ds_read_b128 v[88:91], v248 offset:18432\n"
    "ds_read_b128 v[92:95], v248 offset:18688\n"
    "s_waitcnt vmcnt(0) lgkmcnt(0)\n"
    "v_max3_u32 v255, v64, v65, v66\n"
    "v_max3_u32 v255, v255, v67, v68\n"
    "v_max3_u32 v255, v255, v69, v70\n"
    "v_max3_u32 v255, v255, v71, v72\n"
    "v_max3_u32 v255, v255, v73, v74\n"
    "v_max3_u32 v255, v255, v75, v76\n"
    "v_max3_u32 v255, v255, v77, v78\n"
    "v_max3_u32 v255, v255, v79, v80\n"
    "v_max3_u32 v255, v255, v81, v82\n"
    "v_max3_u32 v255, v255, v83, v84\n"
    "v_max3_u32 v255, v255, v85, v86\n"
    "v_max3_u32 v255, v255, v87, v88\n"
    "v_max3_u32 v255, v255, v89, v90\n"
    "v_max3_u32 v255, v255, v91, v92\n"
    "v_max3_u32 v255, v255, v93, v94\n"
    "v_max_u32 v255, v255, v95\n"
    "v_cmp_eq_u32 vcc, -1, v255\n"
    "s_cbranch_vccnz Lo1r_29l\n"
    "s_waitcnt vmcnt(0) lgkmcnt(0)\n"
    "s_mov_b32 m0, s50\n"
    "s_nop 15\n"
    "s_branch Lo1b_30\n"
    "Lo1b_30x:\n"
    "s_mov_b32 s64, 0\n"
    "s_waitcnt vmcnt(0) lgkmcnt(0)\n"
    "s_mov_b32 m0, s50\n"
    "s_nop 4\n"
    "s_branch Lo1b_30\n"
    "Lcl2r_32:\n"
    "s_mov_b32 s50, m0\n"
    "Lcl2r_32l:\n"
    "s_nop 15\n"
    "s_sub_u32 s64, s64, 1\n"
    "s_cbranch_scc1 Lcl2b_33x\n"
    "s_sleep 1\n"
    "s_waitcnt lgkmcnt(0)\n"
    "buffer_load_dwordx2 v[174:175], v176, s[16:19], s69 offen sc1\n"
    "s_waitcnt vmcnt(0) lgkmcnt(0)\n"
    "v_cmp_ne_u64 vcc, s[62:63], v[174:175]\n"
    "s_cbranch_vccnz Lcl2r_32l\n"
    "s_waitcnt vmcnt(0) lgkmcnt(0)\n"
    "s_mov_b32 m0, s50\n"
    "s_nop 15\n"
    "s_branch Lcl2b_33\n"
    "Lcl2b_33x:\n"
    "s_mov_b32 s64, 0\n"
    "s_waitcnt vmcnt(0) lgkmcnt(0)\n"
    "s_mov_b32 m0, s50\n"
    "s_nop 4\n"
    "s_branch Lcl2b_33\n"
    "Ls0r_37:\n"
    "s_mov_b32 s50, m0\n"
    "Ls0r_37l:\n"
    "s_nop 15\n"
    "s_sub_u32 s64, s64, 1\n"
    "s_cbranch_scc1 Ls0b_38x\n"
    "s_sleep 1\n"
    "s_waitcnt lgkmcnt(0)\n"
    "s_and_b32 s51, s88, 0xfffff1ff\n"
    "s_mov_b32 m0, 0x24000\n"
    "s_nop 0\n"
    "buffer_load_dwordx4 v244, s[16:19], s51 offen offset:0 sc1 lds\n"
    "s_add_u32 s51, s51, 512\n"
    "s_mov_b32 m0, 0x24400\n"
    "s_nop 0\n"
    "buffer_load_dwordx4 v244, s[16:19], s51 offen offset:0 sc1 lds\n"
    "s_add_u32 s51, s51, 512\n"
    "s_mov_b32 m0, 0x24800\n"
    "s_nop 0\n"
    "buffer_load_dwordx4 v244, s[16:19], s51 offen offset:0 sc1 lds\n"
    "s_add_u32 s51, s51, 512\n"
    "s_mov_b32 m0, 0x24c00\n"
    "s_nop 0\n"
    "buffer_load_dwordx4 v244, s[16:19], s51 offen offset:0 sc1 lds\n"
    "s_add_u32 s51, s51, 512\n"
    "s_waitcnt vmcnt(0)\n"
    "ds_read_b128 v[192:195], v246 offset:0\n"
    "ds_read_b128 v[196:199], v246 offset:1024\n"
    "s_waitcnt vmcnt(0) lgkmcnt(0)\n"
    "v_max3_u32 v209, v192, v193, v194\n"
    "v_max3_u32 v209, v209, v195, v196\n"
    "v_max3_u32 v209, v209, v197, v198\n"
    "v_max_u32 v209, v209, v199\n"
    "v_cmp_eq_u32 vcc, -1, v209\n"
    "s_cbranch_vccnz Ls0r_37l\n"
    "s_waitcnt vmcnt(0) lgkmcnt(0)\n"
    "s_mov_b32 m0, s50\n"
    "s_nop 15\n"
    "s_branch Ls0b_38\n"
    "Ls0b_38x:\n"
    "s_mov_b32 s64, 0\n"
    "s_waitcnt vmcnt(0) lgkmcnt(0)\n"
    "s_mov_b32 m0, s50\n"
    "s_nop 4\n"
    "s_branch Ls0b_38\n"
    "Ly0r_39:\n"
    "s_mov_b32 s50, m0\n"
    "Ly0r_39l:\n"
    "s_nop 15\n"
    "s_sub_u32 s64, s64, 1\n"
    "s_cbranch_scc1 Ly0b_40x\n"
    "s_sleep 1\n"
    "s_waitcnt lgkmcnt(0)\n"
    "s_xor_b32 s51, s87, 0x0\n"
    "v_and_b32 v208, 7, v1\n"
    "v_cmp_eq_u32 vcc, 3, v208\n"
    "v_mov_b32 v208, 0x0\n"
    "s_nop 0\n"
    "v_cndmask_b32 v208, 0, v208, vcc\n"
    "v_lshl_add_u32 v208, v1, 7, v208\n"
    "buffer_load_dword v208, v208, s[16:19], s51 offen offset:124 sc1\n"
    "s_waitcnt vmcnt(0)\n"
    "v_cmp_eq_u32 vcc, -1, v208\n"
    "s_cbranch_vccnz Ly0r_39l\n"
    "s_xor_b32 s51, s87, 0x0\n"
    "s_add_u32 m0, s42, 0xc000\n"
    "s_nop 0\n"
    "buffer_load_dwordx4 v10, s[16:19], s51 offen offset:0 sc1 lds\n"
    "buffer_load_dwordx4 v10, s[16:19], s51 offen offset:1024 sc1 lds\n"
    "buffer_load_dwordx4 v10, s[16:19], s51 offen offset:2048 sc1 lds\n"
    "buffer_load_dwordx4 v10, s[16:19], s51 offen offset:3072 sc1 lds\n"
    "s_xor_b32 s51, s87, 0x1000\n"
    "s_add_u32 m0, s42, 0x10000\n"
    "s_nop 0\n"
    "buffer_load_dwordx4 v10, s[16:19], s51 offen offset:0 sc1 lds\n"
    "buffer_load_dwordx4 v10, s[16:19], s51 offen offset:1024 sc1 lds\n"
    "buffer_load_dwordx4 v10, s[16:19], s51 offen offset:2048 sc1 lds\n"
    "buffer_load_dwordx4 v10, s[16:19], s51 offen offset:3072 sc1 lds\n"
    "s_waitcnt vmcnt(0)\n"
    "ds_read_b128 v[48:51], v240 offset:0\n"
    "ds_read_b128 v[52:55], v240 offset:1024\n"
    "ds_read_b128 v[56:59], v240 offset:2048\n"
    "ds_read_b128 v[60:63], v240 offset:3072\n"
    "ds_read_b128 v[64:67], v240 offset:16384\n"
    "ds_read_b128 v[68:71], v240 offset:17408\n"
    "ds_read_b128 v[72:75], v240 offset:18432\n"
    "ds_read_b128 v[76:79], v240 offset:19456\n"
    "s_waitcnt vmcnt(0) lgkmcnt(0)\n"
    "v_max3_u32 v208, v48, v49, v50\n"
    "v_max3_u32 v208, v208, v51, v52\n"
    "v_max3_u32 v208, v208, v53, v54\n"
    "v_max3_u32 v208, v208, v55, v56\n"
    "v_max3_u32 v208, v208, v57, v58\n"
    "v_max3_u32 v208, v208, v59, v60\n"
    "v_max3_u32 v208, v208, v61, v62\n"
    "v_max3_u32 v208, v208, v63, v64\n"
    "v_max3_u32 v208, v208, v65, v66\n"
    "v_max3_u32 v208, v208, v67, v68\n"
    "v_max3_u32 v208, v208, v69, v70\n"
    "v_max3_u32 v208, v208, v71, v72\n"
    "v_max3_u32 v208, v208, v73, v74\n"
    "v_max3_u32 v208, v208, v75, v76\n"
    "v_max3_u32 v208, v208, v77, v78\n"
    "v_max_u32 v208, v208, v79\n"
    "v_cmp_eq_u32 vcc, -1, v208\n"
    "s_cbranch_vccnz Ly0r_39l\n"
    "v_mfma_f32_32x32x16_bf16 v[16:31], a[64:67], v[48:51], 0\n"
    "v_mfma_f32_32x32x16_bf16 v[16:31], a[68:71], v[52:55], v[16:31]\n"
    "v_mfma_f32_32x32x16_bf16 v[16:31], a[72:75], v[56:59], v[16:31]\n"
    "v_mfma_f32_32x32x16_bf16 v[16:31], a[76:79], v[60:63], v[16:31]\n"
    "s_waitcnt vmcnt(0) lgkmcnt(0)\n"
    "s_mov_b32 m0, s50\n"
    "s_nop 15\n"
    "s_branch Ly0b_40\n"
    "Ly0b_40x:\n"
    "s_mov_b32 s64, 0\n"
    "s_waitcnt vmcnt(0) lgkmcnt(0)\n"
    "s_mov_b32 m0, s50\n"
    "s_nop 4\n"
    "s_branch Ly0b_40\n"
    "Ly1r_41:\n"
    "s_mov_b32 s50, m0\n"
    "Ly1r_41l:\n"
    "s_nop 15\n"
    "s_sub_u32 s64, s64, 1\n"
    "s_cbranch_scc1 Ly1b_42x\n"
    "s_sleep 1\n"
    "s_waitcnt lgkmcnt(0)\n"
    "s_xor_b32 s51, s87, 0x10000\n"
    "v_and_b32 v208, 7, v1\n"
    "v_cmp_eq_u32 vcc, 3, v208\n"
    "v_mov_b32 v208, 0x0\n"
    "s_nop 0\n"
    "v_cndmask_b32 v208, 0, v208, vcc\n"
    "v_lshl_add_u32 v208, v1, 7, v208\n"
    "buffer_load_dword v208, v208, s[16:19], s51 offen offset:124 sc1\n"
    "s_waitcnt vmcnt(0)\n"
    "v_cmp_eq_u32 vcc, -1, v208\n"
    "s_cbranch_vccnz Ly1r_41l\n"
    "s_xor_b32 s51, s87, 0x10000\n"
    "s_add_u32 m0, s42, 0x14000\n"
    "s_nop 0\n"
    "buffer_load_dwordx4 v10, s[16:19], s51 offen offset:0 sc1 lds\n"
    "buffer_load_dwordx4 v10, s[16:19], s51 offen offset:1024 sc1 lds\n"
    "buffer_load_dwordx4 v10, s[16:19], s51 offen offset:2048 sc1 lds\n"
    "buffer_load_dwordx4 v10, s[16:19], s51 offen offset:3072 sc1 lds\n"
    "s_xor_b32 s51, s87, 0x11000\n"
    "s_add_u32 m0, s42, 0x18000\n"
    "s_nop 0\n"
    "buffer_load_dwordx4 v10, s[16:19], s51 offen offset:0 sc1 lds\n"
    "buffer_load_dwordx4 v10, s[16:19], s51 offen offset:1024 sc1 lds\n"
    "buffer_load_dwordx4 v10, s[16:19], s51 offen offset:2048 sc1 lds\n"
    "buffer_load_dwordx4 v10, s[16:19], s51 offen offset:3072 sc1 lds\n"
    "s_waitcnt vmcnt(0)\n"
    "ds_read_b128 v[80:83], v240 offset:32768\n"
    "ds_read_b128 v[84:87], v240 offset:33792\n"
    "ds_read_b128 v[88:91], v240 offset:34816\n"
    "ds_read_b128 v[92:95], v240 offset:35840\n"
    "ds_read_b128 v[48:51], v240 offset:49152\n"
    "ds_read_b128 v[52:55], v240 offset:50176\n"
    "ds_read_b128 v[56:59], v240 offset:51200\n"
    "ds_read_b128 v[60:63], v240 offset:52224\n"
    "s_waitcnt vmcnt(0) lgkmcnt(0)\n"
    "v_max3_u32 v208, v80, v81, v82\n"
    "v_max3_u32 v208, v208, v83, v84\n"
    "v_max3_u32 v208, v208, v85, v86\n"
    "v_max3_u32 v208, v208, v87, v88\n"
    "v_max3_u32 v208, v208, v89, v90\n"
    "v_max3_u32 v208, v208, v91, v92\n"
    "v_max3_u32 v208, v208, v93, v94\n"
    "v_max3_u32 v208, v208, v95, v48\n"
    "v_max3_u32 v208, v208, v49, v50\n"
    "v_max3_u32 v208, v208, v51, v52\n"
    "v_max3_u32 v208, v208, v53, v54\n"
    "v_max3_u32 v208, v208, v55, v56\n"
    "v_max3_u32 v208, v208, v57, v58\n"
    "v_max3_u32 v208, v208, v59, v60\n"
    "v_max3_u32 v208, v208, v61, v62\n"
    "v_max_u32 v208, v208, v63\n"
    "v_cmp_eq_u32 vcc, -1, v208\n"
    "s_cbranch_vccnz Ly1r_41l\n"
    "s_waitcnt vmcnt(0) lgkmcnt(0)\n"
    "s_mov_b32 m0, s50\n"
    "s_nop 15\n"
    "s_branch Ly1b_42\n"
    "Ly1b_42x:\n"
    "s_mov_b32 s64, 0\n"
    "s_waitcnt vmcnt(0) lgkmcnt(0)\n"
    "s_mov_b32 m0, s50\n"
    "s_nop 4\n"
    "s_branch Ly1b_42\n"
    "Ls1r_43:\n"
    "s_mov_b32 s50, m0\n"
    "Ls1r_43l:\n"
    "s_nop 15\n"
    "s_sub_u32 s64, s64, 1\n"
    "s_cbranch_scc1 Ls1b_44x\n"
    "s_sleep 1\n"
    "s_waitcnt lgkmcnt(0)\n"
    "s_and_b32 s51, s88, 0xfffff1ff\n"
    "s_mov_b32 m0, 0x24000\n"
    "s_nop 0\n"
    "buffer_load_dwordx4 v244, s[16:19], s51 offen offset:0 sc1 lds\n"
    "s_add_u32 s51, s51, 512\n"
    "s_mov_b32 m0, 0x24400\n"
    "s_nop 0\n"
    "buffer_load_dwordx4 v244, s[16:19], s51 offen offset:0 sc1 lds\n"
    "s_add_u32 s51, s51, 512\n"
    "s_mov_b32 m0, 0x24800\n"
    "s_nop 0\n"
    "buffer_load_dwordx4 v244, s[16:19], s51 offen offset:0 sc1 lds\n"
    "s_add_u32 s51, s51, 512\n"
    "s_mov_b32 m0, 0x24c00\n"
    "s_nop 0\n"
    "buffer_load_dwordx4 v244, s[16:19], s51 offen offset:0 sc1 lds\n"
    "s_add_u32 s51, s51, 512\n"
    "s_waitcnt vmcnt(0)\n"
    "ds_read_b128 v[192:195], v246 offset:0\n"
    "ds_read_b128 v[196:199], v246 offset:1024\n"
    "s_waitcnt vmcnt(0) lgkmcnt(0)\n"
    "v_max3_u32 v209, v192, v193, v194\n"
    "v_max3_u32 v209, v209, v195, v196\n"
    "v_max3_u32 v209, v209, v197, v198\n"
    "v_max_u32 v209, v209, v199\n"
    "v_cmp_eq_u32 vcc, -1, v209\n"
    "s_cbranch_vccnz Ls1r_43l\n"
    "s_waitcnt vmcnt(0) lgkmcnt(0)\n"
    "s_mov_b32 m0, s50\n"
    "s_nop 15\n"
    "s_branch Ls1b_44\n"
    "Ls1b_44x:\n"
    "s_mov_b32 s64, 0\n"
    "s_waitcnt vmcnt(0) lgkmcnt(0)\n"
    "s_mov_b32 m0, s50\n"
    "s_nop 4\n"
    "s_branch Ls1b_44\n"
    "Ly2r_45:\n"
    "s_mov_b32 s50, m0\n"
    "Ly2r_45l:\n"
    "s_nop 15\n"
    "s_sub_u32 s64, s64, 1\n"
    "s_cbranch_scc1 Ly2b_46x\n"
    "s_sleep 1\n"
    "s_waitcnt lgkmcnt(0)\n"
    "s_xor_b32 s51, s87, 0x8000\n"
    "v_and_b32 v208, 7, v1\n"
    "v_cmp_eq_u32 vcc, 3, v208\n"
    "v_mov_b32 v208, 0x0\n"
    "s_nop 0\n"
    "v_cndmask_b32 v208, 0, v208, vcc\n"
    "v_lshl_add_u32 v208, v1, 7, v208\n"
    "buffer_load_dword v208, v208, s[16:19], s51 offen offset:124 sc1\n"
    "s_waitcnt vmcnt(0)\n"
    "v_cmp_eq_u32 vcc, -1, v208\n"
    "s_cbranch_vccnz Ly2r_45l\n"
    "s_xor_b32 s51, s87, 0x8000\n"
    "s_add_u32 m0, s42, 0x0\n"
    "s_nop 0\n"
    "buffer_load_dwordx4 v10, s[16:19], s51 offen offset:0 sc1 lds\n"
    "buffer_load_dwordx4 v10, s[16:19], s51 offen offset:1024 sc1 lds\n"
    "buffer_load_dwordx4 v10, s[16:19], s51 offen offset:2048 sc1 lds\n"
    "buffer_load_dwordx4 v10, s[16:19], s51 offen offset:3072 sc1 lds\n"
    "s_xor_b32 s51, s87, 0x9000\n"
    "s_add_u32 m0, s42, 0x4000\n"
    "s_nop 0\n"
    "buffer_load_dwordx4 v10, s[16:19], s51 offen offset:0 sc1 lds\n"
    "buffer_load_dwordx4 v10, s[16:19], s51 offen offset:1024 sc1 lds\n"
    "buffer_load_dwordx4 v10, s[16:19], s51 offen offset:2048 sc1 lds\n"
    "buffer_load_dwordx4 v10, s[16:19], s51 offen offset:3072 sc1 lds\n"
    "s_waitcnt vmcnt(0)\n"
    "ds_read_b128 v[64:67], v240 offset:0\n"
    "ds_read_b128 v[68:71], v240 offset:1024\n"
    "ds_read_b128 v[72:75], v240 offset:2048\n"
    "ds_read_b128 v[76:79], v240 offset:3072\n"
    "ds_read_b128 v[80:83], v240 offset:16384\n"
    "ds_read_b128 v[84:87], v240 offset:17408\n"
    "ds_read_b128 v[88:91], v240 offset:18432\n"
    "ds_read_b128 v[92:95], v240 offset:19456\n"
    "s_waitcnt vmcnt(0) lgkmcnt(0)\n"
    "v_max3_u32 v208, v64, v65, v66\n"
    "v_max3_u32 v208, v208, v67, v68\n"
    "v_max3_u32 v208, v208, v69, v70\n"
    "v_max3_u32 v208, v208, v71, v72\n"
    "v_max3_u32 v208, v208, v73, v74\n"
    "v_max3_u32 v208, v208, v75, v76\n"
    "v_max3_u32 v208, v208, v77, v78\n"
    "v_max3_u32 v208, v208, v79, v80\n"
    "v_max3_u32 v208, v208, v81, v82\n"
    "v_max3_u32 v208, v208, v83, v84\n"
    "v_max3_u32 v208, v208, v85, v86\n"
    "v_max3_u32 v208, v208, v87, v88\n"
    "v_max3_u32 v208, v208, v89, v90\n"
    "v_max3_u32 v208, v208, v91, v92\n"
    "v_max3_u32 v208, v208, v93, v94\n"
    "v_max_u32 v208, v208, v95\n"
    "v_cmp_eq_u32 vcc, -1, v208\n"
    "s_cbranch_vccnz Ly2r_45l\n"
    "s_waitcnt vmcnt(0) lgkmcnt(0)\n"
    "s_mov_b32 m0, s50\n"
    "s_nop 15\n"
    "s_branch Ly2b_46\n"
    "Ly2b_46x:\n"
    "s_mov_b32 s64, 0\n"
    "s_waitcnt vmcnt(0) lgkmcnt(0)\n"
    "s_mov_b32 m0, s50\n"
    "s_nop 4\n"
    "s_branch Ly2b_46\n"
    "Ls2r_47:\n"
    "s_mov_b32 s50, m0\n"
    "Ls2r_47l:\n"
    "s_nop 15\n"
    "s_sub_u32 s64, s64, 1\n"
    "s_cbranch_scc1 Ls2b_48x\n"
    "s_sleep 1\n"
    "s_waitcnt lgkmcnt(0)\n"
    "s_and_b32 s51, s88, 0xfffff1ff\n"
    "s_mov_b32 m0, 0x26800\n"
    "s_nop 0\n"
    "buffer_load_dwordx4 v244, s[16:19], s51 offen offset:2048 sc1 lds\n"
    "s_add_u32 s51, s51, 512\n"
    "s_mov_b32 m0, 0x26c00\n"
    "s_nop 0\n"
    "buffer_load_dwordx4 v244, s[16:19], s51 offen offset:2048 sc1 lds\n"
    "s_add_u32 s51, s51, 512\n"
    "s_mov_b32 m0, 0x27000\n"
    "s_nop 0\n"
    "buffer_load_dwordx4 v244, s[16:19], s51 offen offset:2048 sc1 lds\n"
    "s_add_u32 s51, s51, 512\n"
    "s_mov_b32 m0, 0x27400\n"
    "s_nop 0\n"
    "buffer_load_dwordx4 v244, s[16:19], s51 offen offset:2048 sc1 lds\n"
    "s_add_u32 s51, s51, 512\n"
    "s_waitcnt vmcnt(0)\n"
    "ds_read_b128 v[192:195], v246 offset:12288\n"
    "ds_read_b128 v[196:199], v246 offset:13312\n"
    "s_waitcnt vmcnt(0) lgkmcnt(0)\n"
    "v_max3_u32 v209, v192, v193, v194\n"
    "v_max3_u32 v209, v209, v195, v196\n"
    "v_max3_u32 v209, v209, v197, v198\n"
    "v_max_u32 v209, v209, v199\n"
    "v_cmp_eq_u32 vcc, -1, v209\n"
    "s_cbranch_vccnz Ls2r_47l\n"
    "s_waitcnt vmcnt(0) lgkmcnt(0)\n"
    "s_mov_b32 m0, s50\n"
    "s_nop 15\n"
    "s_branch Ls2b_48\n"
    "Ls2b_48x:\n"
    "s_mov_b32 s64, 0\n"
    "s_waitcnt vmcnt(0) lgkmcnt(0)\n"
    "s_mov_b32 m0, s50\n"
    "s_nop 4\n"
    "s_branch Ls2b_48\n"
    "Ly3r_49:\n"
    "s_mov_b32 s50, m0\n"
    "Ly3r_49l:\n"
    "s_nop 15\n"
    "s_sub_u32 s64, s64, 1\n"
    "s_cbranch_scc1 Ly3b_50x\n"
    "s_sleep 1\n"
    "s_waitcnt lgkmcnt(0)\n"
    "s_xor_b32 s51, s87, 0x18000\n"
    "v_and_b32 v208, 7, v1\n"
    "v_cmp_eq_u32 vcc, 3, v208\n"
    "v_mov_b32 v208, 0x0\n"
    "s_nop 0\n"
    "v_cndmask_b32 v208, 0, v208, vcc\n"
    "v_lshl_add_u32 v208, v1, 7, v208\n"
    "buffer_load_dword v208, v208, s[16:19], s51 offen offset:124 sc1\n"
    "s_waitcnt vmcnt(0)\n"
    "v_cmp_eq_u32 vcc, -1, v208\n"
    "s_cbranch_vccnz Ly3r_49l\n"
    "s_xor_b32 s51, s87, 0x18000\n"
    "s_add_u32 m0, s42, 0x1c000\n"
    "s_nop 0\n"
    "buffer_load_dwordx4 v10, s[16:19], s51 offen offset:0 sc1 lds\n"
    "buffer_load_dwordx4 v10, s[16:19], s51 offen offset:1024 sc1 lds\n"
    "buffer_load_dwordx4 v10, s[16:19], s51 offen offset:2048 sc1 lds\n"
    "buffer_load_dwordx4 v10, s[16:19], s51 offen offset:3072 sc1 lds\n"
    "s_xor_b32 s51, s87, 0x19000\n"
    "s_add_u32 m0, s42, 0x20000\n"
    "s_nop 0\n"
    "buffer_load_dwordx4 v10, s[16:19], s51 offen offset:0 sc1 lds\n"
    "buffer_load_dwordx4 v10, s[16:19], s51 offen offset:1024 sc1 lds\n"
    "buffer_load_dwordx4 v10, s[16:19], s51 offen offset:2048 sc1 lds\n"
    "buffer_load_dwordx4 v10, s[16:19], s51 offen offset:3072 sc1 lds\n"
    "s_waitcnt vmcnt(0)\n"
    "ds_read_b128 v[48:51], v240 offset:0\n"
    "ds_read_b128 v[52:55], v240 offset:1024\n"
    "ds_read_b128 v[56:59], v240 offset:2048\n"
    "ds_read_b128 v[60:63], v240 offset:3072\n"
    "ds_read_b128 v[64:67], v240 offset:16384\n"
    "ds_read_b128 v[68:71], v240 offset:17408\n"
    "ds_read_b128 v[72:75], v240 offset:18432\n"
    "ds_read_b128 v[76:79], v240 offset:19456\n"
    "s_waitcnt vmcnt(0) lgkmcnt(0)\n"
    "v_max3_u32 v208, v48, v49, v50\n"
    "v_max3_u32 v208, v208, v51, v52\n"
    "v_max3_u32 v208, v208, v53, v54\n"
    "v_max3_u32 v208, v208, v55, v56\n"
    "v_max3_u32 v208, v208, v57, v58\n"
    "v_max3_u32 v208, v208, v59, v60\n"
    "v_max3_u32 v208, v208, v61, v62\n"
    "v_max3_u32 v208, v208, v63, v64\n"
    "v_max3_u32 v208, v208, v65, v66\n"
    "v_max3_u32 v208, v208, v67, v68\n"
    "v_max3_u32 v208, v208, v69, v70\n"
    "v_max3_u32 v208, v208, v71, v72\n"
    "v_max3_u32 v208, v208, v73, v74\n"
    "v_max3_u32 v208, v208, v75, v76\n"
    "v_max3_u32 v208, v208, v77, v78\n"
    "v_max_u32 v208, v208, v79\n"
    "v_cmp_eq_u32 vcc, -1, v208\n"
    "s_cbranch_vccnz Ly3r_49l\n"
    "s_waitcnt vmcnt(0) lgkmcnt(0)\n"
    "s_mov_b32 m0, s50\n"
    "s_nop 15\n"
    "s_branch Ly3b_50\n"
    "Ly3b_50x:\n"
    "s_mov_b32 s64, 0\n"
    "s_waitcnt vmcnt(0) lgkmcnt(0)\n"
    "s_mov_b32 m0, s50\n"
    "s_nop 4\n"
    "s_branch Ly3b_50\n"
    "Ls3r_51:\n"
    "s_mov_b32 s50, m0\n"
    "Ls3r_51l:\n"
    "s_nop 15\n"
    "s_sub_u32 s64, s64, 1\n"
    "s_cbranch_scc1 Ls3b_52x\n"
    "s_sleep 1\n"
    "s_waitcnt lgkmcnt(0)\n"
    "s_and_b32 s51, s88, 0xfffff1ff\n"
    "s_mov_b32 m0, 0x26800\n"
    "s_nop 0\n"
    "buffer_load_dwordx4 v244, s[16:19], s51 offen offset:2048 sc1 lds\n"
    "s_add_u32 s51, s51, 512\n"
    "s_mov_b32 m0, 0x26c00\n"
    "s_nop 0\n"
    "buffer_load_dwordx4 v244, s[16:19], s51 offen offset:2048 sc1 lds\n"
    "s_add_u32 s51, s51, 512\n"
    "s_mov_b32 m0, 0x27000\n"
    "s_nop 0\n"
    "buffer_load_dwordx4 v244, s[16:19], s51 offen offset:2048 sc1 lds\n"
    "s_add_u32 s51, s51, 512\n"
    "s_mov_b32 m0, 0x27400\n"
    "s_nop 0\n"
    "buffer_load_dwordx4 v244, s[16:19], s51 offen offset:2048 sc1 lds\n"
    "s_add_u32 s51, s51, 512\n"
    "s_waitcnt vmcnt(0)\n"
    "ds_read_b128 v[192:195], v246 offset:12288\n"
    "ds_read_b128 v[196:199], v246 offset:13312\n"
    "s_waitcnt vmcnt(0) lgkmcnt(0)\n"
    "v_max3_u32 v209, v192, v193, v194\n"
    "v_max3_u32 v209, v209, v195, v196\n"
    "v_max3_u32 v209, v209, v197, v198\n"
    "v_max_u32 v209, v209, v199\n"
    "v_cmp_eq_u32 vcc, -1, v209\n"
    "s_cbranch_vccnz Ls3r_51l\n"
    "s_waitcnt vmcnt(0) lgkmcnt(0)\n"
    "s_mov_b32 m0, s50\n"
    "s_nop 15\n"
    "s_branch Ls3b_52\n"
    "Ls3b_52x:\n"
    "s_mov_b32 s64, 0\n"
    "s_waitcnt vmcnt(0) lgkmcnt(0)\n"
    "s_mov_b32 m0, s50\n"
    "s_nop 4\n"
    "s_branch Ls3b_52\n"
    "Lhp00r_53:\n"
    "s_mov_b32 s50, m0\n"
    "Lhp00r_53l:\n"
    "s_nop 15\n"
    "s_sub_u32 s64, s64, 1\n"
    "s_cbranch_scc1 Lhp00b_54x\n"
    "s_sleep 1\n"
    "s_waitcnt lgkmcnt(0)\n"
    "buffer_load_dwordx2 v[210:211], v212, s[16:19], s92 offen sc1\n"
    "s_waitcnt vmcnt(0) lgkmcnt(0)\n"
    "v_cmp_ne_u64 vcc, s[62:63], v[210:211]\n"
    "s_cbranch_vccnz Lhp00r_53l\n"
    "s_waitcnt vmcnt(0) lgkmcnt(0)\n"
    "s_mov_b32 m0, s50\n"
    "s_nop 15\n"
    "s_branch Lhp00b_54\n"
    "Lhp00b_54x:\n"
    "s_mov_b32 s64, 0\n"
    "s_waitcnt vmcnt(0) lgkmcnt(0)\n"
    "s_mov_b32 m0, s50\n"
    "s_nop 4\n"
    "s_branch Lhp00b_54\n"
    "Lepr_57:\n"
    "s_mov_b32 s50, m0\n"
    "Lepr_57l:\n"
    "s_nop 15\n"
    "s_sub_u32 s64, s64, 1\n"
    "s_cbranch_scc1 Lepb_58x\n"
    "s_sleep 1\n"
    "s_waitcnt lgkmcnt(0)\n"
    "s_mov_b32 s51, 0x8000\n"
    "buffer_load_dwordx4 v[48:51], v0, s[16:19], s51 offen offset:0 sc1\n"
    "buffer_load_dwordx4 v[52:55], v0, s[16:19], s51 offen offset:1024 sc1\n"
    "buffer_load_dwordx4 v[56:59], v0, s[16:19], s51 offen offset:2048 sc1\n"
    "buffer_load_dwordx4 v[60:63], v0, s[16:19], s51 offen offset:3072 sc1\n"
    "s_mov_b32 s51, 0x9000\n"
    "buffer_load_dwordx4 v[64:67], v0, s[16:19], s51 offen offset:0 sc1\n"
    "buffer_load_dwordx4 v[68:71], v0, s[16:19], s51 offen offset:1024 sc1\n"
    "buffer_load_dwordx4 v[72:75], v0, s[16:19], s51 offen offset:2048 sc1\n"
    "buffer_load_dwordx4 v[76:79], v0, s[16:19], s51 offen offset:3072 sc1\n"
    "s_waitcnt vmcnt(0) lgkmcnt(0)\n"
    "v_xor_b32 v48, s62, v48\n"
    "v_xor_b32 v49, s63, v49\n"
    "v_xor_b32 v50, s62, v50\n"
    "v_xor_b32 v51, s63, v51\n"
    "v_xor_b32 v52, s62, v52\n"
    "v_xor_b32 v53, s63, v53\n"
    "v_xor_b32 v54, s62, v54\n"
    "v_xor_b32 v55, s63, v55\n"
    "v_xor_b32 v56, s62, v56\n"
    "v_xor_b32 v57, s63, v57\n"
    "v_xor_b32 v58, s62, v58\n"
    "v_xor_b32 v59, s63, v59\n"
    "v_xor_b32 v60, s62, v60\n"
    "v_xor_b32 v61, s63, v61\n"
    "v_xor_b32 v62, s62, v62\n"
    "v_xor_b32 v63, s63, v63\n"
    "v_xor_b32 v64, s62, v64\n"
    "v_xor_b32 v65, s63, v65\n"
    "v_xor_b32 v66, s62, v66\n"
    "v_xor_b32 v67, s63, v67\n"
    "v_xor_b32 v68, s62, v68\n"
    "v_xor_b32 v69, s63, v69\n"
    "v_xor_b32 v70, s62, v70\n"
    "v_xor_b32 v71, s63, v71\n"
    "v_xor_b32 v72, s62, v72\n"
    "v_xor_b32 v73, s63, v73\n"
    "v_xor_b32 v74, s62, v74\n"
    "v_xor_b32 v75, s63, v75\n"
    "v_xor_b32 v76, s62, v76\n"
    "v_xor_b32 v77, s63, v77\n"
    "v_xor_b32 v78, s62, v78\n"
    "v_xor_b32 v79, s63, v79\n"
    "v_or3_b32 v208, v48, v49, v50\n"
    "v_or3_b32 v208, v208, v51, v52\n"
    "v_or3_b32 v208, v208, v53, v54\n"
    "v_or3_b32 v208, v208, v55, v56\n"
    "v_or3_b32 v208, v208, v57, v58\n"
    "v_or3_b32 v208, v208, v59, v60\n"
    "v_or3_b32 v208, v208, v61, v62\n"
    "v_or3_b32 v208, v208, v63, v64\n"
    "v_or3_b32 v208, v208, v65, v66\n"
    "v_or3_b32 v208, v208, v67, v68\n"
    "v_or3_b32 v208, v208, v69, v70\n"
    "v_or3_b32 v208, v208, v71, v72\n"
    "v_or3_b32 v208, v208, v73, v74\n"
    "v_or3_b32 v208, v208, v75, v76\n"
    "v_or3_b32 v208, v208, v77, v78\n"
    "v_or_b32 v208, v208, v79\n"
    "v_cmp_ne_u32 vcc, 0, v208\n"
    "s_cbranch_vccnz Lepr_57l\n"
    "s_waitcnt vmcnt(0) lgkmcnt(0)\n"
    "s_mov_b32 m0, s50\n"
    "s_nop 15\n"
    "s_branch Lepb_58\n"
    "Lepb_58x:\n"
    "s_mov_b32 s64, 0\n"
    "s_waitcnt vmcnt(0) lgkmcnt(0)\n"
    "s_mov_b32 m0, s50\n"
    "s_nop 4\n"
    "s_branch Lepb_58\n"
    "Lhp00r_59:\n"
    "s_mov_b32 s50, m0\n"
    "Lhp00r_59l:\n"
    "s_nop 15\n"
    "s_sub_u32 s64, s64, 1\n"
    "s_cbranch_scc1 Lhp00b_60x\n"
    "s_sleep 1\n"
    "s_waitcnt lgkmcnt(0)\n"
    "buffer_load_dwordx2 v[210:211], v212, s[16:19], s92 offen sc1\n"
    "s_waitcnt vmcnt(0) lgkmcnt(0)\n"
    "v_cmp_ne_u64 vcc, s[62:63], v[210:211]\n"
    "s_cbranch_vccnz Lhp00r_59l\n"
    "s_waitcnt vmcnt(0) lgkmcnt(0)\n"
    "s_mov_b32 m0, s50\n"
    "s_nop 15\n"
    "s_branch Lhp00b_60\n"
    "Lhp00b_60x:\n"
    "s_mov_b32 s64, 0\n"
    "s_waitcnt vmcnt(0) lgkmcnt(0)\n"
    "s_mov_b32 m0, s50\n"
    "s_nop 4\n"
    "s_branch Lhp00b_60\n"
    "Lhp01r_61:\n"
    "s_mov_b32 s50, m0\n"
    "Lhp01r_61l:\n"
    "s_nop 15\n"
    "s_sub_u32 s64, s64, 1\n"
    "s_cbranch_scc1 Lhp01b_62x\n"
    "s_sleep 1\n"
    "s_waitcnt lgkmcnt(0)\n"
    "buffer_load_dwordx2 v[210:211], v213, s[16:19], s92 offen sc1\n"
    "s_waitcnt vmcnt(0) lgkmcnt(0)\n"
    "v_cmp_ne_u64 vcc, s[62:63], v[210:211]\n"
    "s_cbranch_vccnz Lhp01r_61l\n"
    "s_waitcnt vmcnt(0) lgkmcnt(0)\n"
    "s_mov_b32 m0, s50\n"
    "s_nop 15\n"
    "s_branch Lhp01b_62\n"
    "Lhp01b_62x:\n"
    "s_mov_b32 s64, 0\n"
    "s_waitcnt vmcnt(0) lgkmcnt(0)\n"
    "s_mov_b32 m0, s50\n"
    "s_nop 4\n"
    "s_branch Lhp01b_62\n"
    "Lhp10r_63:\n"
    "s_mov_b32 s50, m0\n"
    "Lhp10r_63l:\n"
    "s_nop 15\n"
    "s_sub_u32 s64, s64, 1\n"
    "s_cbranch_scc1 Lhp10b_64x\n"
    "s_sleep 1\n"
    "s_waitcnt lgkmcnt(0)\n"
    "buffer_load_dwordx2 v[210:211], v212, s[16:19], s66 offen sc1\n"
    "s_waitcnt vmcnt(0) lgkmcnt(0)\n"
    "v_cmp_ne_u64 vcc, s[62:63], v[210:211]\n"
    "s_cbranch_vccnz Lhp10r_63l\n"
    "s_waitcnt vmcnt(0) lgkmcnt(0)\n"
    "s_mov_b32 m0, s50\n"
    "s_nop 15\n"
    "s_branch Lhp10b_64\n"
    "Lhp10b_64x:\n"
    "s_mov_b32 s64, 0\n"
    "s_waitcnt vmcnt(0) lgkmcnt(0)\n"
    "s_mov_b32 m0, s50\n"
    "s_nop 4\n"
    "s_branch Lhp10b_64\n"
    "Lhsr_65:\n"
    "s_mov_b32 s50, m0\n"
    "Lhsr_65l:\n"
    "s_nop 15\n"
    "s_sub_u32 s64, s64, 1\n"
    "s_cbranch_scc1 Lhsb_66x\n"
    "s_sleep 1\n"
    "s_waitcnt lgkmcnt(0)\n"
    "s_add_u32 s51, s76, s94\n"
    "buffer_load_dwordx4 v[168:171], v10, s[16:19], s51 offen offset:0 sc1\n"
    "buffer_load_dwordx4 v[172:175], v10, s[16:19], s51 offen offset:1024 sc1\n"
    "s_add_u32 s51, s77, s94\n"
    "buffer_load_dwordx4 v[176:179], v10, s[16:19], s51 offen offset:0 sc1\n"
    "buffer_load_dwordx4 v[180:183], v10, s[16:19], s51 offen offset:1024 sc1\n"
    "s_waitcnt vmcnt(0) lgkmcnt(0)\n"
    "v_max3_u32 v208, v168, v169, v170\n"
    "v_max3_u32 v208, v208, v171, v172\n"
    "v_max3_u32 v208, v208, v173, v174\n"
    "v_max3_u32 v208, v208, v175, v176\n"
    "v_max3_u32 v208, v208, v177, v178\n"
    "v_max3_u32 v208, v208, v179, v180\n"
    "v_max3_u32 v208, v208, v181, v182\n"
    "v_max_u32 v208, v208, v183\n"
    "v_cmp_eq_u32 vcc, -1, v208\n"
    "s_cbranch_vccnz Lhsr_65l\n"
    "s_waitcnt vmcnt(0) lgkmcnt(0)\n"
    "s_mov_b32 m0, s50\n"
    "s_nop 15\n"
    "s_branch Lhsb_66\n"
    "Lhsb_66x:\n"
    "s_mov_b32 s64, 0\n"
    "s_waitcnt vmcnt(0) lgkmcnt(0)\n"
    "s_mov_b32 m0, s50\n"
    "s_nop 4\n"
    "s_branch Lhsb_66\n"
    "Lhsr_67:\n"
    "s_mov_b32 s50, m0\n"
    "Lhsr_67l:\n"
    "s_nop 15\n"
    "s_sub_u32 s64, s64, 1\n"
    "s_cbranch_scc1 Lhsb_68x\n"
    "s_sleep 1\n"
    "s_waitcnt lgkmcnt(0)\n"
    "s_add_u32 s51, s78, s94\n"
    "buffer_load_dwordx4 v[184:187], v10, s[16:19], s51 offen offset:0 sc1\n"
    "buffer_load_dwordx4 v[188:191], v10, s[16:19], s51 offen offset:1024 sc1\n"
    "s_add_u32 s51, s79, s94\n"
    "buffer_load_dwordx4 v[96:99], v10, s[16:19], s51 offen offset:0 sc1\n"
    "buffer_load_dwordx4 v[100:103], v10, s[16:19], s51 offen offset:1024 sc1\n"
    "s_waitcnt vmcnt(0) lgkmcnt(0)\n"
    "v_max3_u32 v208, v184, v185, v186\n"
    "v_max3_u32 v208, v208, v187, v188\n"
    "v_max3_u32 v208, v208, v189, v190\n"
    "v_max3_u32 v208, v208, v191, v96\n"
    "v_max3_u32 v208, v208, v97, v98\n"
    "v_max3_u32 v208, v208, v99, v100\n"
    "v_max3_u32 v208, v208, v101, v102\n"
    "v_max_u32 v208, v208, v103\n"
    "v_cmp_eq_u32 vcc, -1, v208\n"
    "s_cbranch_vccnz Lhsr_67l\n"
    "s_waitcnt vmcnt(0) lgkmcnt(0)\n"
    "s_mov_b32 m0, s50\n"
    "s_nop 15\n"
    "s_branch Lhsb_68\n"
    "Lhsb_68x:\n"
    "s_mov_b32 s64, 0\n"
    "s_waitcnt vmcnt(0) lgkmcnt(0)\n"
    "s_mov_b32 m0, s50\n"
    "s_nop 4\n"
    "s_branch Lhsb_68\n"
    "Lhsr_69:\n"
    "s_mov_b32 s50, m0\n"
    "Lhsr_69l:\n"
    "s_nop 15\n"
    "s_sub_u32 s64, s64, 1\n"
    "s_cbranch_scc1 Lhsb_70x\n"
    "s_sleep 1\n"
    "s_waitcnt lgkmcnt(0)\n"
    "s_add_u32 s51, s80, s94\n"
    "buffer_load_dwordx4 v[104:107], v10, s[16:19], s51 offen offset:0 sc1\n"
    "buffer_load_dwordx4 v[108:111], v10, s[16:19], s51 offen offset:1024 sc1\n"
    "s_add_u32 s51, s81, s94\n"
    "buffer_load_dwordx4 v[112:115], v10, s[16:19], s51 offen offset:0 sc1\n"
    "buffer_load_dwordx4 v[116:119], v10, s[16:19], s51 offen offset:1024 sc1\n"
    "s_add_u32 s51, s82, s94\n"
    "buffer_load_dwordx4 v[120:123], v10, s[16:19], s51 offen offset:0 sc1\n"
    "buffer_load_dwordx4 v[124:127], v10, s[16:19], s51 offen offset:1024 sc1\n"
    "s_waitcnt vmcnt(0) lgkmcnt(0)\n"
    "v_max3_u32 v208, v104, v105, v106\n"
    "v_max3_u32 v208, v208, v107, v108\n"
    "v_max3_u32 v208, v208, v109, v110\n"
    "v_max3_u32 v208, v208, v111, v112\n"
    "v_max3_u32 v208, v208, v113, v114\n"
    "v_max3_u32 v208, v208, v115, v116\n"
    "v_max3_u32 v208, v208, v117, v118\n"
    "v_max3_u32 v208, v208, v119, v120\n"
    "v_max3_u32 v208, v208, v121, v122\n"
    "v_max3_u32 v208, v208, v123, v124\n"
    "v_max3_u32 v208, v208, v125, v126\n"
    "v_max_u32 v208, v208, v127\n"
    "v_cmp_eq_u32 vcc, -1, v208\n"
    "s_cbranch_vccnz Lhsr_69l\n"
    "s_waitcnt vmcnt(0) lgkmcnt(0)\n"
    "s_mov_b32 m0, s50\n"
    "s_nop 15\n"
    "s_branch Lhsb_70\n"
    "Lhsb_70x:\n"
    "s_mov_b32 s64, 0\n"
    "s_waitcnt vmcnt(0) lgkmcnt(0)\n"
    "s_mov_b32 m0, s50\n"
    "s_nop 4\n"
    "s_branch Lhsb_70\n"
    "Lallend:\n"
    "Lslow_13:\n"
    "s_nop 15\n"
    "v_mov_b32 v205, v202\n"
    "v_mfma_f32_32x32x16_bf16 v[16:31], v[64:67], v[176:179], 0\n"
    "s_nop 11\n"
    "v_max_f32 v204, v16, v17\n"
    "v_max3_f32 v204, v204, v18, v19\n"
    "v_max3_f32 v204, v204, v20, v21\n"
    "v_max3_f32 v204, v204, v22, v23\n"
    "v_max3_f32 v204, v204, v24, v25\n"
    "v_max3_f32 v204, v204, v26, v27\n"
    "v_max3_f32 v204, v204, v28, v29\n"
    "v_max3_f32 v204, v204, v30, v31\n"
    "s_cmp_gt_u32 s54, 0\n"
    "s_cselect_b32 s50, 0, 0xf149f2ca\n"
    "v_add_f32 v204, s50, v204\n"
    "v_max_f32 v205, v205, v204\n"
    "v_mfma_f32_32x32x16_bf16 v[16:31], v[68:71], v[176:179], 0\n"
    "s_nop 11\n"
    "v_max_f32 v204, v16, v17\n"
    "v_max3_f32 v204, v204, v18, v19\n"
    "v_max3_f32 v204, v204, v20, v21\n"
    "v_max3_f32 v204, v204, v22, v23\n"
    "v_max3_f32 v204, v204, v24, v25\n"
    "v_max3_f32 v204, v204, v26, v27\n"
    "v_max3_f32 v204, v204, v28, v29\n"
    "v_max3_f32 v204, v204, v30, v31\n"
    "s_cmp_gt_u32 s54, 0\n"
    "s_cselect_b32 s50, 0, 0xf149f2ca\n"
    "v_add_f32 v204, s50, v204\n"
    "v_max_f32 v205, v205, v204\n"
    "v_mfma_f32_32x32x16_bf16 v[16:31], v[72:75], v[176:179], 0\n"
    "s_nop 11\n"
    "v_max_f32 v204, v16, v17\n"
    "v_max3_f32 v204, v204, v18, v19\n"
    "v_max3_f32 v204, v204, v20, v21\n"
    "v_max3_f32 v204, v204, v22, v23\n"
    "v_max3_f32 v204, v204, v24, v25\n"
    "v_max3_f32 v204, v204, v26, v27\n"
    "v_max3_f32 v204, v204, v28, v29\n"
    "v_max3_f32 v204, v204, v30, v31\n"
    "s_cmp_gt_u32 s54, 1\n"
    "s_cselect_b32 s50, 0, 0xf149f2ca\n"
    "v_add_f32 v204, s50, v204\n"
    "v_max_f32 v205, v205, v204\n"
    "v_mfma_f32_32x32x16_bf16 v[16:31], v[76:79], v[176:179], 0\n"
    "s_nop 11\n"
    "v_max_f32 v204, v16, v17\n"
    "v_max3_f32 v204, v204, v18, v19\n"
    "v_max3_f32 v204, v204, v20, v21\n"
    "v_max3_f32 v204, v204, v22, v23\n"
    "v_max3_f32 v204, v204, v24, v25\n"
    "v_max3_f32 v204, v204, v26, v27\n"
    "v_max3_f32 v204, v204, v28, v29\n"
    "v_max3_f32 v204, v204, v30, v31\n"
    "s_cmp_gt_u32 s54, 1\n"
    "s_cselect_b32 s50, 0, 0xf149f2ca\n"
    "v_add_f32 v204, s50, v204\n"
    "v_max_f32 v205, v205, v204\n"
    "v_mfma_f32_32x32x16_bf16 v[16:31], v[80:83], v[176:179], 0\n"
    "s_nop 11\n"
    "v_max_f32 v204, v16, v17\n"
    "v_max3_f32 v204, v204, v18, v19\n"
    "v_max3_f32 v204, v204, v20, v21\n"
    "v_max3_f32 v204, v204, v22, v23\n"
    "v_max3_f32 v204, v204, v24, v25\n"
    "v_max3_f32 v204, v204, v26, v27\n"
    "v_max3_f32 v204, v204, v28, v29\n"
    "v_max3_f32 v204, v204, v30, v31\n"
    "s_cmp_gt_u32 s54, 2\n"
    "s_cselect_b32 s50, 0, 0xf149f2ca\n"
    "v_add_f32 v204, s50, v204\n"
    "v_max_f32 v205, v205, v204\n"
    "v_mfma_f32_32x32x16_bf16 v[16:31], v[84:87], v[176:179], 0\n"
    "s_nop 11\n"
    "v_max_f32 v204, v16, v17\n"
    "v_max3_f32 v204, v204, v18, v19\n"
    "v_max3_f32 v204, v204, v20, v21\n"
    "v_max3_f32 v204, v204, v22, v23\n"
    "v_max3_f32 v204, v204, v24, v25\n"
    "v_max3_f32 v204, v204, v26, v27\n"
    "v_max3_f32 v204, v204, v28, v29\n"
    "v_max3_f32 v204, v204, v30, v31\n"
    "s_cmp_gt_u32 s54, 2\n"
    "s_cselect_b32 s50, 0, 0xf149f2ca\n"
    "v_add_f32 v204, s50, v204\n"
    "v_max_f32 v205, v205, v204\n"
    "v_mfma_f32_32x32x16_bf16 v[16:31], v[88:91], v[176:179], 0\n"
    "s_nop 11\n"
    "v_max_f32 v204, v16, v17\n"
    "v_max3_f32 v204, v204, v18, v19\n"
    "v_max3_f32 v204, v204, v20, v21\n"
    "v_max3_f32 v204, v204, v22, v23\n"
    "v_max3_f32 v204, v204, v24, v25\n"
    "v_max3_f32 v204, v204, v26, v27\n"
    "v_max3_f32 v204, v204, v28, v29\n"
    "v_max3_f32 v204, v204, v30, v31\n"
    "s_cmp_gt_u32 s54, 3\n"
    "s_cselect_b32 s50, 0, 0xf149f2ca\n"
    "v_add_f32 v204, s50, v204\n"
    "v_max_f32 v205, v205, v204\n"
    "v_min_f32 v16, -v224, 0\n"
    "v_min_f32 v17, -v225, 0\n"
    "v_min_f32 v18, -v226, 0\n"
    "v_min_f32 v19, -v227, 0\n"
    "v_min_f32 v20, -v228, 0\n"
    "v_min_f32 v21, -v229, 0\n"
    "v_min_f32 v22, -v230, 0\n"
    "v_min_f32 v23, -v231, 0\n"
    "v_min_f32 v24, -v232, 0\n"
    "v_min_f32 v25, -v233, 0\n"
    "v_min_f32 v26, -v234, 0\n"
    "v_min_f32 v27, -v235, 0\n"
    "v_min_f32 v28, -v236, 0\n"
    "v_min_f32 v29, -v237, 0\n"
    "v_min_f32 v30, -v238, 0\n"
    "v_min_f32 v31, -v239, 0\n"
    "s_nop 1\n"
    "v_mfma_f32_32x32x16_bf16 v[16:31], v[92:95], v[176:179], v[16:31]\n"
    "s_nop 11\n"
    "v_max_f32 v204, v16, v17\n"
    "v_max3_f32 v204, v204, v18, v19\n"
    "v_max3_f32 v204, v204, v20, v21\n"
    "v_max3_f32 v204, v204, v22, v23\n"
    "v_max3_f32 v204, v204, v24, v25\n"
    "v_max3_f32 v204, v204, v26, v27\n"
    "v_max3_f32 v204, v204, v28, v29\n"
    "v_max3_f32 v204, v204, v30, v31\n"
    "s_cmp_gt_u32 s54, 3\n"
    "s_cselect_b32 s50, 0, 0xf149f2ca\n"
    "v_add_f32 v204, s50, v204\n"
    "v_max_f32 v205, v205, v204\n"
    "v_mov_b32 v204, v205\n"
    "s_nop 1\n"
    "v_permlane32_swap_b32 v205, v204\n"
    "v_max_f32 v205, v205, v204\n"
    "v_sub_f32 v204, v202, v205\n"
    "v_exp_f32 v204, v204\n"
    "s_nop 0\n"
    "v_mul_f32 v184, v184, v204\n"
    "v_mul_f32 v185, v185, v204\n"
    "v_mul_f32 v186, v186, v204\n"
    "v_mul_f32 v187, v187, v204\n"
    "v_mul_f32 v188, v188, v204\n"
    "v_mul_f32 v189, v189, v204\n"
    "v_mul_f32 v190, v190, v204\n"
    "v_mul_f32 v191, v191, v204\n"
    "v_mul_f32 v200, v200, v204\n"
    "s_cmp_gt_u32 s54, 0\n"
    "s_cselect_b32 s50, 0, 0xf149f2ca\n"
    "v_sub_f32 v32, s50, v205\n"
    "v_sub_f32 v33, s50, v205\n"
    "v_sub_f32 v34, s50, v205\n"
    "v_sub_f32 v35, s50, v205\n"
    "v_sub_f32 v36, s50, v205\n"
    "v_sub_f32 v37, s50, v205\n"
    "v_sub_f32 v38, s50, v205\n"
    "v_sub_f32 v39, s50, v205\n"
    "v_sub_f32 v40, s50, v205\n"
    "v_sub_f32 v41, s50, v205\n"
    "v_sub_f32 v42, s50, v205\n"
    "v_sub_f32 v43, s50, v205\n"
    "v_sub_f32 v44, s50, v205\n"
    "v_sub_f32 v45, s50, v205\n"
    "v_sub_f32 v46, s50, v205\n"
    "v_sub_f32 v47, s50, v205\n"
    "s_nop 1\n"
    "v_mfma_f32_32x32x16_bf16 v[16:31], v[64:67], v[176:179], v[32:47]\n"
    "s_nop 11\n"
    "v_exp_f32 v16, v16\n"
    "v_exp_f32 v17, v17\n"
    "v_exp_f32 v18, v18\n"
    "v_exp_f32 v19, v19\n"
    "v_cvt_pk_bf16_f32 v216, v16, v17\n"
    "v_exp_f32 v20, v20\n"
    "v_exp_f32 v21, v21\n"
    "v_cvt_pk_bf16_f32 v217, v18, v19\n"
    "v_exp_f32 v22, v22\n"
    "v_exp_f32 v23, v23\n"
    "v_cvt_pk_bf16_f32 v218, v20, v21\n"
    "v_exp_f32 v24, v24\n"
    "v_exp_f32 v25, v25\n"
    "v_cvt_pk_bf16_f32 v219, v22, v23\n"
    "v_exp_f32 v26, v26\n"
    "v_exp_f32 v27, v27\n"
    "v_cvt_pk_bf16_f32 v220, v24, v25\n"
    "v_exp_f32 v28, v28\n"
    "v_exp_f32 v29, v29\n"
    "v_cvt_pk_bf16_f32 v221, v26, v27\n"
    "v_exp_f32 v30, v30\n"
    "v_exp_f32 v31, v31\n"
    "v_cvt_pk_bf16_f32 v222, v28, v29\n"
    "v_cvt_pk_bf16_f32 v223, v30, v31\n"
    "v_mfma_f32_32x32x16_bf16 v[128:143], v[96:99], v[216:219], 0\n"
    "v_mfma_f32_16x16x32_bf16 v[208:211], v[212:215], v[216:219], 0\n"
    "v_mfma_f32_32x32x16_bf16 v[144:159], v[96:99], v[220:223], 0\n"
    "v_mfma_f32_16x16x32_bf16 v[208:211], v[212:215], v[220:223], v[208:211]\n"
    "s_cmp_gt_u32 s54, 0\n"
    "s_cselect_b32 s50, 0, 0xf149f2ca\n"
    "v_sub_f32 v32, s50, v205\n"
    "v_sub_f32 v33, s50, v205\n"
    "v_sub_f32 v34, s50, v205\n"
    "v_sub_f32 v35, s50, v205\n"
    "v_sub_f32 v36, s50, v205\n"
    "v_sub_f32 v37, s50, v205\n"
    "v_sub_f32 v38, s50, v205\n"
    "v_sub_f32 v39, s50, v205\n"
    "v_sub_f32 v40, s50, v205\n"
    "v_sub_f32 v41, s50, v205\n"
    "v_sub_f32 v42, s50, v205\n"
    "v_sub_f32 v43, s50, v205\n"
    "v_sub_f32 v44, s50, v205\n"
    "v_sub_f32 v45, s50, v205\n"
    "v_sub_f32 v46, s50, v205\n"
    "v_sub_f32 v47, s50, v205\n"
    "s_nop 1\n"
    "v_mfma_f32_32x32x16_bf16 v[16:31], v[68:71], v[176:179], v[32:47]\n"
    "s_nop 11\n"
    "v_exp_f32 v16, v16\n"
    "v_exp_f32 v17, v17\n"
    "v_exp_f32 v18, v18\n"
    "v_exp_f32 v19, v19\n"
    "v_cvt_pk_bf16_f32 v216, v16, v17\n"
    "v_exp_f32 v20, v20\n"
    "v_exp_f32 v21, v21\n"
    "v_cvt_pk_bf16_f32 v217, v18, v19\n"
    "v_exp_f32 v22, v22\n"
    "v_exp_f32 v23, v23\n"
    "v_cvt_pk_bf16_f32 v218, v20, v21\n"
    "v_exp_f32 v24, v24\n"
    "v_exp_f32 v25, v25\n"
    "v_cvt_pk_bf16_f32 v219, v22, v23\n"
    "v_exp_f32 v26, v26\n"
    "v_exp_f32 v27, v27\n"
    "v_cvt_pk_bf16_f32 v220, v24, v25\n"
    "v_exp_f32 v28, v28\n"
    "v_exp_f32 v29, v29\n"
    "v_cvt_pk_bf16_f32 v221, v26, v27\n"
    "v_exp_f32 v30, v30\n"
    "v_exp_f32 v31, v31\n"
    "v_cvt_pk_bf16_f32 v222, v28, v29\n"
    "v_cvt_pk_bf16_f32 v223, v30, v31\n"
    "v_mfma_f32_32x32x16_bf16 v[128:143], v[100:103], v[216:219], v[128:143]\n"
    "v_mfma_f32_16x16x32_bf16 v[208:211], v[212:215], v[216:219], v[208:211]\n"
    "v_mfma_f32_32x32x16_bf16 v[144:159], v[100:103], v[220:223], v[144:159]\n"
    "v_mfma_f32_16x16x32_bf16 v[208:211], v[212:215], v[220:223], v[208:211]\n"
    "s_cmp_gt_u32 s54, 1\n"
    "s_cselect_b32 s50, 0, 0xf149f2ca\n"
    "v_sub_f32 v32, s50, v205\n"
    "v_sub_f32 v33, s50, v205\n"
    "v_sub_f32 v34, s50, v205\n"
    "v_sub_f32 v35, s50, v205\n"
    "v_sub_f32 v36, s50, v205\n"
    "v_sub_f32 v37, s50, v205\n"
    "v_sub_f32 v38, s50, v205\n"
    "v_sub_f32 v39, s50, v205\n"
    "v_sub_f32 v40, s50, v205\n"
    "v_sub_f32 v41, s50, v205\n"
    "v_sub_f32 v42, s50, v205\n"
    "v_sub_f32 v43, s50, v205\n"
    "v_sub_f32 v44, s50, v205\n"
    "v_sub_f32 v45, s50, v205\n"
    "v_sub_f32 v46, s50, v205\n"
    "v_sub_f32 v47, s50, v205\n"
    "s_nop 1\n"
    "v_mfma_f32_32x32x16_bf16 v[16:31], v[72:75], v[176:179], v[32:47]\n"
    "s_nop 11\n"
    "v_exp_f32 v16, v16\n"
    "v_exp_f32 v17, v17\n"
    "v_exp_f32 v18, v18\n"
    "v_exp_f32 v19, v19\n"
    "v_cvt_pk_bf16_f32 v216, v16, v17\n"
    "v_exp_f32 v20, v20\n"
    "v_exp_f32 v21, v21\n"
    "v_cvt_pk_bf16_f32 v217, v18, v19\n"
    "v_exp_f32 v22, v22\n"
    "v_exp_f32 v23, v23\n"
    "v_cvt_pk_bf16_f32 v218, v20, v21\n"
    "v_exp_f32 v24, v24\n"
    "v_exp_f32 v25, v25\n"
    "v_cvt_pk_bf16_f32 v219, v22, v23\n"
    "v_exp_f32 v26, v26\n"
    "v_exp_f32 v27, v27\n"
    "v_cvt_pk_bf16_f32 v220, v24, v25\n"
    "v_exp_f32 v28, v28\n"
    "v_exp_f32 v29, v29\n"
    "v_cvt_pk_bf16_f32 v221, v26, v27\n"
    "v_exp_f32 v30, v30\n"
    "v_exp_f32 v31, v31\n"
    "v_cvt_pk_bf16_f32 v222, v28, v29\n"
    "v_cvt_pk_bf16_f32 v223, v30, v31\n"
    "v_mfma_f32_32x32x16_bf16 v[128:143], v[104:107], v[216:219], v[128:143]\n"
    "v_mfma_f32_16x16x32_bf16 v[208:211], v[212:215], v[216:219], v[208:211]\n"
    "v_mfma_f32_32x32x16_bf16 v[144:159], v[104:107], v[220:223], v[144:159]\n"
    "v_mfma_f32_16x16x32_bf16 v[208:211], v[212:215], v[220:223], v[208:211]\n"
    "s_cmp_gt_u32 s54, 1\n"
    "s_cselect_b32 s50, 0, 0xf149f2ca\n"
    "v_sub_f32 v32, s50, v205\n"
    "v_sub_f32 v33, s50, v205\n"
    "v_sub_f32 v34, s50, v205\n"
    "v_sub_f32 v35, s50, v205\n"
    "v_sub_f32 v36, s50, v205\n"
    "v_sub_f32 v37, s50, v205\n"
    "v_sub_f32 v38, s50, v205\n"
    "v_sub_f32 v39, s50, v205\n"
    "v_sub_f32 v40, s50, v205\n"
    "v_sub_f32 v41, s50, v205\n"
    "v_sub_f32 v42, s50, v205\n"
    "v_sub_f32 v43, s50, v205\n"
    "v_sub_f32 v44, s50, v205\n"
    "v_sub_f32 v45, s50, v205\n"
    "v_sub_f32 v46, s50, v205\n"
    "v_sub_f32 v47, s50, v205\n"
    "s_nop 1\n"
    "v_mfma_f32_32x32x16_bf16 v[16:31], v[76:79], v[176:179], v[32:47]\n"
    "s_nop 11\n"
    "v_exp_f32 v16, v16\n"
    "v_exp_f32 v17, v17\n"
    "v_exp_f32 v18, v18\n"
    "v_exp_f32 v19, v19\n"
    "v_cvt_pk_bf16_f32 v216, v16, v17\n"
    "v_exp_f32 v20, v20\n"
    "v_exp_f32 v21, v21\n"
    "v_cvt_pk_bf16_f32 v217, v18, v19\n"
    "v_exp_f32 v22, v22\n"
    "v_exp_f32 v23, v23\n"
    "v_cvt_pk_bf16_f32 v218, v20, v21\n"
    "v_exp_f32 v24, v24\n"
    "v_exp_f32 v25, v25\n"
    "v_cvt_pk_bf16_f32 v219, v22, v23\n"
    "v_exp_f32 v26, v26\n"
    "v_exp_f32 v27, v27\n"
    "v_cvt_pk_bf16_f32 v220, v24, v25\n"
    "v_exp_f32 v28, v28\n"
    "v_exp_f32 v29, v29\n"
    "v_cvt_pk_bf16_f32 v221, v26, v27\n"
    "v_exp_f32 v30, v30\n"
    "v_exp_f32 v31, v31\n"
    "v_cvt_pk_bf16_f32 v222, v28, v29\n"
    "v_cvt_pk_bf16_f32 v223, v30, v31\n"
    "v_mfma_f32_32x32x16_bf16 v[128:143], v[108:111], v[216:219], v[128:143]\n"
    "v_mfma_f32_16x16x32_bf16 v[208:211], v[212:215], v[216:219], v[208:211]\n"
    "v_mfma_f32_32x32x16_bf16 v[144:159], v[108:111], v[220:223], v[144:159]\n"
    "v_mfma_f32_16x16x32_bf16 v[208:211], v[212:215], v[220:223], v[208:211]\n"
    "s_cmp_gt_u32 s54, 2\n"
    "s_cselect_b32 s50, 0, 0xf149f2ca\n"
    "v_sub_f32 v32, s50, v205\n"
    "v_sub_f32 v33, s50, v205\n"
    "v_sub_f32 v34, s50, v205\n"
    "v_sub_f32 v35, s50, v205\n"
    "v_sub_f32 v36, s50, v205\n"
    "v_sub_f32 v37, s50, v205\n"
    "v_sub_f32 v38, s50, v205\n"
    "v_sub_f32 v39, s50, v205\n"
    "v_sub_f32 v40, s50, v205\n"
    "v_sub_f32 v41, s50, v205\n"
    "v_sub_f32 v42, s50, v205\n"
    "v_sub_f32 v43, s50, v205\n"
    "v_sub_f32 v44, s50, v205\n"
    "v_sub_f32 v45, s50, v205\n"
    "v_sub_f32 v46, s50, v205\n"
    "v_sub_f32 v47, s50, v205\n"
    "s_nop 1\n"
    "v_mfma_f32_32x32x16_bf16 v[16:31], v[80:83], v[176:179], v[32:47]\n"
    "s_nop 11\n"
    "v_exp_f32 v16, v16\n"
    "v_exp_f32 v17, v17\n"
    "v_exp_f32 v18, v18\n"
    "v_exp_f32 v19, v19\n"
    "v_cvt_pk_bf16_f32 v216, v16, v17\n"
    "v_exp_f32 v20, v20\n"
    "v_exp_f32 v21, v21\n"
    "v_cvt_pk_bf16_f32 v217, v18, v19\n"
    "v_exp_f32 v22, v22\n"
    "v_exp_f32 v23, v23\n"
    "v_cvt_pk_bf16_f32 v218, v20, v21\n"
    "v_exp_f32 v24, v24\n"
    "v_exp_f32 v25, v25\n"
    "v_cvt_pk_bf16_f32 v219, v22, v23\n"
    "v_exp_f32 v26, v26\n"
    "v_exp_f32 v27, v27\n"
    "v_cvt_pk_bf16_f32 v220, v24, v25\n"
    "v_exp_f32 v28, v28\n"
    "v_exp_f32 v29, v29\n"
    "v_cvt_pk_bf16_f32 v221, v26, v27\n"
    "v_exp_f32 v30, v30\n"
    "v_exp_f32 v31, v31\n"
    "v_cvt_pk_bf16_f32 v222, v28, v29\n"
    "v_cvt_pk_bf16_f32 v223, v30, v31\n"
    "v_mfma_f32_32x32x16_bf16 v[128:143], v[112:115], v[216:219], v[128:143]\n"
    "v_mfma_f32_16x16x32_bf16 v[208:211], v[212:215], v[216:219], v[208:211]\n"
    "v_mfma_f32_32x32x16_bf16 v[144:159], v[112:115], v[220:223], v[144:159]\n"
    "v_mfma_f32_16x16x32_bf16 v[208:211], v[212:215], v[220:223], v[208:211]\n"
    "s_cmp_gt_u32 s54, 2\n"
    "s_cselect_b32 s50, 0, 0xf149f2ca\n"
    "v_sub_f32 v32, s50, v205\n"
    "v_sub_f32 v33, s50, v205\n"
    "v_sub_f32 v34, s50, v205\n"
    "v_sub_f32 v35, s50, v205\n"
    "v_sub_f32 v36, s50, v205\n"
    "v_sub_f32 v37, s50, v205\n"
    "v_sub_f32 v38, s50, v205\n"
    "v_sub_f32 v39, s50, v205\n"
    "v_sub_f32 v40, s50, v205\n"
    "v_sub_f32 v41, s50, v205\n"
    "v_sub_f32 v42, s50, v205\n"
    "v_sub_f32 v43, s50, v205\n"
    "v_sub_f32 v44, s50, v205\n"
    "v_sub_f32 v45, s50, v205\n"
    "v_sub_f32 v46, s50, v205\n"
    "v_sub_f32 v47, s50, v205\n"
    "s_nop 1\n"
    "v_mfma_f32_32x32x16_bf16 v[16:31], v[84:87], v[176:179], v[32:47]\n"
    "s_nop 11\n"
    "v_exp_f32 v16, v16\n"
    "v_exp_f32 v17, v17\n"
    "v_exp_f32 v18, v18\n"
    "v_exp_f32 v19, v19\n"
    "v_cvt_pk_bf16_f32 v216, v16, v17\n"
    "v_exp_f32 v20, v20\n"
    "v_exp_f32 v21, v21\n"
    "v_cvt_pk_bf16_f32 v217, v18, v19\n"
    "v_exp_f32 v22, v22\n"
    "v_exp_f32 v23, v23\n"
    "v_cvt_pk_bf16_f32 v218, v20, v21\n"
    "v_exp_f32 v24, v24\n"
    "v_exp_f32 v25, v25\n"
    "v_cvt_pk_bf16_f32 v219, v22, v23\n"
    "v_exp_f32 v26, v26\n"
    "v_exp_f32 v27, v27\n"
    "v_cvt_pk_bf16_f32 v220, v24, v25\n"
    "v_exp_f32 v28, v28\n"
    "v_exp_f32 v29, v29\n"
    "v_cvt_pk_bf16_f32 v221, v26, v27\n"
    "v_exp_f32 v30, v30\n"
    "v_exp_f32 v31, v31\n"
    "v_cvt_pk_bf16_f32 v222, v28, v29\n"
    "v_cvt_pk_bf16_f32 v223, v30, v31\n"
    "v_mfma_f32_32x32x16_bf16 v[128:143], v[116:119], v[216:219], v[128:143]\n"
    "v_mfma_f32_16x16x32_bf16 v[208:211], v[212:215], v[216:219], v[208:211]\n"
    "v_mfma_f32_32x32x16_bf16 v[144:159], v[116:119], v[220:223], v[144:159]\n"
    "v_mfma_f32_16x16x32_bf16 v[208:211], v[212:215], v[220:223], v[208:211]\n"
    "s_cmp_gt_u32 s54, 3\n"
    "s_cselect_b32 s50, 0, 0xf149f2ca\n"
    "v_sub_f32 v32, s50, v205\n"
    "v_sub_f32 v33, s50, v205\n"
    "v_sub_f32 v34, s50, v205\n"
    "v_sub_f32 v35, s50, v205\n"
    "v_sub_f32 v36, s50, v205\n"
    "v_sub_f32 v37, s50, v205\n"
    "v_sub_f32 v38, s50, v205\n"
    "v_sub_f32 v39, s50, v205\n"
    "v_sub_f32 v40, s50, v205\n"
    "v_sub_f32 v41, s50, v205\n"
    "v_sub_f32 v42, s50, v205\n"
    "v_sub_f32 v43, s50, v205\n"
    "v_sub_f32 v44, s50, v205\n"
    "v_sub_f32 v45, s50, v205\n"
    "v_sub_f32 v46, s50, v205\n"
    "v_sub_f32 v47, s50, v205\n"
    "s_nop 1\n"
    "v_mfma_f32_32x32x16_bf16 v[16:31], v[88:91], v[176:179], v[32:47]\n"
    "s_nop 11\n"
    "v_exp_f32 v16, v16\n"
    "v_exp_f32 v17, v17\n"
    "v_exp_f32 v18, v18\n"
    "v_exp_f32 v19, v19\n"
    "v_cvt_pk_bf16_f32 v216, v16, v17\n"
    "v_exp_f32 v20, v20\n"
    "v_exp_f32 v21, v21\n"
    "v_cvt_pk_bf16_f32 v217, v18, v19\n"
    "v_exp_f32 v22, v22\n"
    "v_exp_f32 v23, v23\n"
    "v_cvt_pk_bf16_f32 v218, v20, v21\n"
    "v_exp_f32 v24, v24\n"
    "v_exp_f32 v25, v25\n"
    "v_cvt_pk_bf16_f32 v219, v22, v23\n"
    "v_exp_f32 v26, v26\n"
    "v_exp_f32 v27, v27\n"
    "v_cvt_pk_bf16_f32 v220, v24, v25\n"
    "v_exp_f32 v28, v28\n"
    "v_exp_f32 v29, v29\n"
    "v_cvt_pk_bf16_f32 v221, v26, v27\n"
    "v_exp_f32 v30, v30\n"
    "v_exp_f32 v31, v31\n"
    "v_cvt_pk_bf16_f32 v222, v28, v29\n"
    "v_cvt_pk_bf16_f32 v223, v30, v31\n"
    "v_mfma_f32_32x32x16_bf16 v[128:143], v[120:123], v[216:219], v[128:143]\n"
    "v_mfma_f32_16x16x32_bf16 v[208:211], v[212:215], v[216:219], v[208:211]\n"
    "v_mfma_f32_32x32x16_bf16 v[144:159], v[120:123], v[220:223], v[144:159]\n"
    "v_mfma_f32_16x16x32_bf16 v[208:211], v[212:215], v[220:223], v[208:211]\n"
    "s_cmp_gt_u32 s54, 3\n"
    "s_cselect_b32 s50, 0, 0xf149f2ca\n"
    "v_sub_f32 v32, s50, v205\n"
    "v_sub_f32 v33, s50, v205\n"
    "v_sub_f32 v34, s50, v205\n"
    "v_sub_f32 v35, s50, v205\n"
    "v_sub_f32 v36, s50, v205\n"
    "v_sub_f32 v37, s50, v205\n"
    "v_sub_f32 v38, s50, v205\n"
    "v_sub_f32 v39, s50, v205\n"
    "v_sub_f32 v40, s50, v205\n"
    "v_sub_f32 v41, s50, v205\n"
    "v_sub_f32 v42, s50, v205\n"
    "v_sub_f32 v43, s50, v205\n"
    "v_sub_f32 v44, s50, v205\n"
    "v_sub_f32 v45, s50, v205\n"
    "v_sub_f32 v46, s50, v205\n"
    "v_sub_f32 v47, s50, v205\n"
    "v_min_f32 v32, -v224, v32\n"
    "v_min_f32 v33, -v225, v33\n"
    "v_min_f32 v34, -v226, v34\n"
    "v_min_f32 v35, -v227, v35\n"
    "v_min_f32 v36, -v228, v36\n"
    "v_min_f32 v37, -v229, v37\n"
    "v_min_f32 v38, -v230, v38\n"
    "v_min_f32 v39, -v231, v39\n"
    "v_min_f32 v40, -v232, v40\n"
    "v_min_f32 v41, -v233, v41\n"
    "v_min_f32 v42, -v234, v42\n"
    "v_min_f32 v43, -v235, v43\n"
    "v_min_f32 v44, -v236, v44\n"
    "v_min_f32 v45, -v237, v45\n"
    "v_min_f32 v46, -v238, v46\n"
    "v_min_f32 v47, -v239, v47\n"
    "s_nop 1\n"
    "v_mfma_f32_32x32x16_bf16 v[16:31], v[92:95], v[176:179], v[32:47]\n"
    "s_nop 11\n"
    "v_exp_f32 v16, v16\n"
    "v_exp_f32 v17, v17\n"
    "v_exp_f32 v18, v18\n"
    "v_exp_f32 v19, v19\n"
    "v_cvt_pk_bf16_f32 v216, v16, v17\n"
    "v_exp_f32 v20, v20\n"
    "v_exp_f32 v21, v21\n"
    "v_cvt_pk_bf16_f32 v217, v18, v19\n"
    "v_exp_f32 v22, v22\n"
    "v_exp_f32 v23, v23\n"
    "v_cvt_pk_bf16_f32 v218, v20, v21\n"
    "v_exp_f32 v24, v24\n"
    "v_exp_f32 v25, v25\n"
    "v_cvt_pk_bf16_f32 v219, v22, v23\n"
    "v_exp_f32 v26, v26\n"
    "v_exp_f32 v27, v27\n"
    "v_cvt_pk_bf16_f32 v220, v24, v25\n"
    "v_exp_f32 v28, v28\n"
    "v_exp_f32 v29, v29\n"
    "v_cvt_pk_bf16_f32 v221, v26, v27\n"
    "v_exp_f32 v30, v30\n"
    "v_exp_f32 v31, v31\n"
    "v_cvt_pk_bf16_f32 v222, v28, v29\n"
    "v_cvt_pk_bf16_f32 v223, v30, v31\n"
    "v_mfma_f32_32x32x16_bf16 v[128:143], v[124:127], v[216:219], v[128:143]\n"
    "v_mfma_f32_16x16x32_bf16 v[208:211], v[212:215], v[216:219], v[208:211]\n"
    "v_mfma_f32_32x32x16_bf16 v[144:159], v[124:127], v[220:223], v[144:159]\n"
    "v_mfma_f32_16x16x32_bf16 v[208:211], v[212:215], v[220:223], v[208:211]\n"
    "s_nop 15\n"
    "v_add_f32 v204, v200, v208\n"
    "s_nop 1\n"
    "s_branch Lslowb_14\n"
    "Lslow_21:\n"
    "s_nop 15\n"
    "v_mov_b32 v205, v203\n"
    "v_mfma_f32_32x32x16_bf16 v[16:31], v[64:67], v[180:183], 0\n"
    "s_nop 11\n"
    "v_max_f32 v204, v16, v17\n"
    "v_max3_f32 v204, v204, v18, v19\n"
    "v_max3_f32 v204, v204, v20, v21\n"
    "v_max3_f32 v204, v204, v22, v23\n"
    "v_max3_f32 v204, v204, v24, v25\n"
    "v_max3_f32 v204, v204, v26, v27\n"
    "v_max3_f32 v204, v204, v28, v29\n"
    "v_max3_f32 v204, v204, v30, v31\n"
    "s_cmp_gt_u32 s54, 0\n"
    "s_cselect_b32 s50, 0, 0xf149f2ca\n"
    "v_add_f32 v204, s50, v204\n"
    "v_max_f32 v205, v205, v204\n"
    "v_mfma_f32_32x32x16_bf16 v[16:31], v[68:71], v[180:183], 0\n"
    "s_nop 11\n"
    "v_max_f32 v204, v16, v17\n"
    "v_max3_f32 v204, v204, v18, v19\n"
    "v_max3_f32 v204, v204, v20, v21\n"
    "v_max3_f32 v204, v204, v22, v23\n"
    "v_max3_f32 v204, v204, v24, v25\n"
    "v_max3_f32 v204, v204, v26, v27\n"
    "v_max3_f32 v204, v204, v28, v29\n"
    "v_max3_f32 v204, v204, v30, v31\n"
    "s_cmp_gt_u32 s54, 0\n"
    "s_cselect_b32 s50, 0, 0xf149f2ca\n"
    "v_add_f32 v204, s50, v204\n"
    "v_max_f32 v205, v205, v204\n"
    "v_mfma_f32_32x32x16_bf16 v[16:31], v[72:75], v[180:183], 0\n"
    "s_nop 11\n"
    "v_max_f32 v204, v16, v17\n"
    "v_max3_f32 v204, v204, v18, v19\n"
    "v_max3_f32 v204, v204, v20, v21\n"
    "v_max3_f32 v204, v204, v22, v23\n"
    "v_max3_f32 v204, v204, v24, v25\n"
    "v_max3_f32 v204, v204, v26, v27\n"
    "v_max3_f32 v204, v204, v28, v29\n"
    "v_max3_f32 v204, v204, v30, v31\n"
    "s_cmp_gt_u32 s54, 1\n"
    "s_cselect_b32 s50, 0, 0xf149f2ca\n"
    "v_add_f32 v204, s50, v204\n"
    "v_max_f32 v205, v205, v204\n"
    "v_mfma_f32_32x32x16_bf16 v[16:31], v[76:79], v[180:183], 0\n"
    "s_nop 11\n"
    "v_max_f32 v204, v16, v17\n"
    "v_max3_f32 v204, v204, v18, v19\n"
    "v_max3_f32 v204, v204, v20, v21\n"
    "v_max3_f32 v204, v204, v22, v23\n"
    "v_max3_f32 v204, v204, v24, v25\n"
    "v_max3_f32 v204, v204, v26, v27\n"
    "v_max3_f32 v204, v204, v28, v29\n"
    "v_max3_f32 v204, v204, v30, v31\n"
    "s_cmp_gt_u32 s54, 1\n"
    "s_cselect_b32 s50, 0, 0xf149f2ca\n"
    "v_add_f32 v204, s50, v204\n"
    "v_max_f32 v205, v205, v204\n"
    "v_mfma_f32_32x32x16_bf16 v[16:31], v[80:83], v[180:183], 0\n"
    "s_nop 11\n"
    "v_max_f32 v204, v16, v17\n"
    "v_max3_f32 v204, v204, v18, v19\n"
    "v_max3_f32 v204, v204, v20, v21\n"
    "v_max3_f32 v204, v204, v22, v23\n"
    "v_max3_f32 v204, v204, v24, v25\n"
    "v_max3_f32 v204, v204, v26, v27\n"
    "v_max3_f32 v204, v204, v28, v29\n"
    "v_max3_f32 v204, v204, v30, v31\n"
    "s_cmp_gt_u32 s54, 2\n"
    "s_cselect_b32 s50, 0, 0xf149f2ca\n"
    "v_add_f32 v204, s50, v204\n"
    "v_max_f32 v205, v205, v204\n"
    "v_mfma_f32_32x32x16_bf16 v[16:31], v[84:87], v[180:183], 0\n"
    "s_nop 11\n"
    "v_max_f32 v204, v16, v17\n"
    "v_max3_f32 v204, v204, v18, v19\n"
    "v_max3_f32 v204, v204, v20, v21\n"
    "v_max3_f32 v204, v204, v22, v23\n"
    "v_max3_f32 v204, v204, v24, v25\n"
    "v_max3_f32 v204, v204, v26, v27\n"
    "v_max3_f32 v204, v204, v28, v29\n"
    "v_max3_f32 v204, v204, v30, v31\n"
    "s_cmp_gt_u32 s54, 2\n"
    "s_cselect_b32 s50, 0, 0xf149f2ca\n"
    "v_add_f32 v204, s50, v204\n"
    "v_max_f32 v205, v205, v204\n"
    "v_min_f32 v16, -v224, 0\n"
    "v_min_f32 v17, -v225, 0\n"
    "v_min_f32 v18, -v226, 0\n"
    "v_min_f32 v19, -v227, 0\n"
    "v_min_f32 v20, -v228, 0\n"
    "v_min_f32 v21, -v229, 0\n"
    "v_min_f32 v22, -v230, 0\n"
    "v_min_f32 v23, -v231, 0\n"
    "v_min_f32 v24, -v232, 0\n"
    "v_min_f32 v25, -v233, 0\n"
    "v_min_f32 v26, -v234, 0\n"
    "v_min_f32 v27, -v235, 0\n"
    "v_min_f32 v28, -v236, 0\n"
    "v_min_f32 v29, -v237, 0\n"
    "v_min_f32 v30, -v238, 0\n"
    "v_min_f32 v31, -v239, 0\n"
    "s_nop 1\n"
    "v_mfma_f32_32x32x16_bf16 v[16:31], v[88:91], v[180:183], v[16:31]\n"
    "s_nop 11\n"
    "v_max_f32 v204, v16, v17\n"
    "v_max3_f32 v204, v204, v18, v19\n"
    "v_max3_f32 v204, v204, v20, v21\n"
    "v_max3_f32 v204, v204, v22, v23\n"
    "v_max3_f32 v204, v204, v24, v25\n"
    "v_max3_f32 v204, v204, v26, v27\n"
    "v_max3_f32 v204, v204, v28, v29\n"
    "v_max3_f32 v204, v204, v30, v31\n"
    "s_cmp_gt_u32 s54, 3\n"
    "s_cselect_b32 s50, 0, 0xf149f2ca\n"
    "v_add_f32 v204, s50, v204\n"
    "v_max_f32 v205, v205, v204\n"
    "v_mov_b32 v204, v205\n"
    "s_nop 1\n"
    "v_permlane32_swap_b32 v205, v204\n"
    "v_max_f32 v205, v205, v204\n"
    "v_sub_f32 v204, v203, v205\n"
    "v_exp_f32 v204, v204\n"
    "s_nop 0\n"
    "v_mul_f32 v192, v192, v204\n"
    "v_mul_f32 v193, v193, v204\n"
    "v_mul_f32 v194, v194, v204\n"
    "v_mul_f32 v195, v195, v204\n"
    "v_mul_f32 v196, v196, v204\n"
    "v_mul_f32 v197, v197, v204\n"
    "v_mul_f32 v198, v198, v204\n"
    "v_mul_f32 v199, v199, v204\n"
    "v_mul_f32 v201, v201, v204\n"
    "s_cmp_gt_u32 s54, 0\n"
    "s_cselect_b32 s50, 0, 0xf149f2ca\n"
    "v_sub_f32 v32, s50, v205\n"
    "v_sub_f32 v33, s50, v205\n"
    "v_sub_f32 v34, s50, v205\n"
    "v_sub_f32 v35, s50, v205\n"
    "v_sub_f32 v36, s50, v205\n"
    "v_sub_f32 v37, s50, v205\n"
    "v_sub_f32 v38, s50, v205\n"
    "v_sub_f32 v39, s50, v205\n"
    "v_sub_f32 v40, s50, v205\n"
    "v_sub_f32 v41, s50, v205\n"
    "v_sub_f32 v42, s50, v205\n"
    "v_sub_f32 v43, s50, v205\n"
    "v_sub_f32 v44, s50, v205\n"
    "v_sub_f32 v45, s50, v205\n"
    "v_sub_f32 v46, s50, v205\n"
    "v_sub_f32 v47, s50, v205\n"
    "s_nop 1\n"
    "v_mfma_f32_32x32x16_bf16 v[16:31], v[64:67], v[180:183], v[32:47]\n"
    "s_nop 11\n"
    "v_exp_f32 v16, v16\n"
    "v_exp_f32 v17, v17\n"
    "v_exp_f32 v18, v18\n"
    "v_exp_f32 v19, v19\n"
    "v_cvt_pk_bf16_f32 v216, v16, v17\n"
    "v_exp_f32 v20, v20\n"
    "v_exp_f32 v21, v21\n"
    "v_cvt_pk_bf16_f32 v217, v18, v19\n"
    "v_exp_f32 v22, v22\n"
    "v_exp_f32 v23, v23\n"
    "v_cvt_pk_bf16_f32 v218, v20, v21\n"
    "v_exp_f32 v24, v24\n"
    "v_exp_f32 v25, v25\n"
    "v_cvt_pk_bf16_f32 v219, v22, v23\n"
    "v_exp_f32 v26, v26\n"
    "v_exp_f32 v27, v27\n"
    "v_cvt_pk_bf16_f32 v220, v24, v25\n"
    "v_exp_f32 v28, v28\n"
    "v_exp_f32 v29, v29\n"
    "v_cvt_pk_bf16_f32 v221, v26, v27\n"
    "v_exp_f32 v30, v30\n"
    "v_exp_f32 v31, v31\n"
    "v_cvt_pk_bf16_f32 v222, v28, v29\n"
    "v_cvt_pk_bf16_f32 v223, v30, v31\n"
    "v_mfma_f32_32x32x16_bf16 v[128:143], v[96:99], v[216:219], 0\n"
    "v_mfma_f32_16x16x32_bf16 v[208:211], v[212:215], v[216:219], 0\n"
    "v_mfma_f32_32x32x16_bf16 v[144:159], v[96:99], v[220:223], 0\n"
    "v_mfma_f32_16x16x32_bf16 v[208:211], v[212:215], v[220:223], v[208:211]\n"
    "s_cmp_gt_u32 s54, 0\n"
    "s_cselect_b32 s50, 0, 0xf149f2ca\n"
    "v_sub_f32 v32, s50, v205\n"
    "v_sub_f32 v33, s50, v205\n"
    "v_sub_f32 v34, s50, v205\n"
    "v_sub_f32 v35, s50, v205\n"
    "v_sub_f32 v36, s50, v205\n"
    "v_sub_f32 v37, s50, v205\n"
    "v_sub_f32 v38, s50, v205\n"
    "v_sub_f32 v39, s50, v205\n"
    "v_sub_f32 v40, s50, v205\n"
    "v_sub_f32 v41, s50, v205\n"
    "v_sub_f32 v42, s50, v205\n"
    "v_sub_f32 v43, s50, v205\n"
    "v_sub_f32 v44, s50, v205\n"
    "v_sub_f32 v45, s50, v205\n"
    "v_sub_f32 v46, s50, v205\n"
    "v_sub_f32 v47, s50, v205\n"
    "s_nop 1\n"
    "v_mfma_f32_32x32x16_bf16 v[16:31], v[68:71], v[180:183], v[32:47]\n"
    "s_nop 11\n"
    "v_exp_f32 v16, v16\n"
    "v_exp_f32 v17, v17\n"
    "v_exp_f32 v18, v18\n"
    "v_exp_f32 v19, v19\n"
    "v_cvt_pk_bf16_f32 v216, v16, v17\n"
    "v_exp_f32 v20, v20\n"
    "v_exp_f32 v21, v21\n"
    "v_cvt_pk_bf16_f32 v217, v18, v19\n"
    "v_exp_f32 v22, v22\n"
    "v_exp_f32 v23, v23\n"
    "v_cvt_pk_bf16_f32 v218, v20, v21\n"
    "v_exp_f32 v24, v24\n"
    "v_exp_f32 v25, v25\n"
    "v_cvt_pk_bf16_f32 v219, v22, v23\n"
    "v_exp_f32 v26, v26\n"
    "v_exp_f32 v27, v27\n"
    "v_cvt_pk_bf16_f32 v220, v24, v25\n"
    "v_exp_f32 v28, v28\n"
    "v_exp_f32 v29, v29\n"
    "v_cvt_pk_bf16_f32 v221, v26, v27\n"
    "v_exp_f32 v30, v30\n"
    "v_exp_f32 v31, v31\n"
    "v_cvt_pk_bf16_f32 v222, v28, v29\n"
    "v_cvt_pk_bf16_f32 v223, v30, v31\n"
    "v_mfma_f32_32x32x16_bf16 v[128:143], v[100:103], v[216:219], v[128:143]\n"
    "v_mfma_f32_16x16x32_bf16 v[208:211], v[212:215], v[216:219], v[208:211]\n"
    "v_mfma_f32_32x32x16_bf16 v[144:159], v[100:103], v[220:223], v[144:159]\n"
    "v_mfma_f32_16x16x32_bf16 v[208:211], v[212:215], v[220:223], v[208:211]\n"
    "s_cmp_gt_u32 s54, 1\n"
    "s_cselect_b32 s50, 0, 0xf149f2ca\n"
    "v_sub_f32 v32, s50, v205\n"
    "v_sub_f32 v33, s50, v205\n"
    "v_sub_f32 v34, s50, v205\n"
    "v_sub_f32 v35, s50, v205\n"
    "v_sub_f32 v36, s50, v205\n"
    "v_sub_f32 v37, s50, v205\n"
    "v_sub_f32 v38, s50, v205\n"
    "v_sub_f32 v39, s50, v205\n"
    "v_sub_f32 v40, s50, v205\n"
    "v_sub_f32 v41, s50, v205\n"
    "v_sub_f32 v42, s50, v205\n"
    "v_sub_f32 v43, s50, v205\n"
    "v_sub_f32 v44, s50, v205\n"
    "v_sub_f32 v45, s50, v205\n"
    "v_sub_f32 v46, s50, v205\n"
    "v_sub_f32 v47, s50, v205\n"
    "s_nop 1\n"
    "v_mfma_f32_32x32x16_bf16 v[16:31], v[72:75], v[180:183], v[32:47]\n"
    "s_nop 11\n"
    "v_exp_f32 v16, v16\n"
    "v_exp_f32 v17, v17\n"
    "v_exp_f32 v18, v18\n"
    "v_exp_f32 v19, v19\n"
    "v_cvt_pk_bf16_f32 v216, v16, v17\n"
    "v_exp_f32 v20, v20\n"
    "v_exp_f32 v21, v21\n"
    "v_cvt_pk_bf16_f32 v217, v18, v19\n"
    "v_exp_f32 v22, v22\n"
    "v_exp_f32 v23, v23\n"
    "v_cvt_pk_bf16_f32 v218, v20, v21\n"
    "v_exp_f32 v24, v24\n"
    "v_exp_f32 v25, v25\n"
    "v_cvt_pk_bf16_f32 v219, v22, v23\n"
    "v_exp_f32 v26, v26\n"
    "v_exp_f32 v27, v27\n"
    "v_cvt_pk_bf16_f32 v220, v24, v25\n"
    "v_exp_f32 v28, v28\n"
    "v_exp_f32 v29, v29\n"
    "v_cvt_pk_bf16_f32 v221, v26, v27\n"
    "v_exp_f32 v30, v30\n"
    "v_exp_f32 v31, v31\n"
    "v_cvt_pk_bf16_f32 v222, v28, v29\n"
    "v_cvt_pk_bf16_f32 v223, v30, v31\n"
    "v_mfma_f32_32x32x16_bf16 v[128:143], v[104:107], v[216:219], v[128:143]\n"
    "v_mfma_f32_16x16x32_bf16 v[208:211], v[212:215], v[216:219], v[208:211]\n"
    "v_mfma_f32_32x32x16_bf16 v[144:159], v[104:107], v[220:223], v[144:159]\n"
    "v_mfma_f32_16x16x32_bf16 v[208:211], v[212:215], v[220:223], v[208:211]\n"
    "s_cmp_gt_u32 s54, 1\n"
    "s_cselect_b32 s50, 0, 0xf149f2ca\n"
    "v_sub_f32 v32, s50, v205\n"
    "v_sub_f32 v33, s50, v205\n"
    "v_sub_f32 v34, s50, v205\n"
    "v_sub_f32 v35, s50, v205\n"
    "v_sub_f32 v36, s50, v205\n"
    "v_sub_f32 v37, s50, v205\n"
    "v_sub_f32 v38, s50, v205\n"
    "v_sub_f32 v39, s50, v205\n"
    "v_sub_f32 v40, s50, v205\n"
    "v_sub_f32 v41, s50, v205\n"
    "v_sub_f32 v42, s50, v205\n"
    "v_sub_f32 v43, s50, v205\n"
    "v_sub_f32 v44, s50, v205\n"
    "v_sub_f32 v45, s50, v205\n"
    "v_sub_f32 v46, s50, v205\n"
    "v_sub_f32 v47, s50, v205\n"
    "s_nop 1\n"
    "v_mfma_f32_32x32x16_bf16 v[16:31], v[76:79], v[180:183], v[32:47]\n"
    "s_nop 11\n"
    "v_exp_f32 v16, v16\n"
    "v_exp_f32 v17, v17\n"
    "v_exp_f32 v18, v18\n"
    "v_exp_f32 v19, v19\n"
    "v_cvt_pk_bf16_f32 v216, v16, v17\n"
    "v_exp_f32 v20, v20\n"
    "v_exp_f32 v21, v21\n"
    "v_cvt_pk_bf16_f32 v217, v18, v19\n"
    "v_exp_f32 v22, v22\n"
    "v_exp_f32 v23, v23\n"
    "v_cvt_pk_bf16_f32 v218, v20, v21\n"
    "v_exp_f32 v24, v24\n"
    "v_exp_f32 v25, v25\n"
    "v_cvt_pk_bf16_f32 v219, v22, v23\n"
    "v_exp_f32 v26, v26\n"
    "v_exp_f32 v27, v27\n"
    "v_cvt_pk_bf16_f32 v220, v24, v25\n"
    "v_exp_f32 v28, v28\n"
    "v_exp_f32 v29, v29\n"
    "v_cvt_pk_bf16_f32 v221, v26, v27\n"
    "v_exp_f32 v30, v30\n"
    "v_exp_f32 v31, v31\n"
    "v_cvt_pk_bf16_f32 v222, v28, v29\n"
    "v_cvt_pk_bf16_f32 v223, v30, v31\n"
    "v_mfma_f32_32x32x16_bf16 v[128:143], v[108:111], v[216:219], v[128:143]\n"
    "v_mfma_f32_16x16x32_bf16 v[208:211], v[212:215], v[216:219], v[208:211]\n"
    "v_mfma_f32_32x32x16_bf16 v[144:159], v[108:111], v[220:223], v[144:159]\n"
    "v_mfma_f32_16x16x32_bf16 v[208:211], v[212:215], v[220:223], v[208:211]\n"
    "s_cmp_gt_u32 s54, 2\n"
    "s_cselect_b32 s50, 0, 0xf149f2ca\n"
    "v_sub_f32 v32, s50, v205\n"
    "v_sub_f32 v33, s50, v205\n"
    "v_sub_f32 v34, s50, v205\n"
    "v_sub_f32 v35, s50, v205\n"
    "v_sub_f32 v36, s50, v205\n"
    "v_sub_f32 v37, s50, v205\n"
    "v_sub_f32 v38, s50, v205\n"
    "v_sub_f32 v39, s50, v205\n"
    "v_sub_f32 v40, s50, v205\n"
    "v_sub_f32 v41, s50, v205\n"
    "v_sub_f32 v42, s50, v205\n"
    "v_sub_f32 v43, s50, v205\n"
    "v_sub_f32 v44, s50, v205\n"
    "v_sub_f32 v45, s50, v205\n"
    "v_sub_f32 v46, s50, v205\n"
    "v_sub_f32 v47, s50, v205\n"
    "s_nop 1\n"
    "v_mfma_f32_32x32x16_bf16 v[16:31], v[80:83], v[180:183], v[32:47]\n"
    "s_nop 11\n"
    "v_exp_f32 v16, v16\n"
    "v_exp_f32 v17, v17\n"
    "v_exp_f32 v18, v18\n"
    "v_exp_f32 v19, v19\n"
    "v_cvt_pk_bf16_f32 v216, v16, v17\n"
    "v_exp_f32 v20, v20\n"
    "v_exp_f32 v21, v21\n"
    "v_cvt_pk_bf16_f32 v217, v18, v19\n"
    "v_exp_f32 v22, v22\n"
    "v_exp_f32 v23, v23\n"
    "v_cvt_pk_bf16_f32 v218, v20, v21\n"
    "v_exp_f32 v24, v24\n"
    "v_exp_f32 v25, v25\n"
    "v_cvt_pk_bf16_f32 v219, v22, v23\n"
    "v_exp_f32 v26, v26\n"
    "v_exp_f32 v27, v27\n"
    "v_cvt_pk_bf16_f32 v220, v24, v25\n"
    "v_exp_f32 v28, v28\n"
    "v_exp_f32 v29, v29\n"
    "v_cvt_pk_bf16_f32 v221, v26, v27\n"
    "v_exp_f32 v30, v30\n"
    "v_exp_f32 v31, v31\n"
    "v_cvt_pk_bf16_f32 v222, v28, v29\n"
    "v_cvt_pk_bf16_f32 v223, v30, v31\n"
    "v_mfma_f32_32x32x16_bf16 v[128:143], v[112:115], v[216:219], v[128:143]\n"
    "v_mfma_f32_16x16x32_bf16 v[208:211], v[212:215], v[216:219], v[208:211]\n"
    "v_mfma_f32_32x32x16_bf16 v[144:159], v[112:115], v[220:223], v[144:159]\n"
    "v_mfma_f32_16x16x32_bf16 v[208:211], v[212:215], v[220:223], v[208:211]\n"
    "s_cmp_gt_u32 s54, 2\n"
    "s_cselect_b32 s50, 0, 0xf149f2ca\n"
    "v_sub_f32 v32, s50, v205\n"
    "v_sub_f32 v33, s50, v205\n"
    "v_sub_f32 v34, s50, v205\n"
    "v_sub_f32 v35, s50, v205\n"
    "v_sub_f32 v36, s50, v205\n"
    "v_sub_f32 v37, s50, v205\n"
    "v_sub_f32 v38, s50, v205\n"
    "v_sub_f32 v39, s50, v205\n"
    "v_sub_f32 v40, s50, v205\n"
    "v_sub_f32 v41, s50, v205\n"
    "v_sub_f32 v42, s50, v205\n"
    "v_sub_f32 v43, s50, v205\n"
    "v_sub_f32 v44, s50, v205\n"
    "v_sub_f32 v45, s50, v205\n"
    "v_sub_f32 v46, s50, v205\n"
    "v_sub_f32 v47, s50, v205\n"
    "s_nop 1\n"
    "v_mfma_f32_32x32x16_bf16 v[16:31], v[84:87], v[180:183], v[32:47]\n"
    "s_nop 11\n"
    "v_exp_f32 v16, v16\n"
    "v_exp_f32 v17, v17\n"
    "v_exp_f32 v18, v18\n"
    "v_exp_f32 v19, v19\n"
    "v_cvt_pk_bf16_f32 v216, v16, v17\n"
    "v_exp_f32 v20, v20\n"
    "v_exp_f32 v21, v21\n"
    "v_cvt_pk_bf16_f32 v217, v18, v19\n"
    "v_exp_f32 v22, v22\n"
    "v_exp_f32 v23, v23\n"
    "v_cvt_pk_bf16_f32 v218, v20, v21\n"
    "v_exp_f32 v24, v24\n"
    "v_exp_f32 v25, v25\n"
    "v_cvt_pk_bf16_f32 v219, v22, v23\n"
    "v_exp_f32 v26, v26\n"
    "v_exp_f32 v27, v27\n"
    "v_cvt_pk_bf16_f32 v220, v24, v25\n"
    "v_exp_f32 v28, v28\n"
    "v_exp_f32 v29, v29\n"
    "v_cvt_pk_bf16_f32 v221, v26, v27\n"
    "v_exp_f32 v30, v30\n"
    "v_exp_f32 v31, v31\n"
    "v_cvt_pk_bf16_f32 v222, v28, v29\n"
    "v_cvt_pk_bf16_f32 v223, v30, v31\n"
    "v_mfma_f32_32x32x16_bf16 v[128:143], v[116:119], v[216:219], v[128:143]\n"
    "v_mfma_f32_16x16x32_bf16 v[208:211], v[212:215], v[216:219], v[208:211]\n"
    "v_mfma_f32_32x32x16_bf16 v[144:159], v[116:119], v[220:223], v[144:159]\n"
    "v_mfma_f32_16x16x32_bf16 v[208:211], v[212:215], v[220:223], v[208:211]\n"
    "s_cmp_gt_u32 s54, 3\n"
    "s_cselect_b32 s50, 0, 0xf149f2ca\n"
    "v_sub_f32 v32, s50, v205\n"
    "v_sub_f32 v33, s50, v205\n"
    "v_sub_f32 v34, s50, v205\n"
    "v_sub_f32 v35, s50, v205\n"
    "v_sub_f32 v36, s50, v205\n"
    "v_sub_f32 v37, s50, v205\n"
    "v_sub_f32 v38, s50, v205\n"
    "v_sub_f32 v39, s50, v205\n"
    "v_sub_f32 v40, s50, v205\n"
    "v_sub_f32 v41, s50, v205\n"
    "v_sub_f32 v42, s50, v205\n"
    "v_sub_f32 v43, s50, v205\n"
    "v_sub_f32 v44, s50, v205\n"
    "v_sub_f32 v45, s50, v205\n"
    "v_sub_f32 v46, s50, v205\n"
    "v_sub_f32 v47, s50, v205\n"
    "v_min_f32 v32, -v224, v32\n"
    "v_min_f32 v33, -v225, v33\n"
    "v_min_f32 v34, -v226, v34\n"
    "v_min_f32 v35, -v227, v35\n"
    "v_min_f32 v36, -v228, v36\n"
    "v_min_f32 v37, -v229, v37\n"
    "v_min_f32 v38, -v230, v38\n"
    "v_min_f32 v39, -v231, v39\n"
    "v_min_f32 v40, -v232, v40\n"
    "v_min_f32 v41, -v233, v41\n"
    "v_min_f32 v42, -v234, v42\n"
    "v_min_f32 v43, -v235, v43\n"
    "v_min_f32 v44, -v236, v44\n"
    "v_min_f32 v45, -v237, v45\n"
    "v_min_f32 v46, -v238, v46\n"
    "v_min_f32 v47, -v239, v47\n"
    "s_nop 1\n"
    "v_mfma_f32_32x32x16_bf16 v[16:31], v[88:91], v[180:183], v[32:47]\n"
    "s_nop 11\n"
    "v_exp_f32 v16, v16\n"
    "v_exp_f32 v17, v17\n"
    "v_exp_f32 v18, v18\n"
    "v_exp_f32 v19, v19\n"
    "v_cvt_pk_bf16_f32 v216, v16, v17\n"
    "v_exp_f32 v20, v20\n"
    "v_exp_f32 v21, v21\n"
    "v_cvt_pk_bf16_f32 v217, v18, v19\n"
    "v_exp_f32 v22, v22\n"
    "v_exp_f32 v23, v23\n"
    "v_cvt_pk_bf16_f32 v218, v20, v21\n"
    "v_exp_f32 v24, v24\n"
    "v_exp_f32 v25, v25\n"
    "v_cvt_pk_bf16_f32 v219, v22, v23\n"
    "v_exp_f32 v26, v26\n"
    "v_exp_f32 v27, v27\n"
    "v_cvt_pk_bf16_f32 v220, v24, v25\n"
    "v_exp_f32 v28, v28\n"
    "v_exp_f32 v29, v29\n"
    "v_cvt_pk_bf16_f32 v221, v26, v27\n"
    "v_exp_f32 v30, v30\n"
    "v_exp_f32 v31, v31\n"
    "v_cvt_pk_bf16_f32 v222, v28, v29\n"
    "v_cvt_pk_bf16_f32 v223, v30, v31\n"
    "v_mfma_f32_32x32x16_bf16 v[128:143], v[120:123], v[216:219], v[128:143]\n"
    "v_mfma_f32_16x16x32_bf16 v[208:211], v[212:215], v[216:219], v[208:211]\n"
    "v_mfma_f32_32x32x16_bf16 v[144:159], v[120:123], v[220:223], v[144:159]\n"
    "v_mfma_f32_16x16x32_bf16 v[208:211], v[212:215], v[220:223], v[208:211]\n"
    "s_nop 15\n"
    "v_add_f32 v204, v201, v208\n"
    "s_nop 1\n"
    "s_branch Lslowb_22\n"
    :
    : "{s[0:1]}"(__builtin_amdgcn_kernarg_segment_ptr()), "{s2}"(__builtin_amdgcn_workgroup_id_x()),
      "{v0}"(__builtin_amdgcn_workitem_id_x()), "i"((__attribute__((address_space(3))) char*)lds)
    :
      "v1", "v2", "v3", "v4", "v5", "v6", "v7", "v8", "v9", "v10", "v11", "v12", "v13", "v14", "v15", "v16",
      "v17", "v18", "v19", "v20", "v21", "v22", "v23", "v24", "v25", "v26", "v27", "v28", "v29", "v30", "v31", "v32",
      "v33", "v34", "v35", "v36", "v37", "v38", "v39", "v40", "v41", "v42", "v43", "v44", "v45", "v46", "v47", "v48",
      "v49", "v50", "v51", "v52", "v53", "v54", "v55", "v56", "v57", "v58", "v59", "v60", "v61", "v62", "v63", "v64",
      "v65", "v66", "v67", "v68", "v69", "v70", "v71", "v72", "v73", "v74", "v75", "v76", "v77", "v78", "v79", "v80",
      "v81", "v82", "v83", "v84", "v85", "v86", "v87", "v88", "v89", "v90", "v91", "v92", "v93", "v94", "v95", "v96",
      "v97", "v98", "v99", "v100", "v101", "v102", "v103", "v104", "v105", "v106", "v107", "v108", "v109", "v110", "v111", "v112",
      "v113", "v114", "v115", "v116", "v117", "v118", "v119", "v120", "v121", "v122", "v123", "v124", "v125", "v126", "v127", "v128",
      "v129", "v130", "v131", "v132", "v133", "v134", "v135", "v136", "v137", "v138", "v139", "v140", "v141", "v142", "v143", "v144",
      "v145", "v146", "v147", "v148", "v149", "v150", "v151", "v152", "v153", "v154", "v155", "v156", "v157", "v158", "v159", "v160",
      "v161", "v162", "v163", "v164", "v165", "v166", "v167", "v168", "v169", "v170", "v171", "v172", "v173", "v174", "v175", "v176",
      "v177", "v178", "v179", "v180", "v181", "v182", "v183", "v184", "v185", "v186", "v187", "v188", "v189", "v190", "v191", "v192",
      "v193", "v194", "v195", "v196", "v197", "v198", "v199", "v200", "v201", "v202", "v203", "v204", "v205", "v206", "v207", "v208",
      "v209", "v210", "v211", "v212", "v213", "v214", "v215", "v216", "v217", "v218", "v219", "v220", "v221", "v222", "v223", "v224",
      "v225", "v226", "v227", "v228", "v229", "v230", "v231", "v232", "v233", "v234", "v235", "v236", "v237", "v238", "v239", "v240",
      "v241", "v242", "v243", "v244", "v245", "v246", "v247", "v248", "v249", "v250", "v251", "v252", "v253", "v254", "v255", "a0",
      "a1", "a2", "a3", "a4", "a5", "a6", "a7", "a8", "a9", "a10", "a11", "a12", "a13", "a14", "a15", "a16",
      "a17", "a18", "a19", "a20", "a21", "a22", "a23", "a24", "a25", "a26", "a27", "a28", "a29", "a30", "a31", "a32",
      "a33", "a34", "a35", "a36", "a37", "a38", "a39", "a40", "a41", "a42", "a43", "a44", "a45", "a46", "a47", "a48",
      "a49", "a50", "a51", "a52", "a53", "a54", "a55", "a56", "a57", "a58", "a59", "a60", "a61", "a62", "a63", "a64",
      "a65", "a66", "a67", "a68", "a69", "a70", "a71", "a72", "a73", "a74", "a75", "a76", "a77", "a78", "a79", "a80",
      "a81", "a82", "a83", "a84", "a85", "a86", "a87", "a88", "a89", "a90", "a91", "a92", "a93", "a94", "a95", "a96",
      "a97", "a98", "a99", "a100", "a101", "a102", "a103", "a104", "a105", "a106", "a107", "a108", "a109", "a110", "a111", "a112",
      "a113", "a114", "a115", "a116", "a117", "a118", "a119", "a120", "a121", "a122", "a123", "a124", "a125", "a126", "a127", "a128",
      "a129", "a130", "a131", "a132", "a133", "a134", "a135", "a136", "a137", "a138", "a139", "a140", "a141", "a142", "a143", "a144",
      "a145", "a146", "a147", "a148", "a149", "a150", "a151", "a152", "a153", "a154", "a155", "a156", "a157", "a158", "a159", "a160",
      "a161", "a162", "a163", "a164", "a165", "a166", "a167", "a168", "a169", "a170", "a171", "a172", "a173", "a174", "a175", "a176",
      "a177", "a178", "a179", "a180", "a181", "a182", "a183", "a184", "a185", "a186", "a187", "a188", "a189", "a190", "a191", "a192",
      "a193", "a194", "a195", "a196", "a197", "a198", "a199", "a200", "a201", "a202", "a203", "a204", "a205", "a206", "a207", "a208",
      "a209", "a210", "a211", "a212", "a213", "a214", "a215", "a216", "a217", "a218", "a219", "a220", "a221", "a222", "a223", "a224",
      "a225", "a226", "a227", "a228", "a229", "a230", "a231", "a232", "a233", "a234", "a235", "a236", "a237", "a238", "a239", "a240",
      "a241", "a242", "a243", "a244", "a245", "a246", "a247", "a248", "a249", "a250", "a251", "a252", "a253", "a254", "a255", "s3",
      "s4", "s5", "s6", "s7", "s8", "s9", "s10", "s11", "s12", "s13", "s14", "s15", "s16", "s17", "s18", "s19",
      "s20", "s21", "s22", "s23", "s24", "s25", "s26", "s27", "s28", "s29", "s30", "s31", "s33", "s34", "s35", "s36",
      "s37", "s38", "s39", "s40", "s41", "s42", "s43", "s44", "s45", "s46", "s47", "s48", "s49", "s50", "s51", "s52",
      "s53", "s54", "s55", "s56", "s57", "s58", "s59", "s60", "s61", "s62", "s63", "s64", "s65", "s66", "s67", "s68",
      "s69", "s70", "s71", "s72", "s73", "s74", "s75", "s76", "s77", "s78", "s79", "s80", "s81", "s82", "s83", "s84",
      "s85", "s86", "s87", "s88", "s89", "s90", "s91", "s92", "s93", "s94", "s95", "s96", "s97", "s98", "s99", "vcc",
      "scc", "memory"
  );
}

static inline Srd mk(const void* p, unsigned bytes) { Srd s; s.p = p; s.bytes = bytes; s.fmt = 0x00020000u; return s; }

extern "C" void kernel_launch(void* const* d_in, const int* in_sizes, int n_in, void* d_out, int out_size,
                              void* d_ws, size_t ws_size, hipStream_t stream) {
  KArgs a;
  a.x = mk(d_in[0], 2u * in_sizes[0]); a.wqkv = mk(d_in[1], 2u * in_sizes[1]); a.wo = mk(d_in[2], 2u * in_sizes[2]);
  a.w1 = mk(d_in[4], 2u * in_sizes[4]); a.b1 = mk(d_in[5], 2u * in_sizes[5]); a.w2 = mk(d_in[6], 2u * in_sizes[6]);
  a.b2 = mk(d_in[7], 2u * in_sizes[7]); a.c = mk(d_in[8], 2u * in_sizes[8]); a.wmod = mk(d_in[9], 2u * in_sizes[9]);
  a.out = mk(d_out, 2u * out_size); a.ws = mk(d_ws, 33558528u); a.spare = mk(d_ws, 0u);
  fused_block_kernel<<<256, 256, 0, stream>>>(a);
}
```
